```python
import torch

def _softmax(x, axis=-1):
    return torch.softmax(x, dim=axis)
def _gelu(x):
    return torch.nn.functional.gelu(x, approximate="tanh")
def _silu(x):
    return torch.nn.functional.silu(x)
def _sigmoid(x):
    return torch.sigmoid(x)
def _layernorm(x, axis=-1, eps=1e-5):
    m = x.mean(dim=axis, keepdim=True)
    v = x.var(dim=axis, keepdim=True, unbiased=False)
    return (x - m) / torch.sqrt(v + eps)
def _scatter_add(tgt, idx, src, axis=0):
    return tgt.clone().index_add_(axis, idx, src)
def _segment_sum(src, seg, n):
    out = torch.zeros((n,) + src.shape[1:], dtype=src.dtype)
    return out.index_add_(0, seg, src)
def _pad(x, pw):
    flat = tuple(v for pair in reversed(pw) for v in pair)
    return torch.nn.functional.pad(x, flat)

kernel_name = 'interleave_layers__ssm_conv_h3__glu_gated_geglu__prompt'

def setup_inputs():

    _w = (lambda a: (lambda t: t.to(torch.bfloat16) if t.is_floating_point() else t)(torch.as_tensor(a)))
    def _A_setup():
        B, S, D = 1, 2048, 1024
        ks = 4
        return {
            "q": _w((lambda *s: torch.randn(*s, dtype=torch.bfloat16) * 0.05)(B, S, D)),
            "k": _w((lambda *s: torch.randn(*s, dtype=torch.bfloat16) * 0.2)(B, S, D)),
            "v": _w((lambda *s: torch.randn(*s, dtype=torch.bfloat16) * 0.2)(B, S, D)),
            "shift_k": _w((lambda *s: torch.randn(*s, dtype=torch.bfloat16) * 0.2)(D, ks) * 0.5),
            "ssm_k": _w((lambda *s: torch.randn(*s, dtype=torch.bfloat16) * 0.4)(D, S) * (1.0 / S ** 0.5)),
            "ssm_D": _w((lambda *s: torch.randn(*s, dtype=torch.bfloat16) * 0.2)(D)),
        }
    _A_in = _A_setup()

    def _B_setup():
        B, S, Dm, Df = 1, 2048, 1024, 3584
        return {
            "x": _w((lambda *s: torch.randn(*s, dtype=torch.bfloat16) * 0.05)(B, S, Dm)),
            "w_gate": _w((lambda *s: torch.randn(*s, dtype=torch.bfloat16) * 0.4)(Dm, Df) * (Dm ** -0.5)),
            "w_up": _w((lambda *s: torch.randn(*s, dtype=torch.bfloat16) * 0.4)(Dm, Df) * (Dm ** -0.5)),
            "w_down": _w((lambda *s: torch.randn(*s, dtype=torch.bfloat16) * 0.4)(Df, Dm) * (Df ** -0.5)),
        }
    _B_in = _B_setup()

    return {
        "A_q": _A_in["q"],
        "A_k": _A_in["k"],
        "A_v": _A_in["v"],
        "A_shift_k": _A_in["shift_k"],
        "A_ssm_k": _A_in["ssm_k"],
        "A_ssm_D": _A_in["ssm_D"],
        "B_x": _B_in["x"],
        "B_w_gate": _B_in["w_gate"],
        "B_w_up": _B_in["w_up"],
        "B_w_down": _B_in["w_down"],
    }

def reference(A_q, A_k, A_v, A_shift_k, A_ssm_k, A_ssm_D, B_x, B_w_gate, B_w_up, B_w_down):
    def _A_ref(q, k, v, shift_k, ssm_k, ssm_D):
        B, S, D = q.shape
        ks = shift_k.shape[-1]
        k_pad = _pad(k, ((0, 0), (ks - 1, 0), (0, 0)))
        k_shift = torch.zeros_like(k)
        for t in range(ks):
            k_shift = k_shift + k_pad[:, t:t + S, :] * shift_k[:, ks - 1 - t][None, None, :]
        kv = k_shift * v
        L = 2 * S
        kv_pad = _pad(kv, ((0, 0), (0, S), (0, 0)))
        kf_pad = _pad(ssm_k, ((0, 0), (0, S)))
        kv_f = (lambda x, axes=(-2,-1): torch.fft.rfft2(x.float(), dim=axes))(kv_pad[:, None, :, :], axes=(1, 2))
        kf_f = (lambda x, axes=(-2,-1): torch.fft.rfft2(x.float(), dim=axes))(kf_pad[:, None, :], axes=(1, 2))
        kf_f = (lambda x, *d: x.permute(*d))(kf_f, 1, 2, 0)
        y_f = kv_f * kf_f[None, :, :, :]
        y = (lambda x, s, axes=(-2,-1): torch.fft.irfft2(x, s=s, dim=axes).to(torch.bfloat16))(y_f, (1, L), axes=(1, 2))[:, 0, :S, :]
        y = y + kv * ssm_D[None, None, :]
        return q * y

    def _B_ref(x, w_gate, w_up, w_down):
        g = torch.einsum("bsd,df->bsf", x, w_gate)
        u = torch.einsum("bsd,df->bsf", x, w_up)
        h = _gelu(g) * u
        return torch.einsum("bsf,fd->bsd", h, w_down)

    x = A_q
    x = x + _A_ref(_layernorm(x, axis=-1), A_k, A_v, A_shift_k, A_ssm_k, A_ssm_D)
    x = x + _B_ref(_layernorm(x, axis=-1), B_w_gate, B_w_up, B_w_down)
    x = x + _A_ref(_layernorm(x, axis=-1), A_k, A_v, A_shift_k, A_ssm_k, A_ssm_D)
    x = x + _B_ref(_layernorm(x, axis=-1), B_w_gate, B_w_up, B_w_down)
    return x
```

```cpp
#include <hip/hip_runtime.h>
#include <hip/hip_bf16.h>
typedef unsigned short u16;
typedef short bf16x8 __attribute__((ext_vector_type(8)));
typedef float f32x4 __attribute__((ext_vector_type(4)));
typedef unsigned u32x4 __attribute__((ext_vector_type(4)));
constexpr int S=2048, D=1024, F=3584;
__device__ __forceinline__ float b2f(u16 x){ return __uint_as_float(((unsigned)x)<<16); }
__device__ __forceinline__ u16 f2b(float f){ unsigned u=__float_as_uint(f); u += 0x7fff + ((u>>16)&1); return (u16)(u>>16); }
__device__ __forceinline__ float rb(float f){ return b2f(f2b(f)); }

#define PROBE 0
__global__ __launch_bounds__(256) void k_probe(const u32x4* p0, unsigned n0, const u32x4* p1, unsigned n1, const u32x4* p2, unsigned n2, unsigned* sink){
  unsigned long long t0=__builtin_amdgcn_s_memrealtime();
  u32x4 acc={0,0,0,0};
  unsigned gid=blockIdx.x*256+threadIdx.x, gs=gridDim.x*256;
  #pragma unroll 8
  for(unsigned i=gid;i<n0;i+=gs) acc^=p0[i];
  #pragma unroll 8
  for(unsigned i=gid;i<n1;i+=gs) acc^=p1[i];
  #pragma unroll 8
  for(unsigned i=gid;i<n2;i+=gs) acc^=p2[i];
  unsigned x=acc[0]^acc[1]^acc[2]^acc[3];
  if(x==0x12345678u) sink[gid]=x;
  asm volatile("s_waitcnt vmcnt(0)":::"memory");
  unsigned long long t1=__builtin_amdgcn_s_memrealtime();
  unsigned long long dt=t1-t0; if(dt>3000) dt=3000;
  while(__builtin_amdgcn_s_memrealtime()-t1 < dt*50) __builtin_amdgcn_s_sleep(8);
}

__global__ void k_kv(const u16* k,const u16* v,const u16* sk,u16* kvT){
  int d=blockIdx.x*64+(threadIdx.x&63); int t0=blockIdx.y*64;
  __shared__ u16 tile[64][66];
  for(int tt=threadIdx.x>>6; tt<64; tt+=4){ int t=t0+tt; float a=0;
    for(int j=0;j<4;j++){ int ts=t-3+j; float kk= ts>=0? b2f(k[ts*D+d]):0.f; a=rb(a+rb(kk*b2f(sk[d*4+3-j]))); }
    tile[threadIdx.x&63][tt]=f2b(a*b2f(v[t*D+d])); }
  __syncthreads();
  for(int c=threadIdx.x>>6;c<64;c+=4) kvT[(blockIdx.x*64+c)*S+t0+(threadIdx.x&63)]=tile[c][threadIdx.x&63];
}
__global__ void k_conv(const u16* kvT,const u16* h,const u16* Dv,u16* y){
  __shared__ float su[S], sh[S]; int d=blockIdx.x;
  for(int i=threadIdx.x;i<S;i+=256){su[i]=b2f(kvT[d*S+i]); sh[i]=b2f(h[d*S+i]);}
  __syncthreads();
  float dd=b2f(Dv[d]);
  for(int t=threadIdx.x;t<S;t+=256){ float a=0; for(int s=0;s<=t;s++) a+=sh[t-s]*su[s];
    y[t*D+d]=f2b(rb(a)+rb(su[t]*dd)); }
}
__device__ float bsum(float v, float* sm){ for(int o=32;o;o>>=1) v+=__shfl_xor(v,o); __syncthreads(); if((threadIdx.x&63)==0) sm[threadIdx.x>>6]=v; __syncthreads(); return sm[0]+sm[1]+sm[2]+sm[3]; }
__global__ void k_ln(const u16* xin,const u16* y,u16* xout,u16* xn){
  __shared__ float sm[4]; int t=blockIdx.x; float x[4];
  for(int i=0;i<4;i++) x[i]=b2f(xin[t*D+threadIdx.x*4+i]);
  float m=bsum(x[0]+x[1]+x[2]+x[3],sm)/D; float q=0; for(int i=0;i<4;i++) q+=(x[i]-m)*(x[i]-m);
  float r=rsqrtf(bsum(q,sm)/D+1e-5f);
  for(int i=0;i<4;i++) x[i]=rb(x[i]+rb(rb((x[i]-m)*r)*b2f(y[t*D+threadIdx.x*4+i])));
  for(int i=0;i<4;i++) xout[t*D+threadIdx.x*4+i]=f2b(x[i]);
  m=bsum(x[0]+x[1]+x[2]+x[3],sm)/D; q=0; for(int i=0;i<4;i++) q+=(x[i]-m)*(x[i]-m);
  r=rsqrtf(bsum(q,sm)/D+1e-5f);
  for(int i=0;i<4;i++) xn[t*D+threadIdx.x*4+i]=f2b((x[i]-m)*r);
}
template<int MODE>
__global__ __launch_bounds__(256) void k_gemm(const u16* A,const u16* B0,const u16* B1,const u16* res,u16* out,int K,int N){
  __shared__ u16 sa[64][40], sb0[64][40], sb1[64][40];
  int tid=threadIdx.x, lane=tid&63, w=tid>>6, wr=w>>1, wc=w&1, m0=blockIdx.y*64, n0=blockIdx.x*64;
  f32x4 c0[2][2]={}, c1[2][2]={};
  for(int k0=0;k0<K;k0+=32){
    { int r=tid>>2, c=(tid&3)*8; *(u32x4*)&sa[r][c]=*(const u32x4*)&A[(size_t)(m0+r)*K+k0+c]; }
    { int kk=tid>>3, c=(tid&7)*8; u32x4 v=*(const u32x4*)&B0[(size_t)(k0+kk)*N+n0+c]; for(int j=0;j<8;j++) sb0[c+j][kk]=((u16*)&v)[j];
      if(MODE==0){ v=*(const u32x4*)&B1[(size_t)(k0+kk)*N+n0+c]; for(int j=0;j<8;j++) sb1[c+j][kk]=((u16*)&v)[j]; } }
    __syncthreads();
    for(int i=0;i<2;i++) for(int j=0;j<2;j++){
      bf16x8 a=*(bf16x8*)&sa[wr*32+i*16+(lane&15)][(lane>>4)*8];
      bf16x8 b=*(bf16x8*)&sb0[wc*32+j*16+(lane&15)][(lane>>4)*8];
      c0[i][j]=__builtin_amdgcn_mfma_f32_16x16x32_bf16(a,b,c0[i][j],0,0,0);
      if(MODE==0){ b=*(bf16x8*)&sb1[wc*32+j*16+(lane&15)][(lane>>4)*8]; c1[i][j]=__builtin_amdgcn_mfma_f32_16x16x32_bf16(a,b,c1[i][j],0,0,0);} }
    __syncthreads();
  }
  for(int i=0;i<2;i++) for(int j=0;j<2;j++) for(int r=0;r<4;r++){
    int m=m0+wr*32+i*16+(lane>>4)*4+r, n=n0+wc*32+j*16+(lane&15);
    if(MODE==0){ float g=c0[i][j][r], u=c1[i][j][r]; float z=0.7978845608f*(g+0.044715f*g*g*g); float ge=g/(1.f+__expf(-2.f*z)); out[(size_t)m*N+n]=f2b(ge*u); }
    else out[(size_t)m*N+n]=f2b(b2f(res[(size_t)m*N+n])+c0[i][j][r]);
  }
}
extern "C" void kernel_launch(void* const* d_in, const int* in_sizes, int n_in, void* d_out, int out_size, void* d_ws, size_t ws_size, hipStream_t st){
  const u16 *q=(u16*)d_in[0],*k=(u16*)d_in[1],*v=(u16*)d_in[2],*sk=(u16*)d_in[3],*hk=(u16*)d_in[4],*Dv=(u16*)d_in[5],*wg=(u16*)d_in[7],*wu=(u16*)d_in[8],*wd=(u16*)d_in[9];
  char* ws=(char*)d_ws; u16* kvT=(u16*)(ws); u16* y=(u16*)(ws+(4<<20)); u16* x1=(u16*)(ws+(8<<20)); u16* xn=(u16*)(ws+(12<<20)); u16* x2=(u16*)(ws+(16<<20)); u16* h=(u16*)(ws+(20<<20));
  unsigned nW=D*F*2/16, nX=S*D*2/16;
  if(PROBE==1) k_probe<<<1024,256,0,st>>>((u32x4*)wg,nW,(u32x4*)wu,nW,(u32x4*)wd,nW,(unsigned*)(ws+(64<<20)));
  if(PROBE==2) k_probe<<<1024,256,0,st>>>((u32x4*)q,nX,(u32x4*)k,nX,(u32x4*)v,nX,(unsigned*)(ws+(64<<20)));
  if(PROBE==3) k_probe<<<1024,256,0,st>>>((u32x4*)(ws),nW,(u32x4*)(ws+(8<<20)),nW,(u32x4*)(ws+(16<<20)),nW,(unsigned*)(ws+(64<<20)));
  if(PROBE==4) k_probe<<<1024,256,0,st>>>((u32x4*)(ws+(232<<20)),nW,(u32x4*)(ws+(240<<20)),nW,(u32x4*)(ws+(248<<20)),nW,(unsigned*)(ws+(64<<20)));
  if(PROBE==5) k_probe<<<1024,256,0,st>>>((u32x4*)wg,0,(u32x4*)wu,0,(u32x4*)wd,0,(unsigned*)(ws+(64<<20)));
  k_kv<<<dim3(16,32),256,0,st>>>(k,v,sk,kvT);
  k_conv<<<D,256,0,st>>>(kvT,hk,Dv,y);
  k_ln<<<S,256,0,st>>>(q,y,x1,xn);
  k_gemm<0><<<dim3(F/64,S/64),256,0,st>>>(xn,wg,wu,nullptr,h,D,F);
  k_gemm<1><<<dim3(D/64,S/64),256,0,st>>>(h,wd,nullptr,x1,x2,F,D);
  k_ln<<<S,256,0,st>>>(x2,y,x1,xn);
  k_gemm<0><<<dim3(F/64,S/64),256,0,st>>>(xn,wg,wu,nullptr,h,D,F);
  k_gemm<1><<<dim3(D/64,S/64),256,0,st>>>(h,wd,nullptr,x1,(u16*)d_out,F,D);
}
```

```cpp
#include <hip/hip_runtime.h>
typedef unsigned short u16;
typedef float f32x32 __attribute__((ext_vector_type(32)));
typedef float f32x4 __attribute__((ext_vector_type(4)));
typedef float f32x2 __attribute__((ext_vector_type(2)));
typedef __bf16 bf16x2 __attribute__((ext_vector_type(2)));
typedef short bf16x8 __attribute__((ext_vector_type(8)));
typedef unsigned u32x16 __attribute__((ext_vector_type(16)));
typedef unsigned u32x4 __attribute__((ext_vector_type(4)));
typedef unsigned u32x2 __attribute__((ext_vector_type(2)));
#define DI __device__ __forceinline__
#define LAS __attribute__((address_space(3)))
DI unsigned pk(float a, float b) { f32x2 v = {a, b}; return __builtin_bit_cast(unsigned, __builtin_convertvector(v, bf16x2)); }
DI float blo(unsigned u) { return __uint_as_float(u << 16); }
DI float bhi(unsigned u) { return __uint_as_float(u & 0xffff0000u); }
DI float b2f(u16 x) { return __uint_as_float((unsigned)x << 16); }
DI __amdgpu_buffer_rsrc_t mk(const void* p, unsigned bytes) { return __builtin_amdgcn_make_buffer_rsrc((void*)p, 0, bytes, 0x00020000); }

constexpr unsigned MB = 1u << 20;
constexpr unsigned F_U = 0, F_Y = 131072, F_LN = 2 * 131072  , F_UP = 4 * 131072  , F_DN = 6 * 131072  , WS_EPOCH = 8 * 131072;
constexpr unsigned WS_U = 2 * MB, WS_Y = 6 * MB, WS_XN = 10 * MB  , WS_P = 18 * MB  , WS_X3 = 26 * MB, WS_XB = 30 * MB  ,
                   WS_H = 38 * MB  , WS_W2 = 68 * MB, WS_BYTES = 76 * MB;
constexpr int CV_CP = 4160;
constexpr int CV_UO = 4 * CV_CP, CV_CH = CV_UO + 4608, CV_T = 4 * CV_CH, CV_Y = CV_T + 16384;
constexpr unsigned LDS_DUMMY = 131072, LDS_RED = LDS_DUMMY + 256, LDS_X1 = LDS_DUMMY + 1024, MAIN_LDS = LDS_X1 + 16384;
typedef __amdgpu_buffer_rsrc_t rsrc_t;
typedef unsigned long long u64;
#define LDSBAR() do { asm volatile("s_waitcnt lgkmcnt(0)" ::: "memory"); __builtin_amdgcn_s_barrier(); asm volatile("" ::: "memory"); } while (0)
#define WAITVM(n) asm volatile("s_waitcnt vmcnt(%0)" : : "n"(n) : "memory")
DI int lane_id() { return __builtin_amdgcn_mbcnt_hi(~0u, __builtin_amdgcn_mbcnt_lo(~0u, 0u)); }
DI u32x4 ld16(rsrc_t s, unsigned v, unsigned o) { return __builtin_amdgcn_raw_buffer_load_b128(s, v, o, 16); }
DI u32x2 ld8(rsrc_t s, unsigned v, unsigned o) { return __builtin_amdgcn_raw_buffer_load_b64(s, v, o, 16); }
DI void st16(u32x4 x, rsrc_t s, unsigned v, unsigned o) {
  asm volatile("buffer_store_dwordx4 %0, %1, %2, %3 offen sc1\n\ts_nop 1" : : "v"(x), "v"(v), "s"(s), "s"(o) : "memory");
}
DI void touch(rsrc_t srd, unsigned voff) {
  asm volatile("s_mov_b32 m0, %2\n\ts_nop 0\n\tbuffer_load_dword %0, %1, 0 offen lds" : : "v"(voff), "s"(srd), "n"(LDS_DUMMY) : "memory");
}
DI void put_flag(rsrc_t ws, unsigned off, u64 tag, int lane) {
  if (lane == 0) __builtin_amdgcn_raw_buffer_store_b64(u32x2{(unsigned)tag, (unsigned)(tag >> 32)}, ws, 0, off, 16);
}
DI void wait_flags(rsrc_t ws, unsigned voff, u64 tag) {
  for (int spin = 0; spin < (1 << 18); ++spin) {
    asm volatile("" ::: "memory");
    const u32x2 f = ld8(ws, voff, 0);
    if (__all((((u64)f[1] << 32) | f[0]) == tag)) break;
  }
  asm volatile("" ::: "memory");
}
DI f32x2 up2(unsigned u) { return f32x2{blo(u), bhi(u)}; }
DI unsigned pk2(f32x2 v) { return __builtin_bit_cast(unsigned, __builtin_convertvector(v, bf16x2)); }
DI float red_lanes(float v) {
  v += __builtin_bit_cast(float, __builtin_amdgcn_update_dpp(0, __builtin_bit_cast(int, v), 0x128, 0xf, 0xf, true));
  { auto s = __builtin_amdgcn_permlane16_swap(__float_as_uint(v), __float_as_uint(v), false, false); v = __uint_as_float(s[0]) + __uint_as_float(s[1]); }
  { auto s = __builtin_amdgcn_permlane32_swap(__float_as_uint(v), __float_as_uint(v), false, false); v = __uint_as_float(s[0]) + __uint_as_float(s[1]); }
  return v;
}
DI void row_partials(const f32x2 (&xs)[16], LAS f32x2* red, int lane) {
  f32x2 s[2] = {xs[0], xs[1]}, s2[2] = {xs[0] * xs[0], xs[1] * xs[1]};
#pragma unroll
  for (int e = 2; e < 16; e++) { s[e & 1] += xs[e]; s2[e & 1] += xs[e] * xs[e]; }
  const f32x2 a = s[0] + s[1], b = s2[0] + s2[1];
  const float t = red_lanes(a[0] + a[1]), t2 = red_lanes(b[0] + b[1]);
  if (lane < 8) red[lane] = f32x2{t, t2};
}
DI void row_stats(const LAS f32x2* red, int r8, float& dm, float& rstd) {
  const f32x2 t = (red[r8] + red[8 + r8]) + (red[16 + r8] + red[24 + r8]);
  dm = t[0] * (1.f / 1024.f); rstd = __builtin_amdgcn_rsqf(fmaxf(t[1] * (1.f / 1024.f) - dm * dm, 0.f) + 1e-5f);
}
#define PIN16(x) asm volatile("" : "+v"(x[0]), "+v"(x[1]), "+v"(x[2]), "+v"(x[3]), "+v"(x[4]), "+v"(x[5]), "+v"(x[6]), "+v"(x[7]), \
                                   "+v"(x[8]), "+v"(x[9]), "+v"(x[10]), "+v"(x[11]), "+v"(x[12]), "+v"(x[13]), "+v"(x[14]), "+v"(x[15]))
DI void w1_offsets(unsigned (&v)[7], int lane, unsigned rot) {
#pragma unroll
  for (int p = 0; p < 7; p++) {
    const unsigned sl = 64 * p + lane, rho = (sl * 4682u) >> 16, j = sl - 14 * rho;
    v[p] = rho * 6944 + lane * 16 + (p >= 4 ? 4096 : 0) + rot - (j >= 14 - (rot >> 4) ? 224 : 0);
  }
}
template <int T> DI void w1_prefetch(rsrc_t srdW1, const unsigned (&v)[7], unsigned w, unsigned w0) {
  asm volatile("s_mov_b32 m0, %8\n\ts_nop 0\n\t"
               "buffer_load_dwordx4 %0, %7, %9 offen lds\n\tbuffer_load_dwordx4 %1, %7, %9 offen offset:1024 lds\n\t"
               "buffer_load_dwordx4 %2, %7, %9 offen offset:2048 lds\n\tbuffer_load_dwordx4 %3, %7, %9 offen offset:3072 lds\n\t"
               "s_add_u32 m0, %8, 4096\n\ts_nop 0\n\t"
               "buffer_load_dwordx4 %4, %7, %9 offen lds\n\tbuffer_load_dwordx4 %5, %7, %9 offen offset:1024 lds\n\t"
               "buffer_load_dwordx4 %6, %7, %9 offen offset:2048 lds"
               : : "v"(v[0]), "v"(v[1]), "v"(v[2]), "v"(v[3]), "v"(v[4]), "v"(v[5]), "v"(v[6]), "s"(srdW1),
                   "s"(8192 + T * 28672 + w * 7168), "s"(w0 + T * 64 * 7168) : "memory", "scc");
}
template <bool FIRST>
DI void ln_tail(LAS char* lds, rsrc_t ws, const f32x2 (&xs)[16], float sh, unsigned fo, int w, int lane) {
  LAS f32x2* red = (LAS f32x2*)(lds + LDS_RED) + 32;
  row_partials(xs, red + w * 8, lane);
#pragma unroll
  for (int it = 0; it < 4; it++) {
    u32x4 o;
#pragma unroll
    for (int j = 0; j < 4; j++) o[j] = pk2(xs[it * 4 + j] + sh);
    if (FIRST) *(LAS u32x4*)(lds + LDS_X1 + (it * 256 + w * 64 + lane) * 16) = o;
    else st16(o, ws, fo + it * 8192, WS_X3);
  }
  LDSBAR();
  float dm, rstd;
  row_stats(red, lane & 7, dm, rstd);
  if (FIRST && w == 0 && lane < 8) *(LAS float*)(lds + LDS_RED + 512 + lane * 4) = sh + dm;
  const float nb = -dm * rstd;
#pragma unroll
  for (int it = 0; it < 4; it++) {
    u32x4 z;
#pragma unroll
    for (int j = 0; j < 4; j++) z[j] = pk2(xs[it * 4 + j] * rstd + nb);
    st16(z, ws, fo + it * 8192, WS_XN + (FIRST ? 0 : 4 * MB));
  }
}

__global__ __launch_bounds__(256, 1) void k_h3_geglu(const u16* __restrict__ qin, const u16* __restrict__ kin, const u16* __restrict__ vin,
                                                     const u16* __restrict__ shk, const u16* __restrict__ hk, const u16* __restrict__ Dv,
                                                     const u16* __restrict__ wg, const u16* __restrict__ wu, const u16* __restrict__ wd,
                                                     char* __restrict__ wsp, u16* __restrict__ out) {
  extern __shared__ __attribute__((aligned(16))) char smem[];
  LAS char* lds = (LAS char*)smem;
  const int w = __builtin_amdgcn_readfirstlane(threadIdx.x >> 6), mh = w & 1, kh = w >> 1;
  const int xcd = blockIdx.x & 7, idx = blockIdx.x >> 3;
  const int blk = xcd * 32 + idx;
  const int mt = idx & 7, nt = xcd * 4 + (idx >> 3);
  const int c = xcd & 1, mt2 = (xcd >> 1) * 4 + (idx & 3), nt2 = idx >> 2;
  const rsrc_t ws = mk(wsp, WS_BYTES), srdW1 = mk((w & 1) ? wu : wg, 1024 * 7168), srdW2 = mk(wd, 3584 * 2048);
  const unsigned w0 = (w >> 1) * 32 * 7168 + nt * 224, rot = (nt & 1) * 32;
  const int m16 = blk >> 1;
  u64 tag;
  {
    const int lane = threadIdx.x & 63, r = lane & 15, q = lane >> 4;
    const int lg = blk >> 4, tb = blk & 15;
    const rsrc_t sK = mk(kin, 4 * MB), sV = mk(vin, 4 * MB), sH = mk(hk + (size_t)(blk * 4 + w) * 2048, 4096);
    const int t0 = 128 * tb + 32 * w + 4 * (lane >> 3);
    const unsigned co = lg * 128 + (lane & 7) * 16;
    u32x4 kr[7], vr[4], sv[4], hd[4]; u32x2 hn[4], qa[4], qb[4];
#pragma unroll
    for (int j = 0; j < 4; j++) kr[3 + j] = __builtin_amdgcn_raw_buffer_load_b128(sK, (t0 + j) * 2048 + co, 0, 0);
#pragma unroll
    for (int j = 0; j < 4; j++) vr[j] = __builtin_amdgcn_raw_buffer_load_b128(sV, (t0 + j) * 2048 + co, 0, 0);
#pragma unroll
    for (int j = 0; j < 3; j++)
      kr[j] = __builtin_amdgcn_raw_buffer_load_b128(sK, (lane < 8 && t0 - 3 + j >= 0) ? (t0 - 3 + j) * 2048 + co : 0x7ffffff0u, 0, 0);
#pragma unroll
    for (int j = 0; j < 4; j++) sv[j] = *(const u32x4*)((const char*)shk + co * 4 + j * 16);
    const u32x2 ep = ld8(ws, 0, WS_EPOCH);
    {
      u64 pc; unsigned sz;
      asm volatile(".Lh3_code_begin:\n\ts_getpc_b64 %0\n\ts_mov_b32 %1, .Lh3_code_end-.Lh3_code_begin" : "=s"(pc), "=s"(sz));
      if (w == 3) touch(mk((const void*)(pc & ~127ull), sz), lane < 16 ? (lane * 32 + idx) * 128 : 0x7ffffff0u);
    }
    asm volatile("" : "+v"(kr[0]), "+v"(kr[1]), "+v"(kr[2]), "+v"(kr[3]), "+v"(kr[4]), "+v"(kr[5]), "+v"(kr[6]), "+v"(vr[0]), "+v"(vr[1]), "+v"(vr[2]),
                      "+v"(vr[3]), "+v"(sv[0]), "+v"(sv[1]), "+v"(sv[2]), "+v"(sv[3]));
    __builtin_amdgcn_sched_barrier(0);
#pragma unroll
    for (int n = 0; n < 4; n++) {
      hd[n] = __builtin_amdgcn_raw_buffer_load_b128(sH, lane * 16 + n * 1024, 0, 0);
      hn[n] = __builtin_amdgcn_raw_buffer_load_b64(sH, lane * 16 + n * 1024 + 16, 0, 0);
    }
    const int r8 = lane & 7, lq = (lane >> 3) & 3, kk = lane >> 5, lr = (blk & 1) * 8 + r8;
    __builtin_amdgcn_sched_barrier(0);
#pragma unroll
    for (int j = 0; j < 3; j++)
#pragma unroll
      for (int d = 0; d < 4; d++) {
        const unsigned nb = __builtin_amdgcn_ds_bpermute((lane - 8) * 4, kr[4 + j][d]);
        kr[j][d] = lane < 8 ? kr[j][d] : nb;
      }
    {
      const int x = 124 - (32 * w + 4 * (lane >> 3));
      LAS char* ip = lds + CV_T + (lane & 7) * 2048 + (((x >> 3) ^ (lane & 7)) * 16) + (x & 7) * 2;
#pragma unroll
      for (int d = 0; d < 4; d++) {
        const f32x2 c0 = {blo(sv[d][0]), blo(sv[d][2])}, c1 = {bhi(sv[d][0]), bhi(sv[d][2])}, c2 = {blo(sv[d][1]), blo(sv[d][3])}, c3 = {bhi(sv[d][1]), bhi(sv[d][3])};
        f32x2 kf[7], u[4];
#pragma unroll
        for (int j = 0; j < 7; j++) kf[j] = up2(kr[j][d]);
#pragma unroll
        for (int j = 0; j < 4; j++) u[j] = (kf[j] * c3 + kf[j + 1] * c2 + kf[j + 2] * c1 + kf[j + 3] * c0) * up2(vr[j][d]);
#pragma unroll
        for (int e = 0; e < 2; e++) *(LAS u32x2*)(ip + (2 * d + e) * 256) = u32x2{pk(u[3][e], u[2][e]), pk(u[1][e], u[0][e])};
      }
    }
    { const unsigned e = __builtin_amdgcn_readfirstlane(ep[0]) + 1u; tag = ((u64)(e ^ 0x9e3779b9u) << 32) | e; }
    LDSBAR();
#pragma unroll
    for (int it = 0; it < 4; it++) {
      const int ch = 16 * w + 4 * it + q;
      st16(*(LAS const u32x4*)(lds + CV_T + ch * 256 + ((r ^ (ch >> 3)) * 16)), ws, (lg * 64 + ch) * 4096 + r * 16, WS_U + (15 - tb) * 256);
    }
    WAITVM(0);
    put_flag(ws, F_U + (blk * 4 + w) * 128, tag, lane);
    __builtin_amdgcn_sched_barrier(0);
    {
      const char* p = (const char*)qin + (m16 * 16 + lr) * 2048 + (8 * w + kk) * 64 + lq * 8;
#pragma unroll
      for (int it = 0; it < 4; it++) { qa[it] = *(const u32x2*)(p + it * 128); qb[it] = *(const u32x2*)(p + it * 128 + 32); }
    }
    const u16 sh16 = qin[(m16 * 16 + lr) * 1024], dd16 = Dv[blk * 4 + w];
    __builtin_amdgcn_sched_barrier(0);
    LAS char* hb = lds + w * CV_CH;
    if (lane < 32) *(LAS u32x4*)(hb + CV_UO + 4096 + lane * 16) = u32x4{0u, 0u, 0u, 0u};
#pragma unroll
    for (int n = 0; n < 4; n++) {
      const unsigned d0 = hd[n][0], d1 = hd[n][1], d2 = hd[n][2], d3 = hd[n][3], d4 = hn[n][0], d5 = hn[n][1];
      const unsigned o0 = __builtin_amdgcn_alignbit(d1, d0, 16), o1 = __builtin_amdgcn_alignbit(d2, d1, 16), o2 = __builtin_amdgcn_alignbit(d3, d2, 16),
                     o3 = __builtin_amdgcn_alignbit(d4, d3, 16), o4 = __builtin_amdgcn_alignbit(d5, d4, 16);
      LAS char* p = hb + 32 + (n * 64 + lane) * 16;
      *(LAS u32x4*)(p) = u32x4{d0, d1, d2, d3};
      *(LAS u32x4*)(p + CV_CP) = u32x4{o0, o1, o2, o3};
      *(LAS u32x4*)(p + 2 * CV_CP) = u32x4{d1, d2, d3, d4};
      *(LAS u32x4*)(p + 3 * CV_CP) = u32x4{o1, o2, o3, o4};
    }
    const unsigned e0 = __builtin_amdgcn_readfirstlane(hd[0][0]), e1 = __builtin_amdgcn_readfirstlane(hd[0][1]);
    if (lane < 8) {
      const int a = lane & 3, half = lane >> 2;
      const unsigned z3 = a == 0 ? 0u : a == 1 ? (e0 << 16) : a == 2 ? e0 : ((e0 >> 16) | (e1 << 16)), z2 = a == 3 ? (e0 << 16) : 0u;
      *(LAS u32x4*)(hb + a * CV_CP + half * 16) = u32x4{0u, 0u, half ? z2 : 0u, half ? z3 : 0u};
    }
    const float sh = b2f(sh16), dd = b2f(dd16);
    f32x2 xs[16];
#pragma unroll
    for (int e = 0; e < 16; e++) xs[e] = up2((e & 2) ? qb[e >> 2][e & 1] : qa[e >> 2][e & 1]) - sh;
    row_partials(xs, (LAS f32x2*)(lds + LDS_RED) + w * 8, lane);
    __builtin_amdgcn_sched_barrier(0);
    wait_flags(ws, F_U + ((lg * 16 + r) * 4 + (tb >> 2)) * 128, tag);
    u32x4 uu[4];
#pragma unroll
    for (int n = 0; n < 4; n++) uu[n] = ld16(ws, lane * 16 + n * 1024, WS_U + (blk * 4 + w) * 4096);
#pragma unroll
    for (int n = 0; n < 4; n++) *(LAS u32x4*)(hb + CV_UO + n * 1024 + lane * 16) = uu[n];
    __builtin_amdgcn_sched_barrier(0);
    const int a4 = (r + 1) & 3;
    const unsigned ap = w * CV_CH + a4 * CV_CP + 2 * (r + 1 - a4 + 8 * q);
    const unsigned bp = w * CV_CH + CV_UO + 2 * (2032 - 16 * r + 8 * q) - 64 * 56;
    const unsigned up = w * CV_CH + CV_UO + 2 * (2044 - 16 * r - 4 * q) - 512 * 7;
    const int pw = blk * 4 + w, ks = pw >> 3, row = (lane >> 2) & 7, l7 = (lane * 37) >> 8;
    const bool shared = ks % 7 == 3;
    const unsigned vd = (lane >> 3) * 2048 + ((((lane & 7) >> 1) ^ ((lane >> 4) & 3)) * 32) + (lane & 1) * 16, ldd = CV_T + w * 4096;
    const unsigned va = ldd + row * 128 + (lane & 3) * 8 + ((row >> 1) & 3) * 32 + (lane >> 5) * (shared ? 2048 : 1024), vb = va + (shared ? 1024 : 2048);
    const unsigned sd = pw < 896 ? ks * 65536 + (pw & 7) * 256 : 0x7ff00000u, so = pw < 896 ? WS_W2 + ((pw & 7) * 112 + ks) * 8192 : 0x7ff00000u;
    const unsigned tl = xcd * 896 + (lane - 7 * l7) * 128;
    const unsigned vt = lane < 56 ? (16 * idx + 8 * kh + l7) * 7168 + tl : 0x7ffffff0u;
    const unsigned vt1 = lane < 28 ? (512 + 8 * idx + 4 * kh + l7) * 7168 + tl : 0x7ffffff0u;
    f32x32 acc; u32x16 uv;
    asm volatile(
        "s_waitcnt lgkmcnt(0)\n\t"
        "ds_read_b64 v[96:97], %[ap] offset:0\n\t"
        "ds_read_b64 v[98:99], %[ap] offset:8\n\t"
        "ds_read_b128 v[160:163], %[bp] offset:3584\n\t"
        "ds_read_b64 v[100:101], %[ap] offset:512\n\t"
        "ds_read_b64 v[102:103], %[ap] offset:520\n\t"
        "ds_read_b128 v[164:167], %[bp] offset:3072\n\t"
        "ds_read_b64 v[104:105], %[ap] offset:1024\n\t"
        "ds_read_b64 v[106:107], %[ap] offset:1032\n\t"
        "ds_read_b128 v[168:171], %[bp] offset:2560\n\t"
        "ds_read_b64 v[108:109], %[ap] offset:1536\n\t"
        "ds_read_b64 v[110:111], %[ap] offset:1544\n\t"
        "ds_read_b128 v[172:175], %[bp] offset:2048\n\t"
        "ds_read_b64 v[112:113], %[ap] offset:2048\n\t"
        "ds_read_b64 v[114:115], %[ap] offset:2056\n\t"
        "ds_read_b128 v[176:179], %[bp] offset:1536\n\t"
        "s_waitcnt lgkmcnt(9)\n\t"
        "ds_read_b64 v[116:117], %[ap] offset:2560\n\t"
        "ds_read_b64 v[118:119], %[ap] offset:2568\n\t"
        "ds_read_b128 v[180:183], %[bp] offset:1024\n\t"
        "ds_read_b64 v[120:121], %[ap] offset:3072\n\t"
        "ds_read_b64 v[122:123], %[ap] offset:3080\n\t"
        "ds_read_b128 v[184:187], %[bp] offset:512\n\t"
        "s_waitcnt lgkmcnt(9)\n\t"
        "ds_read_b64 v[124:125], %[ap] offset:3584\n\t"
        "ds_read_b64 v[126:127], %[ap] offset:3592\n\t"
        "ds_read_b128 v[188:191], %[bp] offset:0\n\t"
        "v_add_u32 v58, 15360, %[vd]\n\t"
        "v_add_u32 v59, 30720, %[vd]\n\t"
        "v_add_u32 v60, 46080, %[vd]\n\t"
        "s_add_u32 s44, %[sd], 128\n\t"
        "s_add_u32 s45, %[so], 4096\n\t"
        "s_mov_b32 m0, %[ld]\n\t"
        "s_nop 0\n\t"
        "buffer_load_dwordx4 %[vd], %[srdW2], %[sd] offen offset:0 lds\n\t"
        "buffer_load_dwordx4 v58, %[srdW2], %[sd] offen offset:1024 lds\n\t"
        "buffer_load_dwordx4 v59, %[srdW2], %[sd] offen offset:2048 lds\n\t"
        "buffer_load_dwordx4 v60, %[srdW2], %[sd] offen offset:3072 lds\n\t"
        "s_add_u32 m0, %[ld], 47104\n\t"
        "s_nop 0\n\t"
        "buffer_load_dwordx4 %[vd], %[srdW2], s44 offen offset:0 lds\n\t"
        "buffer_load_dwordx4 v58, %[srdW2], s44 offen offset:1024 lds\n\t"
        "buffer_load_dwordx4 v59, %[srdW2], s44 offen offset:2048 lds\n\t"
        "buffer_load_dwordx4 v60, %[srdW2], s44 offen offset:3072 lds\n\t"
        "v_mfma_f32_16x16x32_bf16 v[64:67], v[96:99], v[160:163], 0\n\t"
        "ds_read_b64 v[128:129], %[ap] offset:64\n\t"
        "v_mfma_f32_16x16x32_bf16 v[68:71], v[100:103], v[160:163], 0\n\t"
        "v_mfma_f32_16x16x32_bf16 v[68:71], v[96:99], v[164:167], v[68:71]\n\t"
        "ds_read_b64 v[130:131], %[ap] offset:72\n\t"
        "v_mfma_f32_16x16x32_bf16 v[72:75], v[96:99], v[168:171], 0\n\t"
        "ds_read_b128 v[192:195], %[bp] offset:3648\n\t"
        "v_mfma_f32_16x16x32_bf16 v[72:75], v[100:103], v[164:167], v[72:75]\n\t"
        "v_mfma_f32_16x16x32_bf16 v[72:75], v[104:107], v[160:163], v[72:75]\n\t"
        "s_waitcnt lgkmcnt(9)\n\t"
        "ds_read_b64 v[132:133], %[ap] offset:576\n\t"
        "v_mfma_f32_16x16x32_bf16 v[76:79], v[108:111], v[160:163], 0\n\t"
        "ds_read_b64 v[134:135], %[ap] offset:584\n\t"
        "v_mfma_f32_16x16x32_bf16 v[76:79], v[104:107], v[164:167], v[76:79]\n\t"
        "v_mfma_f32_16x16x32_bf16 v[76:79], v[100:103], v[168:171], v[76:79]\n\t"
        "ds_read_b128 v[196:199], %[bp] offset:3136\n\t"
        "v_mfma_f32_16x16x32_bf16 v[76:79], v[96:99], v[172:175], v[76:79]\n\t"
        "ds_read_b64 v[136:137], %[ap] offset:1088\n\t"
        "v_mfma_f32_16x16x32_bf16 v[80:83], v[96:99], v[176:179], 0\n\t"
        "v_mfma_f32_16x16x32_bf16 v[80:83], v[100:103], v[172:175], v[80:83]\n\t"
        "ds_read_b64 v[138:139], %[ap] offset:1096\n\t"
        "v_mfma_f32_16x16x32_bf16 v[80:83], v[104:107], v[168:171], v[80:83]\n\t"
        "ds_read_b128 v[200:203], %[bp] offset:2624\n\t"
        "v_mfma_f32_16x16x32_bf16 v[80:83], v[108:111], v[164:167], v[80:83]\n\t"
        "v_mfma_f32_16x16x32_bf16 v[80:83], v[112:115], v[160:163], v[80:83]\n\t"
        "s_waitcnt lgkmcnt(9)\n\t"
        "ds_read_b64 v[140:141], %[ap] offset:1600\n\t"
        "v_mfma_f32_16x16x32_bf16 v[84:87], v[116:119], v[160:163], 0\n\t"
        "ds_read_b64 v[142:143], %[ap] offset:1608\n\t"
        "v_mfma_f32_16x16x32_bf16 v[84:87], v[112:115], v[164:167], v[84:87]\n\t"
        "v_mfma_f32_16x16x32_bf16 v[84:87], v[108:111], v[168:171], v[84:87]\n\t"
        "ds_read_b128 v[204:207], %[bp] offset:2112\n\t"
        "v_mfma_f32_16x16x32_bf16 v[84:87], v[104:107], v[172:175], v[84:87]\n\t"
        "ds_read_b64 v[144:145], %[ap] offset:2112\n\t"
        "v_mfma_f32_16x16x32_bf16 v[84:87], v[100:103], v[176:179], v[84:87]\n\t"
        "v_mfma_f32_16x16x32_bf16 v[84:87], v[96:99], v[180:183], v[84:87]\n\t"
        "ds_read_b64 v[146:147], %[ap] offset:2120\n\t"
        "v_mfma_f32_16x16x32_bf16 v[88:91], v[96:99], v[184:187], 0\n\t"
        "ds_read_b128 v[208:211], %[bp] offset:1600\n\t"
        "v_mfma_f32_16x16x32_bf16 v[88:91], v[100:103], v[180:183], v[88:91]\n\t"
        "v_mfma_f32_16x16x32_bf16 v[88:91], v[104:107], v[176:179], v[88:91]\n\t"
        "s_waitcnt lgkmcnt(9)\n\t"
        "ds_read_b64 v[148:149], %[ap] offset:2624\n\t"
        "v_mfma_f32_16x16x32_bf16 v[88:91], v[108:111], v[172:175], v[88:91]\n\t"
        "ds_read_b64 v[150:151], %[ap] offset:2632\n\t"
        "v_mfma_f32_16x16x32_bf16 v[88:91], v[112:115], v[168:171], v[88:91]\n\t"
        "v_mfma_f32_16x16x32_bf16 v[88:91], v[116:119], v[164:167], v[88:91]\n\t"
        "ds_read_b128 v[212:215], %[bp] offset:1088\n\t"
        "v_mfma_f32_16x16x32_bf16 v[88:91], v[120:123], v[160:163], v[88:91]\n\t"
        "ds_read_b64 v[152:153], %[ap] offset:3136\n\t"
        "v_mfma_f32_16x16x32_bf16 v[92:95], v[124:127], v[160:163], 0\n\t"
        "v_mfma_f32_16x16x32_bf16 v[92:95], v[120:123], v[164:167], v[92:95]\n\t"
        "ds_read_b64 v[154:155], %[ap] offset:3144\n\t"
        "v_mfma_f32_16x16x32_bf16 v[92:95], v[116:119], v[168:171], v[92:95]\n\t"
        "ds_read_b128 v[216:219], %[bp] offset:576\n\t"
        "v_mfma_f32_16x16x32_bf16 v[92:95], v[112:115], v[172:175], v[92:95]\n\t"
        "v_mfma_f32_16x16x32_bf16 v[92:95], v[108:111], v[176:179], v[92:95]\n\t"
        "s_waitcnt lgkmcnt(9)\n\t"
        "ds_read_b64 v[156:157], %[ap] offset:3648\n\t"
        "v_mfma_f32_16x16x32_bf16 v[92:95], v[104:107], v[180:183], v[92:95]\n\t"
        "ds_read_b64 v[158:159], %[ap] offset:3656\n\t"
        "v_mfma_f32_16x16x32_bf16 v[92:95], v[100:103], v[184:187], v[92:95]\n\t"
        "v_mfma_f32_16x16x32_bf16 v[92:95], v[96:99], v[188:191], v[92:95]\n\t"
        "ds_read_b128 v[220:223], %[bp] offset:64\n\t"
        "v_mfma_f32_16x16x32_bf16 v[64:67], v[128:131], v[192:195], v[64:67]\n\t"
        "ds_read_b64 v[96:97], %[ap] offset:128\n\t"
        "v_mfma_f32_16x16x32_bf16 v[68:71], v[132:135], v[192:195], v[68:71]\n\t"
        "v_mfma_f32_16x16x32_bf16 v[68:71], v[128:131], v[196:199], v[68:71]\n\t"
        "ds_read_b64 v[98:99], %[ap] offset:136\n\t"
        "v_mfma_f32_16x16x32_bf16 v[72:75], v[128:131], v[200:203], v[72:75]\n\t"
        "ds_read_b128 v[160:163], %[bp] offset:3712\n\t"
        "v_mfma_f32_16x16x32_bf16 v[72:75], v[132:135], v[196:199], v[72:75]\n\t"
        "v_mfma_f32_16x16x32_bf16 v[72:75], v[136:139], v[192:195], v[72:75]\n\t"
        "s_waitcnt lgkmcnt(9)\n\t"
        "ds_read_b64 v[100:101], %[ap] offset:640\n\t"
        "v_mfma_f32_16x16x32_bf16 v[76:79], v[140:143], v[192:195], v[76:79]\n\t"
        "ds_read_b64 v[102:103], %[ap] offset:648\n\t"
        "v_mfma_f32_16x16x32_bf16 v[76:79], v[136:139], v[196:199], v[76:79]\n\t"
        "v_mfma_f32_16x16x32_bf16 v[76:79], v[132:135], v[200:203], v[76:79]\n\t"
        "ds_read_b128 v[164:167], %[bp] offset:3200\n\t"
        "v_mfma_f32_16x16x32_bf16 v[76:79], v[128:131], v[204:207], v[76:79]\n\t"
        "ds_read_b64 v[104:105], %[ap] offset:1152\n\t"
        "v_mfma_f32_16x16x32_bf16 v[80:83], v[128:131], v[208:211], v[80:83]\n\t"
        "v_mfma_f32_16x16x32_bf16 v[80:83], v[132:135], v[204:207], v[80:83]\n\t"
        "ds_read_b64 v[106:107], %[ap] offset:1160\n\t"
        "v_mfma_f32_16x16x32_bf16 v[80:83], v[136:139], v[200:203], v[80:83]\n\t"
        "ds_read_b128 v[168:171], %[bp] offset:2688\n\t"
        "v_mfma_f32_16x16x32_bf16 v[80:83], v[140:143], v[196:199], v[80:83]\n\t"
        "v_mfma_f32_16x16x32_bf16 v[80:83], v[144:147], v[192:195], v[80:83]\n\t"
        "s_waitcnt lgkmcnt(9)\n\t"
        "ds_read_b64 v[108:109], %[ap] offset:1664\n\t"
        "v_mfma_f32_16x16x32_bf16 v[84:87], v[148:151], v[192:195], v[84:87]\n\t"
        "ds_read_b64 v[110:111], %[ap] offset:1672\n\t"
        "v_mfma_f32_16x16x32_bf16 v[84:87], v[144:147], v[196:199], v[84:87]\n\t"
        "v_mfma_f32_16x16x32_bf16 v[84:87], v[140:143], v[200:203], v[84:87]\n\t"
        "ds_read_b128 v[172:175], %[bp] offset:2176\n\t"
        "v_mfma_f32_16x16x32_bf16 v[84:87], v[136:139], v[204:207], v[84:87]\n\t"
        "ds_read_b64 v[112:113], %[ap] offset:2176\n\t"
        "v_mfma_f32_16x16x32_bf16 v[84:87], v[132:135], v[208:211], v[84:87]\n\t"
        "v_mfma_f32_16x16x32_bf16 v[84:87], v[128:131], v[212:215], v[84:87]\n\t"
        "ds_read_b64 v[114:115], %[ap] offset:2184\n\t"
        "v_mfma_f32_16x16x32_bf16 v[88:91], v[128:131], v[216:219], v[88:91]\n\t"
        "ds_read_b128 v[176:179], %[bp] offset:1664\n\t"
        "v_mfma_f32_16x16x32_bf16 v[88:91], v[132:135], v[212:215], v[88:91]\n\t"
        "v_mfma_f32_16x16x32_bf16 v[88:91], v[136:139], v[208:211], v[88:91]\n\t"
        "s_waitcnt lgkmcnt(9)\n\t"
        "ds_read_b64 v[116:117], %[ap] offset:2688\n\t"
        "v_mfma_f32_16x16x32_bf16 v[88:91], v[140:143], v[204:207], v[88:91]\n\t"
        "ds_read_b64 v[118:119], %[ap] offset:2696\n\t"
        "v_mfma_f32_16x16x32_bf16 v[88:91], v[144:147], v[200:203], v[88:91]\n\t"
        "v_mfma_f32_16x16x32_bf16 v[88:91], v[148:151], v[196:199], v[88:91]\n\t"
        "ds_read_b128 v[180:183], %[bp] offset:1152\n\t"
        "v_mfma_f32_16x16x32_bf16 v[88:91], v[152:155], v[192:195], v[88:91]\n\t"
        "ds_read_b64 v[120:121], %[ap] offset:3200\n\t"
        "v_mfma_f32_16x16x32_bf16 v[92:95], v[156:159], v[192:195], v[92:95]\n\t"
        "v_mfma_f32_16x16x32_bf16 v[92:95], v[152:155], v[196:199], v[92:95]\n\t"
        "ds_read_b64 v[122:123], %[ap] offset:3208\n\t"
        "v_mfma_f32_16x16x32_bf16 v[92:95], v[148:151], v[200:203], v[92:95]\n\t"
        "ds_read_b128 v[184:187], %[bp] offset:640\n\t"
        "v_mfma_f32_16x16x32_bf16 v[92:95], v[144:147], v[204:207], v[92:95]\n\t"
        "v_mfma_f32_16x16x32_bf16 v[92:95], v[140:143], v[208:211], v[92:95]\n\t"
        "s_waitcnt lgkmcnt(9)\n\t"
        "ds_read_b64 v[124:125], %[ap] offset:3712\n\t"
        "v_mfma_f32_16x16x32_bf16 v[92:95], v[136:139], v[212:215], v[92:95]\n\t"
        "ds_read_b64 v[126:127], %[ap] offset:3720\n\t"
        "v_mfma_f32_16x16x32_bf16 v[92:95], v[132:135], v[216:219], v[92:95]\n\t"
        "v_mfma_f32_16x16x32_bf16 v[92:95], v[128:131], v[220:223], v[92:95]\n\t"
        "ds_read_b128 v[188:191], %[bp] offset:128\n\t"
        "v_mfma_f32_16x16x32_bf16 v[64:67], v[96:99], v[160:163], v[64:67]\n\t"
        "ds_read_b64 v[128:129], %[ap] offset:192\n\t"
        "v_mfma_f32_16x16x32_bf16 v[68:71], v[100:103], v[160:163], v[68:71]\n\t"
        "v_mfma_f32_16x16x32_bf16 v[68:71], v[96:99], v[164:167], v[68:71]\n\t"
        "ds_read_b64 v[130:131], %[ap] offset:200\n\t"
        "v_mfma_f32_16x16x32_bf16 v[72:75], v[96:99], v[168:171], v[72:75]\n\t"
        "ds_read_b128 v[192:195], %[bp] offset:3776\n\t"
        "v_mfma_f32_16x16x32_bf16 v[72:75], v[100:103], v[164:167], v[72:75]\n\t"
        "v_mfma_f32_16x16x32_bf16 v[72:75], v[104:107], v[160:163], v[72:75]\n\t"
        "s_waitcnt lgkmcnt(9)\n\t"
        "ds_read_b64 v[132:133], %[ap] offset:704\n\t"
        "v_mfma_f32_16x16x32_bf16 v[76:79], v[108:111], v[160:163], v[76:79]\n\t"
        "ds_read_b64 v[134:135], %[ap] offset:712\n\t"
        "v_mfma_f32_16x16x32_bf16 v[76:79], v[104:107], v[164:167], v[76:79]\n\t"
        "v_mfma_f32_16x16x32_bf16 v[76:79], v[100:103], v[168:171], v[76:79]\n\t"
        "ds_read_b128 v[196:199], %[bp] offset:3264\n\t"
        "v_mfma_f32_16x16x32_bf16 v[76:79], v[96:99], v[172:175], v[76:79]\n\t"
        "ds_read_b64 v[136:137], %[ap] offset:1216\n\t"
        "v_mfma_f32_16x16x32_bf16 v[80:83], v[96:99], v[176:179], v[80:83]\n\t"
        "v_mfma_f32_16x16x32_bf16 v[80:83], v[100:103], v[172:175], v[80:83]\n\t"
        "ds_read_b64 v[138:139], %[ap] offset:1224\n\t"
        "v_mfma_f32_16x16x32_bf16 v[80:83], v[104:107], v[168:171], v[80:83]\n\t"
        "ds_read_b128 v[200:203], %[bp] offset:2752\n\t"
        "v_mfma_f32_16x16x32_bf16 v[80:83], v[108:111], v[164:167], v[80:83]\n\t"
        "v_mfma_f32_16x16x32_bf16 v[80:83], v[112:115], v[160:163], v[80:83]\n\t"
        "s_waitcnt lgkmcnt(9)\n\t"
        "ds_read_b64 v[140:141], %[ap] offset:1728\n\t"
        "v_mfma_f32_16x16x32_bf16 v[84:87], v[116:119], v[160:163], v[84:87]\n\t"
        "ds_read_b64 v[142:143], %[ap] offset:1736\n\t"
        "v_mfma_f32_16x16x32_bf16 v[84:87], v[112:115], v[164:167], v[84:87]\n\t"
        "v_mfma_f32_16x16x32_bf16 v[84:87], v[108:111], v[168:171], v[84:87]\n\t"
        "ds_read_b128 v[204:207], %[bp] offset:2240\n\t"
        "v_mfma_f32_16x16x32_bf16 v[84:87], v[104:107], v[172:175], v[84:87]\n\t"
        "ds_read_b64 v[144:145], %[ap] offset:2240\n\t"
        "v_mfma_f32_16x16x32_bf16 v[84:87], v[100:103], v[176:179], v[84:87]\n\t"
        "v_mfma_f32_16x16x32_bf16 v[84:87], v[96:99], v[180:183], v[84:87]\n\t"
        "ds_read_b64 v[146:147], %[ap] offset:2248\n\t"
        "v_mfma_f32_16x16x32_bf16 v[88:91], v[96:99], v[184:187], v[88:91]\n\t"
        "ds_read_b128 v[208:211], %[bp] offset:1728\n\t"
        "v_mfma_f32_16x16x32_bf16 v[88:91], v[100:103], v[180:183], v[88:91]\n\t"
        "v_mfma_f32_16x16x32_bf16 v[88:91], v[104:107], v[176:179], v[88:91]\n\t"
        "s_waitcnt lgkmcnt(9)\n\t"
        "ds_read_b64 v[148:149], %[ap] offset:2752\n\t"
        "v_mfma_f32_16x16x32_bf16 v[88:91], v[108:111], v[172:175], v[88:91]\n\t"
        "ds_read_b64 v[150:151], %[ap] offset:2760\n\t"
        "v_mfma_f32_16x16x32_bf16 v[88:91], v[112:115], v[168:171], v[88:91]\n\t"
        "v_mfma_f32_16x16x32_bf16 v[88:91], v[116:119], v[164:167], v[88:91]\n\t"
        "ds_read_b128 v[212:215], %[bp] offset:1216\n\t"
        "v_mfma_f32_16x16x32_bf16 v[88:91], v[120:123], v[160:163], v[88:91]\n\t"
        "ds_read_b64 v[152:153], %[ap] offset:3264\n\t"
        "v_mfma_f32_16x16x32_bf16 v[92:95], v[124:127], v[160:163], v[92:95]\n\t"
        "v_mfma_f32_16x16x32_bf16 v[92:95], v[120:123], v[164:167], v[92:95]\n\t"
        "ds_read_b64 v[154:155], %[ap] offset:3272\n\t"
        "v_mfma_f32_16x16x32_bf16 v[92:95], v[116:119], v[168:171], v[92:95]\n\t"
        "ds_read_b128 v[216:219], %[bp] offset:704\n\t"
        "v_mfma_f32_16x16x32_bf16 v[92:95], v[112:115], v[172:175], v[92:95]\n\t"
        "v_mfma_f32_16x16x32_bf16 v[92:95], v[108:111], v[176:179], v[92:95]\n\t"
        "s_waitcnt lgkmcnt(9)\n\t"
        "ds_read_b64 v[156:157], %[ap] offset:3776\n\t"
        "v_mfma_f32_16x16x32_bf16 v[92:95], v[104:107], v[180:183], v[92:95]\n\t"
        "ds_read_b64 v[158:159], %[ap] offset:3784\n\t"
        "v_mfma_f32_16x16x32_bf16 v[92:95], v[100:103], v[184:187], v[92:95]\n\t"
        "v_mfma_f32_16x16x32_bf16 v[92:95], v[96:99], v[188:191], v[92:95]\n\t"
        "ds_read_b128 v[220:223], %[bp] offset:192\n\t"
        "v_mfma_f32_16x16x32_bf16 v[64:67], v[128:131], v[192:195], v[64:67]\n\t"
        "ds_read_b64 v[96:97], %[ap] offset:256\n\t"
        "v_mfma_f32_16x16x32_bf16 v[68:71], v[132:135], v[192:195], v[68:71]\n\t"
        "v_mfma_f32_16x16x32_bf16 v[68:71], v[128:131], v[196:199], v[68:71]\n\t"
        "ds_read_b64 v[98:99], %[ap] offset:264\n\t"
        "v_mfma_f32_16x16x32_bf16 v[72:75], v[128:131], v[200:203], v[72:75]\n\t"
        "ds_read_b128 v[160:163], %[bp] offset:3840\n\t"
        "s_mov_b32 m0, 131072\n\t"
        "v_mfma_f32_16x16x32_bf16 v[72:75], v[132:135], v[196:199], v[72:75]\n\t"
        "v_mfma_f32_16x16x32_bf16 v[72:75], v[136:139], v[192:195], v[72:75]\n\t"
        "s_waitcnt lgkmcnt(9)\n\t"
        "ds_read_b64 v[100:101], %[ap] offset:768\n\t"
        "buffer_load_dword %[vt], %[srdW1], 0 offen lds\n\t"
        "v_mfma_f32_16x16x32_bf16 v[76:79], v[140:143], v[192:195], v[76:79]\n\t"
        "ds_read_b64 v[102:103], %[ap] offset:776\n\t"
        "v_mfma_f32_16x16x32_bf16 v[76:79], v[136:139], v[196:199], v[76:79]\n\t"
        "v_mfma_f32_16x16x32_bf16 v[76:79], v[132:135], v[200:203], v[76:79]\n\t"
        "ds_read_b128 v[164:167], %[bp] offset:3328\n\t"
        "v_mfma_f32_16x16x32_bf16 v[76:79], v[128:131], v[204:207], v[76:79]\n\t"
        "ds_read_b64 v[104:105], %[ap] offset:1280\n\t"
        "v_mfma_f32_16x16x32_bf16 v[80:83], v[128:131], v[208:211], v[80:83]\n\t"
        "v_mfma_f32_16x16x32_bf16 v[80:83], v[132:135], v[204:207], v[80:83]\n\t"
        "ds_read_b64 v[106:107], %[ap] offset:1288\n\t"
        "v_mfma_f32_16x16x32_bf16 v[80:83], v[136:139], v[200:203], v[80:83]\n\t"
        "ds_read_b128 v[168:171], %[bp] offset:2816\n\t"
        "v_mfma_f32_16x16x32_bf16 v[80:83], v[140:143], v[196:199], v[80:83]\n\t"
        "v_mfma_f32_16x16x32_bf16 v[80:83], v[144:147], v[192:195], v[80:83]\n\t"
        "s_waitcnt lgkmcnt(9)\n\t"
        "ds_read_b64 v[108:109], %[ap] offset:1792\n\t"
        "v_mfma_f32_16x16x32_bf16 v[84:87], v[148:151], v[192:195], v[84:87]\n\t"
        "ds_read_b64 v[110:111], %[ap] offset:1800\n\t"
        "v_mfma_f32_16x16x32_bf16 v[84:87], v[144:147], v[196:199], v[84:87]\n\t"
        "v_mfma_f32_16x16x32_bf16 v[84:87], v[140:143], v[200:203], v[84:87]\n\t"
        "ds_read_b128 v[172:175], %[bp] offset:2304\n\t"
        "v_mfma_f32_16x16x32_bf16 v[84:87], v[136:139], v[204:207], v[84:87]\n\t"
        "ds_read_b64 v[112:113], %[ap] offset:2304\n\t"
        "v_mfma_f32_16x16x32_bf16 v[84:87], v[132:135], v[208:211], v[84:87]\n\t"
        "v_mfma_f32_16x16x32_bf16 v[84:87], v[128:131], v[212:215], v[84:87]\n\t"
        "ds_read_b64 v[114:115], %[ap] offset:2312\n\t"
        "v_mfma_f32_16x16x32_bf16 v[88:91], v[128:131], v[216:219], v[88:91]\n\t"
        "ds_read_b128 v[176:179], %[bp] offset:1792\n\t"
        "v_mfma_f32_16x16x32_bf16 v[88:91], v[132:135], v[212:215], v[88:91]\n\t"
        "v_mfma_f32_16x16x32_bf16 v[88:91], v[136:139], v[208:211], v[88:91]\n\t"
        "s_waitcnt lgkmcnt(9)\n\t"
        "ds_read_b64 v[116:117], %[ap] offset:2816\n\t"
        "v_mfma_f32_16x16x32_bf16 v[88:91], v[140:143], v[204:207], v[88:91]\n\t"
        "ds_read_b64 v[118:119], %[ap] offset:2824\n\t"
        "v_mfma_f32_16x16x32_bf16 v[88:91], v[144:147], v[200:203], v[88:91]\n\t"
        "v_mfma_f32_16x16x32_bf16 v[88:91], v[148:151], v[196:199], v[88:91]\n\t"
        "ds_read_b128 v[180:183], %[bp] offset:1280\n\t"
        "v_mfma_f32_16x16x32_bf16 v[88:91], v[152:155], v[192:195], v[88:91]\n\t"
        "ds_read_b64 v[120:121], %[ap] offset:3328\n\t"
        "v_mfma_f32_16x16x32_bf16 v[92:95], v[156:159], v[192:195], v[92:95]\n\t"
        "v_mfma_f32_16x16x32_bf16 v[92:95], v[152:155], v[196:199], v[92:95]\n\t"
        "ds_read_b64 v[122:123], %[ap] offset:3336\n\t"
        "v_mfma_f32_16x16x32_bf16 v[92:95], v[148:151], v[200:203], v[92:95]\n\t"
        "ds_read_b128 v[184:187], %[bp] offset:768\n\t"
        "v_mfma_f32_16x16x32_bf16 v[92:95], v[144:147], v[204:207], v[92:95]\n\t"
        "v_mfma_f32_16x16x32_bf16 v[92:95], v[140:143], v[208:211], v[92:95]\n\t"
        "s_waitcnt lgkmcnt(9)\n\t"
        "ds_read_b64 v[124:125], %[ap] offset:3840\n\t"
        "v_mfma_f32_16x16x32_bf16 v[92:95], v[136:139], v[212:215], v[92:95]\n\t"
        "ds_read_b64 v[126:127], %[ap] offset:3848\n\t"
        "v_mfma_f32_16x16x32_bf16 v[92:95], v[132:135], v[216:219], v[92:95]\n\t"
        "v_mfma_f32_16x16x32_bf16 v[92:95], v[128:131], v[220:223], v[92:95]\n\t"
        "ds_read_b128 v[188:191], %[bp] offset:256\n\t"
        "v_mfma_f32_16x16x32_bf16 v[64:67], v[96:99], v[160:163], v[64:67]\n\t"
        "ds_read_b64 v[128:129], %[ap] offset:320\n\t"
        "v_mfma_f32_16x16x32_bf16 v[68:71], v[100:103], v[160:163], v[68:71]\n\t"
        "v_mfma_f32_16x16x32_bf16 v[68:71], v[96:99], v[164:167], v[68:71]\n\t"
        "ds_read_b64 v[130:131], %[ap] offset:328\n\t"
        "v_mfma_f32_16x16x32_bf16 v[72:75], v[96:99], v[168:171], v[72:75]\n\t"
        "ds_read_b128 v[192:195], %[bp] offset:3904\n\t"
        "v_mfma_f32_16x16x32_bf16 v[72:75], v[100:103], v[164:167], v[72:75]\n\t"
        "v_mfma_f32_16x16x32_bf16 v[72:75], v[104:107], v[160:163], v[72:75]\n\t"
        "s_waitcnt lgkmcnt(9)\n\t"
        "ds_read_b64 v[132:133], %[ap] offset:832\n\t"
        "v_mfma_f32_16x16x32_bf16 v[76:79], v[108:111], v[160:163], v[76:79]\n\t"
        "ds_read_b64 v[134:135], %[ap] offset:840\n\t"
        "v_mfma_f32_16x16x32_bf16 v[76:79], v[104:107], v[164:167], v[76:79]\n\t"
        "v_mfma_f32_16x16x32_bf16 v[76:79], v[100:103], v[168:171], v[76:79]\n\t"
        "ds_read_b128 v[196:199], %[bp] offset:3392\n\t"
        "v_mfma_f32_16x16x32_bf16 v[76:79], v[96:99], v[172:175], v[76:79]\n\t"
        "ds_read_b64 v[136:137], %[ap] offset:1344\n\t"
        "v_mfma_f32_16x16x32_bf16 v[80:83], v[96:99], v[176:179], v[80:83]\n\t"
        "v_mfma_f32_16x16x32_bf16 v[80:83], v[100:103], v[172:175], v[80:83]\n\t"
        "ds_read_b64 v[138:139], %[ap] offset:1352\n\t"
        "v_mfma_f32_16x16x32_bf16 v[80:83], v[104:107], v[168:171], v[80:83]\n\t"
        "ds_read_b128 v[200:203], %[bp] offset:2880\n\t"
        "v_mfma_f32_16x16x32_bf16 v[80:83], v[108:111], v[164:167], v[80:83]\n\t"
        "v_mfma_f32_16x16x32_bf16 v[80:83], v[112:115], v[160:163], v[80:83]\n\t"
        "s_waitcnt lgkmcnt(9)\n\t"
        "ds_read_b64 v[140:141], %[ap] offset:1856\n\t"
        "v_mfma_f32_16x16x32_bf16 v[84:87], v[116:119], v[160:163], v[84:87]\n\t"
        "ds_read_b64 v[142:143], %[ap] offset:1864\n\t"
        "v_mfma_f32_16x16x32_bf16 v[84:87], v[112:115], v[164:167], v[84:87]\n\t"
        "v_mfma_f32_16x16x32_bf16 v[84:87], v[108:111], v[168:171], v[84:87]\n\t"
        "ds_read_b128 v[204:207], %[bp] offset:2368\n\t"
        "v_mfma_f32_16x16x32_bf16 v[84:87], v[104:107], v[172:175], v[84:87]\n\t"
        "ds_read_b64 v[144:145], %[ap] offset:2368\n\t"
        "v_mfma_f32_16x16x32_bf16 v[84:87], v[100:103], v[176:179], v[84:87]\n\t"
        "v_mfma_f32_16x16x32_bf16 v[84:87], v[96:99], v[180:183], v[84:87]\n\t"
        "ds_read_b64 v[146:147], %[ap] offset:2376\n\t"
        "v_mfma_f32_16x16x32_bf16 v[88:91], v[96:99], v[184:187], v[88:91]\n\t"
        "ds_read_b128 v[208:211], %[bp] offset:1856\n\t"
        "v_mfma_f32_16x16x32_bf16 v[88:91], v[100:103], v[180:183], v[88:91]\n\t"
        "v_mfma_f32_16x16x32_bf16 v[88:91], v[104:107], v[176:179], v[88:91]\n\t"
        "s_waitcnt lgkmcnt(9)\n\t"
        "ds_read_b64 v[148:149], %[ap] offset:2880\n\t"
        "v_mfma_f32_16x16x32_bf16 v[88:91], v[108:111], v[172:175], v[88:91]\n\t"
        "ds_read_b64 v[150:151], %[ap] offset:2888\n\t"
        "v_mfma_f32_16x16x32_bf16 v[88:91], v[112:115], v[168:171], v[88:91]\n\t"
        "v_mfma_f32_16x16x32_bf16 v[88:91], v[116:119], v[164:167], v[88:91]\n\t"
        "ds_read_b128 v[212:215], %[bp] offset:1344\n\t"
        "v_mfma_f32_16x16x32_bf16 v[88:91], v[120:123], v[160:163], v[88:91]\n\t"
        "ds_read_b64 v[152:153], %[ap] offset:3392\n\t"
        "v_mfma_f32_16x16x32_bf16 v[92:95], v[124:127], v[160:163], v[92:95]\n\t"
        "v_mfma_f32_16x16x32_bf16 v[92:95], v[120:123], v[164:167], v[92:95]\n\t"
        "ds_read_b64 v[154:155], %[ap] offset:3400\n\t"
        "v_mfma_f32_16x16x32_bf16 v[92:95], v[116:119], v[168:171], v[92:95]\n\t"
        "ds_read_b128 v[216:219], %[bp] offset:832\n\t"
        "v_mfma_f32_16x16x32_bf16 v[92:95], v[112:115], v[172:175], v[92:95]\n\t"
        "v_mfma_f32_16x16x32_bf16 v[92:95], v[108:111], v[176:179], v[92:95]\n\t"
        "s_waitcnt lgkmcnt(9)\n\t"
        "ds_read_b64 v[156:157], %[ap] offset:3904\n\t"
        "v_mfma_f32_16x16x32_bf16 v[92:95], v[104:107], v[180:183], v[92:95]\n\t"
        "ds_read_b64 v[158:159], %[ap] offset:3912\n\t"
        "v_mfma_f32_16x16x32_bf16 v[92:95], v[100:103], v[184:187], v[92:95]\n\t"
        "v_mfma_f32_16x16x32_bf16 v[92:95], v[96:99], v[188:191], v[92:95]\n\t"
        "ds_read_b128 v[220:223], %[bp] offset:320\n\t"
        "v_mfma_f32_16x16x32_bf16 v[64:67], v[128:131], v[192:195], v[64:67]\n\t"
        "ds_read_b64 v[96:97], %[ap] offset:384\n\t"
        "v_mfma_f32_16x16x32_bf16 v[68:71], v[132:135], v[192:195], v[68:71]\n\t"
        "v_mfma_f32_16x16x32_bf16 v[68:71], v[128:131], v[196:199], v[68:71]\n\t"
        "ds_read_b64 v[98:99], %[ap] offset:392\n\t"
        "v_mfma_f32_16x16x32_bf16 v[72:75], v[128:131], v[200:203], v[72:75]\n\t"
        "ds_read_b128 v[160:163], %[bp] offset:3968\n\t"
        "v_mfma_f32_16x16x32_bf16 v[72:75], v[132:135], v[196:199], v[72:75]\n\t"
        "v_mfma_f32_16x16x32_bf16 v[72:75], v[136:139], v[192:195], v[72:75]\n\t"
        "s_waitcnt lgkmcnt(9)\n\t"
        "ds_read_b64 v[100:101], %[ap] offset:896\n\t"
        "v_mfma_f32_16x16x32_bf16 v[76:79], v[140:143], v[192:195], v[76:79]\n\t"
        "ds_read_b64 v[102:103], %[ap] offset:904\n\t"
        "v_mfma_f32_16x16x32_bf16 v[76:79], v[136:139], v[196:199], v[76:79]\n\t"
        "v_mfma_f32_16x16x32_bf16 v[76:79], v[132:135], v[200:203], v[76:79]\n\t"
        "ds_read_b128 v[164:167], %[bp] offset:3456\n\t"
        "v_mfma_f32_16x16x32_bf16 v[76:79], v[128:131], v[204:207], v[76:79]\n\t"
        "ds_read_b64 v[104:105], %[ap] offset:1408\n\t"
        "v_mfma_f32_16x16x32_bf16 v[80:83], v[128:131], v[208:211], v[80:83]\n\t"
        "v_mfma_f32_16x16x32_bf16 v[80:83], v[132:135], v[204:207], v[80:83]\n\t"
        "ds_read_b64 v[106:107], %[ap] offset:1416\n\t"
        "v_mfma_f32_16x16x32_bf16 v[80:83], v[136:139], v[200:203], v[80:83]\n\t"
        "ds_read_b128 v[168:171], %[bp] offset:2944\n\t"
        "v_mfma_f32_16x16x32_bf16 v[80:83], v[140:143], v[196:199], v[80:83]\n\t"
        "v_mfma_f32_16x16x32_bf16 v[80:83], v[144:147], v[192:195], v[80:83]\n\t"
        "s_waitcnt lgkmcnt(9)\n\t"
        "ds_read_b64 v[108:109], %[ap] offset:1920\n\t"
        "v_mfma_f32_16x16x32_bf16 v[84:87], v[148:151], v[192:195], v[84:87]\n\t"
        "ds_read_b64 v[110:111], %[ap] offset:1928\n\t"
        "v_mfma_f32_16x16x32_bf16 v[84:87], v[144:147], v[196:199], v[84:87]\n\t"
        "v_mfma_f32_16x16x32_bf16 v[84:87], v[140:143], v[200:203], v[84:87]\n\t"
        "ds_read_b128 v[172:175], %[bp] offset:2432\n\t"
        "v_mfma_f32_16x16x32_bf16 v[84:87], v[136:139], v[204:207], v[84:87]\n\t"
        "ds_read_b64 v[112:113], %[ap] offset:2432\n\t"
        "v_mfma_f32_16x16x32_bf16 v[84:87], v[132:135], v[208:211], v[84:87]\n\t"
        "v_mfma_f32_16x16x32_bf16 v[84:87], v[128:131], v[212:215], v[84:87]\n\t"
        "ds_read_b64 v[114:115], %[ap] offset:2440\n\t"
        "v_xor_b32 v58, 32, %[va]\n\t"
        "v_mfma_f32_16x16x32_bf16 v[88:91], v[128:131], v[216:219], v[88:91]\n\t"
        "ds_read_b128 v[176:179], %[bp] offset:1920\n\t"
        "v_mfma_f32_16x16x32_bf16 v[88:91], v[132:135], v[212:215], v[88:91]\n\t"
        "v_xor_b32 v59, 64, %[va]\n\t"
        "v_mfma_f32_16x16x32_bf16 v[88:91], v[136:139], v[208:211], v[88:91]\n\t"
        "s_waitcnt lgkmcnt(9)\n\t"
        "ds_read_b64 v[116:117], %[ap] offset:2944\n\t"
        "v_mfma_f32_16x16x32_bf16 v[88:91], v[140:143], v[204:207], v[88:91]\n\t"
        "ds_read_b64 v[118:119], %[ap] offset:2952\n\t"
        "v_xor_b32 v60, 96, %[va]\n\t"
        "v_mfma_f32_16x16x32_bf16 v[88:91], v[144:147], v[200:203], v[88:91]\n\t"
        "v_mfma_f32_16x16x32_bf16 v[88:91], v[148:151], v[196:199], v[88:91]\n\t"
        "ds_read_b128 v[180:183], %[bp] offset:1408\n\t"
        "v_xor_b32 v61, 32, %[vb]\n\t"
        "v_mfma_f32_16x16x32_bf16 v[88:91], v[152:155], v[192:195], v[88:91]\n\t"
        "ds_read_b64 v[120:121], %[ap] offset:3456\n\t"
        "v_mfma_f32_16x16x32_bf16 v[92:95], v[156:159], v[192:195], v[92:95]\n\t"
        "v_xor_b32 v62, 64, %[vb]\n\t"
        "v_mfma_f32_16x16x32_bf16 v[92:95], v[152:155], v[196:199], v[92:95]\n\t"
        "ds_read_b64 v[122:123], %[ap] offset:3464\n\t"
        "v_mfma_f32_16x16x32_bf16 v[92:95], v[148:151], v[200:203], v[92:95]\n\t"
        "ds_read_b128 v[184:187], %[bp] offset:896\n\t"
        "v_xor_b32 v63, 96, %[vb]\n\t"
        "v_mfma_f32_16x16x32_bf16 v[92:95], v[144:147], v[204:207], v[92:95]\n\t"
        "v_mfma_f32_16x16x32_bf16 v[92:95], v[140:143], v[208:211], v[92:95]\n\t"
        "s_waitcnt lgkmcnt(9)\n\t"
        "ds_read_b64 v[124:125], %[ap] offset:3968\n\t"
        "v_mfma_f32_16x16x32_bf16 v[92:95], v[136:139], v[212:215], v[92:95]\n\t"
        "ds_read_b64 v[126:127], %[ap] offset:3976\n\t"
        "v_mfma_f32_16x16x32_bf16 v[92:95], v[132:135], v[216:219], v[92:95]\n\t"
        "v_mfma_f32_16x16x32_bf16 v[92:95], v[128:131], v[220:223], v[92:95]\n\t"
        "ds_read_b128 v[188:191], %[bp] offset:384\n\t"
        "v_mfma_f32_16x16x32_bf16 v[64:67], v[96:99], v[160:163], v[64:67]\n\t"
        "ds_read_b64 v[128:129], %[ap] offset:448\n\t"
        "v_mfma_f32_16x16x32_bf16 v[68:71], v[100:103], v[160:163], v[68:71]\n\t"
        "s_waitcnt vmcnt(1)\n\t"
        "v_mfma_f32_16x16x32_bf16 v[68:71], v[96:99], v[164:167], v[68:71]\n\t"
        "ds_read_b64 v[130:131], %[ap] offset:456\n\t"
        "ds_read_b64_tr_b16 a[0:1], %[va] offset:0\n\t"
        "v_mfma_f32_16x16x32_bf16 v[72:75], v[96:99], v[168:171], v[72:75]\n\t"
        "s_waitcnt lgkmcnt(9)\n\t"
        "ds_read_b128 v[192:195], %[bp] offset:4032\n\t"
        "v_mfma_f32_16x16x32_bf16 v[72:75], v[100:103], v[164:167], v[72:75]\n\t"
        "ds_read_b64_tr_b16 a[2:3], %[vb] offset:0\n\t"
        "v_mfma_f32_16x16x32_bf16 v[72:75], v[104:107], v[160:163], v[72:75]\n\t"
        "ds_read_b64 v[132:133], %[ap] offset:960\n\t"
        "v_mfma_f32_16x16x32_bf16 v[76:79], v[108:111], v[160:163], v[76:79]\n\t"
        "ds_read_b64 v[134:135], %[ap] offset:968\n\t"
        "ds_read_b64_tr_b16 a[4:5], v58 offset:0\n\t"
        "v_mfma_f32_16x16x32_bf16 v[76:79], v[104:107], v[164:167], v[76:79]\n\t"
        "v_mfma_f32_16x16x32_bf16 v[76:79], v[100:103], v[168:171], v[76:79]\n\t"
        "ds_read_b128 v[196:199], %[bp] offset:3520\n\t"
        "s_waitcnt lgkmcnt(9)\n\t"
        "ds_read_b64_tr_b16 a[6:7], v61 offset:0\n\t"
        "v_mfma_f32_16x16x32_bf16 v[76:79], v[96:99], v[172:175], v[76:79]\n\t"
        "ds_read_b64 v[136:137], %[ap] offset:1472\n\t"
        "v_mfma_f32_16x16x32_bf16 v[80:83], v[96:99], v[176:179], v[80:83]\n\t"
        "ds_read_b64_tr_b16 a[8:9], v59 offset:0\n\t"
        "v_mfma_f32_16x16x32_bf16 v[80:83], v[100:103], v[172:175], v[80:83]\n\t"
        "ds_read_b64 v[138:139], %[ap] offset:1480\n\t"
        "v_mfma_f32_16x16x32_bf16 v[80:83], v[104:107], v[168:171], v[80:83]\n\t"
        "ds_read_b128 v[200:203], %[bp] offset:3008\n\t"
        "ds_read_b64_tr_b16 a[10:11], v62 offset:0\n\t"
        "v_mfma_f32_16x16x32_bf16 v[80:83], v[108:111], v[164:167], v[80:83]\n\t"
        "v_mfma_f32_16x16x32_bf16 v[80:83], v[112:115], v[160:163], v[80:83]\n\t"
        "s_waitcnt lgkmcnt(9)\n\t"
        "ds_read_b64 v[140:141], %[ap] offset:1984\n\t"
        "ds_read_b64_tr_b16 a[12:13], v60 offset:0\n\t"
        "v_mfma_f32_16x16x32_bf16 v[84:87], v[116:119], v[160:163], v[84:87]\n\t"
        "ds_read_b64 v[142:143], %[ap] offset:1992\n\t"
        "v_mfma_f32_16x16x32_bf16 v[84:87], v[112:115], v[164:167], v[84:87]\n\t"
        "ds_read_b64_tr_b16 a[14:15], v63 offset:0\n\t"
        "v_mfma_f32_16x16x32_bf16 v[84:87], v[108:111], v[168:171], v[84:87]\n\t"
        "ds_read_b128 v[204:207], %[bp] offset:2496\n\t"
        "v_mfma_f32_16x16x32_bf16 v[84:87], v[104:107], v[172:175], v[84:87]\n\t"
        "ds_read_b64 v[144:145], %[ap] offset:2496\n\t"
        "s_waitcnt lgkmcnt(9)\n\t"
        "ds_read_b64_tr_b16 a[16:17], %[va] offset:47104\n\t"
        "v_mfma_f32_16x16x32_bf16 v[84:87], v[100:103], v[176:179], v[84:87]\n\t"
        "v_mfma_f32_16x16x32_bf16 v[84:87], v[96:99], v[180:183], v[84:87]\n\t"
        "ds_read_b64 v[146:147], %[ap] offset:2504\n\t"
        "ds_read_b64_tr_b16 a[18:19], %[vb] offset:47104\n\t"
        "v_mfma_f32_16x16x32_bf16 v[88:91], v[96:99], v[184:187], v[88:91]\n\t"
        "ds_read_b128 v[208:211], %[bp] offset:1984\n\t"
        "v_mfma_f32_16x16x32_bf16 v[88:91], v[100:103], v[180:183], v[88:91]\n\t"
        "ds_read_b64_tr_b16 a[20:21], v58 offset:47104\n\t"
        "v_mfma_f32_16x16x32_bf16 v[88:91], v[104:107], v[176:179], v[88:91]\n\t"
        "ds_read_b64 v[148:149], %[ap] offset:3008\n\t"
        "v_mfma_f32_16x16x32_bf16 v[88:91], v[108:111], v[172:175], v[88:91]\n\t"
        "s_waitcnt lgkmcnt(9)\n\t"
        "ds_read_b64 v[150:151], %[ap] offset:3016\n\t"
        "ds_read_b64_tr_b16 a[22:23], v61 offset:47104\n\t"
        "v_mfma_f32_16x16x32_bf16 v[88:91], v[112:115], v[168:171], v[88:91]\n\t"
        "v_mfma_f32_16x16x32_bf16 v[88:91], v[116:119], v[164:167], v[88:91]\n\t"
        "ds_read_b128 v[212:215], %[bp] offset:1472\n\t"
        "ds_read_b64_tr_b16 a[24:25], v59 offset:47104\n\t"
        "v_mfma_f32_16x16x32_bf16 v[88:91], v[120:123], v[160:163], v[88:91]\n\t"
        "ds_read_b64 v[152:153], %[ap] offset:3520\n\t"
        "v_mfma_f32_16x16x32_bf16 v[92:95], v[124:127], v[160:163], v[92:95]\n\t"
        "ds_read_b64_tr_b16 a[26:27], v62 offset:47104\n\t"
        "v_mfma_f32_16x16x32_bf16 v[92:95], v[120:123], v[164:167], v[92:95]\n\t"
        "s_waitcnt lgkmcnt(9)\n\t"
        "ds_read_b64 v[154:155], %[ap] offset:3528\n\t"
        "v_mfma_f32_16x16x32_bf16 v[92:95], v[116:119], v[168:171], v[92:95]\n\t"
        "ds_read_b128 v[216:219], %[bp] offset:960\n\t"
        "ds_read_b64_tr_b16 a[28:29], v60 offset:47104\n\t"
        "v_mfma_f32_16x16x32_bf16 v[92:95], v[112:115], v[172:175], v[92:95]\n\t"
        "v_mfma_f32_16x16x32_bf16 v[92:95], v[108:111], v[176:179], v[92:95]\n\t"
        "ds_read_b64 v[156:157], %[ap] offset:4032\n\t"
        "ds_read_b64_tr_b16 a[30:31], v63 offset:47104\n\t"
        "v_mfma_f32_16x16x32_bf16 v[92:95], v[104:107], v[180:183], v[92:95]\n\t"
        "ds_read_b64 v[158:159], %[ap] offset:4040\n\t"
        "v_mfma_f32_16x16x32_bf16 v[92:95], v[100:103], v[184:187], v[92:95]\n\t"
        "v_mfma_f32_16x16x32_bf16 v[92:95], v[96:99], v[188:191], v[92:95]\n\t"
        "s_waitcnt lgkmcnt(9)\n\t"
        "ds_read_b128 v[220:223], %[bp] offset:448\n\t"
        "v_mfma_f32_16x16x32_bf16 v[64:67], v[128:131], v[192:195], v[64:67]\n\t"
        "ds_read_b64 v[96:97], %[up] offset:3584\n\t"
        "v_mfma_f32_16x16x32_bf16 v[68:71], v[132:135], v[192:195], v[68:71]\n\t"
        "ds_read_b64 v[98:99], %[up] offset:3072\n\t"
        "v_mfma_f32_16x16x32_bf16 v[68:71], v[128:131], v[196:199], v[68:71]\n\t"
        "ds_read_b64 v[100:101], %[up] offset:2560\n\t"
        "v_mfma_f32_16x16x32_bf16 v[72:75], v[128:131], v[200:203], v[72:75]\n\t"
        "ds_read_b64 v[102:103], %[up] offset:2048\n\t"
        "buffer_store_dwordx4 a[0:3], %[vx], %[ws], %[so] offen offset:0 sc1\n\t"
        "v_mfma_f32_16x16x32_bf16 v[72:75], v[132:135], v[196:199], v[72:75]\n\t"
        "ds_read_b64 v[104:105], %[up] offset:1536\n\t"
        "v_mfma_f32_16x16x32_bf16 v[72:75], v[136:139], v[192:195], v[72:75]\n\t"
        "s_waitcnt lgkmcnt(9)\n\t"
        "ds_read_b64 v[106:107], %[up] offset:1024\n\t"
        "v_mfma_f32_16x16x32_bf16 v[76:79], v[140:143], v[192:195], v[76:79]\n\t"
        "ds_read_b64 v[108:109], %[up] offset:512\n\t"
        "v_mfma_f32_16x16x32_bf16 v[76:79], v[136:139], v[196:199], v[76:79]\n\t"
        "ds_read_b64 v[110:111], %[up] offset:0\n\t"
        "buffer_store_dwordx4 a[4:7], %[vx], %[ws], %[so] offen offset:1024 sc1\n\t"
        "v_mfma_f32_16x16x32_bf16 v[76:79], v[132:135], v[200:203], v[76:79]\n\t"
        "v_mfma_f32_16x16x32_bf16 v[76:79], v[128:131], v[204:207], v[76:79]\n\t"
        "v_mfma_f32_16x16x32_bf16 v[80:83], v[128:131], v[208:211], v[80:83]\n\t"
        "v_mfma_f32_16x16x32_bf16 v[80:83], v[132:135], v[204:207], v[80:83]\n\t"
        "buffer_store_dwordx4 a[8:11], %[vx], %[ws], %[so] offen offset:2048 sc1\n\t"
        "v_mfma_f32_16x16x32_bf16 v[80:83], v[136:139], v[200:203], v[80:83]\n\t"
        "v_mfma_f32_16x16x32_bf16 v[80:83], v[140:143], v[196:199], v[80:83]\n\t"
        "v_mfma_f32_16x16x32_bf16 v[80:83], v[144:147], v[192:195], v[80:83]\n\t"
        "v_mfma_f32_16x16x32_bf16 v[84:87], v[148:151], v[192:195], v[84:87]\n\t"
        "buffer_store_dwordx4 a[12:15], %[vx], %[ws], %[so] offen offset:3072 sc1\n\t"
        "v_mfma_f32_16x16x32_bf16 v[84:87], v[144:147], v[196:199], v[84:87]\n\t"
        "v_mfma_f32_16x16x32_bf16 v[84:87], v[140:143], v[200:203], v[84:87]\n\t"
        "v_mfma_f32_16x16x32_bf16 v[84:87], v[136:139], v[204:207], v[84:87]\n\t"
        "v_mfma_f32_16x16x32_bf16 v[84:87], v[132:135], v[208:211], v[84:87]\n\t"
        "buffer_store_dwordx4 a[16:19], %[vx], %[ws], s45 offen offset:0 sc1\n\t"
        "v_mfma_f32_16x16x32_bf16 v[84:87], v[128:131], v[212:215], v[84:87]\n\t"
        "v_mfma_f32_16x16x32_bf16 v[88:91], v[128:131], v[216:219], v[88:91]\n\t"
        "v_mfma_f32_16x16x32_bf16 v[88:91], v[132:135], v[212:215], v[88:91]\n\t"
        "v_mfma_f32_16x16x32_bf16 v[88:91], v[136:139], v[208:211], v[88:91]\n\t"
        "buffer_store_dwordx4 a[20:23], %[vx], %[ws], s45 offen offset:1024 sc1\n\t"
        "v_mfma_f32_16x16x32_bf16 v[88:91], v[140:143], v[204:207], v[88:91]\n\t"
        "v_mfma_f32_16x16x32_bf16 v[88:91], v[144:147], v[200:203], v[88:91]\n\t"
        "v_mfma_f32_16x16x32_bf16 v[88:91], v[148:151], v[196:199], v[88:91]\n\t"
        "v_mfma_f32_16x16x32_bf16 v[88:91], v[152:155], v[192:195], v[88:91]\n\t"
        "buffer_store_dwordx4 a[24:27], %[vx], %[ws], s45 offen offset:2048 sc1\n\t"
        "s_waitcnt lgkmcnt(8)\n\t"
        "v_mfma_f32_16x16x32_bf16 v[92:95], v[156:159], v[192:195], v[92:95]\n\t"
        "v_mfma_f32_16x16x32_bf16 v[92:95], v[152:155], v[196:199], v[92:95]\n\t"
        "v_mfma_f32_16x16x32_bf16 v[92:95], v[148:151], v[200:203], v[92:95]\n\t"
        "v_mfma_f32_16x16x32_bf16 v[92:95], v[144:147], v[204:207], v[92:95]\n\t"
        "buffer_store_dwordx4 a[28:31], %[vx], %[ws], s45 offen offset:3072 sc1\n\t"
        "v_mfma_f32_16x16x32_bf16 v[92:95], v[140:143], v[208:211], v[92:95]\n\t"
        "v_mfma_f32_16x16x32_bf16 v[92:95], v[136:139], v[212:215], v[92:95]\n\t"
        "v_mfma_f32_16x16x32_bf16 v[92:95], v[132:135], v[216:219], v[92:95]\n\t"
        "v_mfma_f32_16x16x32_bf16 v[92:95], v[128:131], v[220:223], v[92:95]\n\t"
        "s_waitcnt lgkmcnt(0)\n\t"
        "s_nop 7\n\t"
        : "=&{v[64:95]}"(acc), "=&{v[96:111]}"(uv)
        : [ap] "v"(ap), [bp] "v"(bp), [up] "v"(up), [vx] "v"(lane * 16), [vd] "v"(vd), [va] "v"(va), [vb] "v"(vb), [vt] "v"(vt), [ws] "s"(ws),
          [srdW1] "s"(srdW1), [srdW2] "s"(srdW2), [sd] "s"(sd), [so] "s"(so), [ld] "s"(ldd)
        : "memory", "scc", "s44", "s45", "v58","v59","v60","v61","v62","v63", "v112","v113","v114","v115","v116","v117","v118","v119","v120","v121","v122","v123","v124","v125","v126","v127","v128","v129","v130","v131","v132","v133","v134","v135","v136","v137","v138","v139","v140","v141","v142","v143","v144","v145","v146","v147","v148","v149","v150","v151","v152","v153","v154","v155","v156","v157","v158","v159","v160","v161","v162","v163","v164","v165","v166","v167","v168","v169","v170","v171","v172","v173","v174","v175","v176","v177","v178","v179","v180","v181","v182","v183","v184","v185","v186","v187","v188","v189","v190","v191","v192","v193","v194","v195","v196","v197","v198","v199","v200","v201","v202","v203","v204","v205","v206","v207","v208","v209","v210","v211","v212","v213","v214","v215","v216","v217","v218","v219","v220","v221","v222","v223", "a0","a1","a2","a3","a4","a5","a6","a7","a8","a9","a10","a11","a12","a13","a14","a15","a16","a17","a18","a19","a20","a21","a22","a23","a24","a25","a26","a27","a28","a29","a30","a31");
#pragma unroll
    for (int n = 0; n < 8; n++)
      *(LAS u32x2*)(lds + CV_Y + w * CV_CP + 2 * (256 * n + 16 * r + 4 * q)) =
          u32x2{pk(acc[4 * n] + dd * bhi(uv[2 * n + 1]), acc[4 * n + 1] + dd * blo(uv[2 * n + 1])),
                pk(acc[4 * n + 2] + dd * bhi(uv[2 * n]), acc[4 * n + 3] + dd * blo(uv[2 * n]))};
    LDSBAR();
    {
      const unsigned ra = CV_Y + ((lane >> 2) & 3) * CV_CP + w * 1024 + q * 64 + (lane & 3) * 16;
      u32x2 o[8];
      asm volatile(
          "ds_read_b64_tr_b16 %0, %8 offset:0\n\tds_read_b64_tr_b16 %1, %8 offset:8\n\tds_read_b64_tr_b16 %2, %8 offset:256\n\tds_read_b64_tr_b16 %3, %8 offset:264\n\t"
          "ds_read_b64_tr_b16 %4, %8 offset:512\n\tds_read_b64_tr_b16 %5, %8 offset:520\n\tds_read_b64_tr_b16 %6, %8 offset:768\n\tds_read_b64_tr_b16 %7, %8 offset:776\n\t"
          "s_waitcnt lgkmcnt(0)"
          : "=&v"(o[0]), "=&v"(o[1]), "=&v"(o[2]), "=&v"(o[3]), "=&v"(o[4]), "=&v"(o[5]), "=&v"(o[6]), "=&v"(o[7]) : "v"(ra) : "memory");
      const int mm = lane >> 3;
      const unsigned yo = (mm >> 2) * 131072 + (mm & 3) * 1024 + (lane & 7) * 16;
#pragma unroll
      for (int it = 0; it < 4; it++)
        st16(u32x4{o[2 * it][0], o[2 * it][1], o[2 * it + 1][0], o[2 * it + 1][1]}, ws, yo,
             WS_Y + (8 * w + 2 * it) * 131072 + (blk >> 3) * 4096 + (blk & 3) * 256 + ((blk >> 2) & 1) * 128);
    }
    WAITVM(0);
    put_flag(ws, F_Y + (blk * 4 + w) * 128, tag, lane);
    unsigned vw[7];
    w1_offsets(vw, lane, rot);
    w1_prefetch<0>(srdW1, vw, w, w0);
    touch(srdW1, vt1);
    float dm, rstd;
    row_stats((LAS f32x2*)(lds + LDS_RED), r8, dm, rstd);
    f32x2 xh[16];
#pragma unroll
    for (int e = 0; e < 16; e++) xh[e] = (xs[e] - dm) * rstd;
    PIN16(xh);
    const unsigned fb = ((m16 >> 2) * 128 + (m16 & 3)) * 1024 + (8 * w + kk) * 4096;
    wait_flags(ws, F_Y + ((w * 64 + lane) * 4 + (m16 >> 5)) * 128, tag);
    u32x2 ya[4], yb[4];
#pragma unroll
    for (int it = 0; it < 4; it++) {
      const unsigned yo = fb + it * 8192 + lq * 256 + (blk & 1) * 64 + (r8 & 3) * 16 + (r8 >> 2) * 8;
      ya[it] = ld8(ws, yo, WS_Y); yb[it] = ld8(ws, yo + 128, WS_Y);
    }
#pragma unroll
    for (int e = 0; e < 16; e++) xs[e] += xh[e] * up2((e & 2) ? yb[e >> 2][e & 1] : ya[e >> 2][e & 1]);
    PIN16(xs);
    w1_prefetch<1>(srdW1, vw, w, w0);
    ln_tail<true>(lds, ws, xs, sh, fb + (lq * 16 + lr) * 16, w, lane);
    WAITVM(0);
    put_flag(ws, F_LN + (blk * 4 + w) * 128, tag, lane);
    touch(srdW1, vt1 + 256 * 7168);
    if (blockIdx.x == 0 && w == 0 && lane == 0) __builtin_amdgcn_raw_buffer_store_b32((unsigned)tag, ws, 0, WS_EPOCH, 16);
  }
  const int m1 = mt * 4 + w, m2 = mt2 * 2 + mh, tile = mt2 * 8 + nt2;
#pragma nounroll
  for (int layer = 0; layer < 2; ++layer) {
    {
      const int lane = lane_id();
      const rsrc_t srdO = mk(out, 4 * MB);
      const unsigned vx = lane * 16;
      const unsigned vp1 = F_LN + layer * 131072 + (m1 * 32 + (lane & 31)) * 128;
      const unsigned vp2 = F_UP + layer * 131072 + (((m2 >> 2) * 32 + c * 16 + kh * 8 + (lane & 7)) * 4 + (m2 & 3)) * 128;
      const unsigned vo = (m2 * 64 + kh * 32 + (lane & 15)) * 2048 + (4 * nt2 + 2 * c) * 64 + (lane >> 4) * 16;
      const unsigned xo = WS_XN + layer * 4 * MB + m1 * 131072, hbase = WS_H + layer * 15 * MB;
      const unsigned hb = hbase + (m1 * 112 + 7 * (nt >> 1)) * 4096, fs1 = F_UP + layer * 131072 + ((mt * 32 + nt) * 4 + w) * 128;
      const unsigned w2 = WS_W2 + (nt2 * 112 + c * 56 + kh * 28 + mh) * 8192, x2 = hbase + (m2 * 112 + c * 56 + kh * 28) * 4096, r2 = kh * 16384 + c * 4096;
      const unsigned e0 = (layer ? WS_X3 : WS_P + c * 4 * MB) + (m2 * 128 + 2 * kh) * 1024 + (4 * nt2 + 2 * c) * 4096;
      const unsigned fs2 = F_DN + layer * 131072 + ((tile * 2 + c) * 4 + w) * 128, xs = WS_XB + ((tile * 2 + c) * 4 + w) * 8192;
      asm volatile(
          "s_nop 4\n\t"
          "s_mov_b32 s44, %[xo]\n\t"
          "s_add_u32 s48, %[xo], 4096\n\t"
          "s_add_u32 s45, %[w0], 917504\n\t"
          "s_mov_b32 s47, 3\n\t"
          "v_lshrrev_b32 v16, 4, %[vx]\n\t"
          "v_add_u32 v18, %[rot], %[vx]\n\t"
          "v_add_u32 v17, 4096, v18\n\t"
          "s_movk_i32 s50, 4682\n\t"
          "s_movk_i32 s51, 6944\n\t"
          "s_lshr_b32 s57, %[rot], 4\n\t"
          "s_sub_u32 s57, 14, s57\n\t"
          "s_mul_i32 s46, %[w], 7168\n\t"
          "s_add_u32 s46, s46, 8192\n\t"
          "v_add_u32 v20, 0, v16\n\t"
          "v_add_u32 v21, 64, v16\n\t"
          "v_add_u32 v22, 128, v16\n\t"
          "v_add_u32 v23, 192, v16\n\t"
          "v_add_u32 v24, 256, v16\n\t"
          "v_add_u32 v25, 320, v16\n\t"
          "v_add_u32 v26, 384, v16\n\t"
          "v_mul_u32_u24 v8, s50, v20\n\t"
          "v_mul_u32_u24 v9, s50, v21\n\t"
          "v_mul_u32_u24 v10, s50, v22\n\t"
          "v_mul_u32_u24 v11, s50, v23\n\t"
          "v_mul_u32_u24 v12, s50, v24\n\t"
          "v_mul_u32_u24 v13, s50, v25\n\t"
          "v_mul_u32_u24 v14, s50, v26\n\t"
          "v_lshrrev_b32 v8, 16, v8\n\t"
          "v_lshrrev_b32 v9, 16, v9\n\t"
          "v_lshrrev_b32 v10, 16, v10\n\t"
          "v_lshrrev_b32 v11, 16, v11\n\t"
          "v_lshrrev_b32 v12, 16, v12\n\t"
          "v_lshrrev_b32 v13, 16, v13\n\t"
          "v_lshrrev_b32 v14, 16, v14\n\t"
          "v_mad_i32_i24 v20, v8, -14, v20\n\t"
          "v_mad_i32_i24 v21, v9, -14, v21\n\t"
          "v_mad_i32_i24 v22, v10, -14, v22\n\t"
          "v_mad_i32_i24 v23, v11, -14, v23\n\t"
          "v_mad_i32_i24 v24, v12, -14, v24\n\t"
          "v_mad_i32_i24 v25, v13, -14, v25\n\t"
          "v_mad_i32_i24 v26, v14, -14, v26\n\t"
          "v_mad_u32_u24 v8, v8, s51, v18\n\t"
          "v_mad_u32_u24 v9, v9, s51, v18\n\t"
          "v_mad_u32_u24 v10, v10, s51, v18\n\t"
          "v_mad_u32_u24 v11, v11, s51, v18\n\t"
          "v_mad_u32_u24 v12, v12, s51, v17\n\t"
          "v_mad_u32_u24 v13, v13, s51, v17\n\t"
          "v_mad_u32_u24 v14, v14, s51, v17\n\t"
          "v_cmp_le_u32 vcc, s57, v20\n\t"
          "v_subrev_u32 v20, 224, v8\n\t"
          "s_nop 0\n\t"
          "v_cndmask_b32 v8, v8, v20, vcc\n\t"
          "v_cmp_le_u32 vcc, s57, v21\n\t"
          "v_subrev_u32 v21, 224, v9\n\t"
          "s_nop 0\n\t"
          "v_cndmask_b32 v9, v9, v21, vcc\n\t"
          "v_cmp_le_u32 vcc, s57, v22\n\t"
          "v_subrev_u32 v22, 224, v10\n\t"
          "s_nop 0\n\t"
          "v_cndmask_b32 v10, v10, v22, vcc\n\t"
          "v_cmp_le_u32 vcc, s57, v23\n\t"
          "v_subrev_u32 v23, 224, v11\n\t"
          "s_nop 0\n\t"
          "v_cndmask_b32 v11, v11, v23, vcc\n\t"
          "v_cmp_le_u32 vcc, s57, v24\n\t"
          "v_subrev_u32 v24, 224, v12\n\t"
          "s_nop 0\n\t"
          "v_cndmask_b32 v12, v12, v24, vcc\n\t"
          "v_cmp_le_u32 vcc, s57, v25\n\t"
          "v_subrev_u32 v25, 224, v13\n\t"
          "s_nop 0\n\t"
          "v_cndmask_b32 v13, v13, v25, vcc\n\t"
          "v_cmp_le_u32 vcc, s57, v26\n\t"
          "v_subrev_u32 v26, 224, v14\n\t"
          "s_nop 0\n\t"
          "v_cndmask_b32 v14, v14, v26, vcc\n\t"
          "v_lshrrev_b32 v16, 6, %[vx]\n\t"
          "v_and_b32 v16, 15, v16\n\t"
          "v_mul_u32_u24 v16, 224, v16\n\t"
          "v_lshrrev_b32 v17, 1, %[vx]\n\t"
          "v_and_b32 v17, 24, v17\n\t"
          "v_add_u32 v6, v16, v17\n\t"
          "v_add_u32 v6, 8192, v6\n\t"
          "v_add_u32 v7, 57344, v6\n\t"
          "v_and_b32 v15, 0x1f0, %[vx]\n\t"
          "v_and_b32 v16, 0x200, %[vx]\n\t"
          "v_lshl_add_u32 v15, v16, 1, v15\n\t"
          "s_waitcnt vmcnt(0)\n\t"
          "s_barrier\n\t"
          "ds_read_b64_tr_b16 a[128:129], v6 offset:0\n\t"
          "ds_read_b64_tr_b16 a[130:131], v6 offset:3584\n\t"
          "ds_read_b64_tr_b16 a[132:133], v6 offset:14336\n\t"
          "ds_read_b64_tr_b16 a[134:135], v6 offset:17920\n\t"
          "ds_read_b64_tr_b16 a[136:137], v6 offset:7168\n\t"
          "ds_read_b64_tr_b16 a[138:139], v6 offset:10752\n\t"
          "ds_read_b64_tr_b16 a[140:141], v6 offset:21504\n\t"
          "ds_read_b64_tr_b16 a[142:143], v6 offset:25088\n\t"
          "s_mov_b32 s58, 262144\n\t"
          "10:\n\t"
          "buffer_load_dwordx2 v[24:25], %[vp1], %[ws], 0 offen sc1\n\t"
          "s_waitcnt vmcnt(0)\n\t"
          "v_cmp_ne_u64 vcc, %[tag], v[24:25]\n\t"
          "s_nop 4\n\t"
          "s_cbranch_vccz 11f\n\t"
          "s_sub_u32 s58, s58, 1\n\t"
          "s_cmp_lg_u32 s58, 0\n\t"
          "s_cbranch_scc1 10b\n\t"
          "11:\n\t"
          "buffer_load_dwordx4 a[0:3], %[vx], %[ws], s44 offen offset:0 sc1\n\t"
          "buffer_load_dwordx4 a[4:7], %[vx], %[ws], s44 offen offset:1024 sc1\n\t"
          "buffer_load_dwordx4 a[8:11], %[vx], %[ws], s44 offen offset:2048 sc1\n\t"
          "buffer_load_dwordx4 a[12:15], %[vx], %[ws], s44 offen offset:3072 sc1\n\t"
          "buffer_load_dwordx4 a[16:19], %[vx], %[ws], s48 offen offset:0 sc1\n\t"
          "buffer_load_dwordx4 a[20:23], %[vx], %[ws], s48 offen offset:1024 sc1\n\t"
          "buffer_load_dwordx4 a[24:27], %[vx], %[ws], s48 offen offset:2048 sc1\n\t"
          "buffer_load_dwordx4 a[28:31], %[vx], %[ws], s48 offen offset:3072 sc1\n\t"
          "s_add_u32 s44, s44, 8192\n\t"
          "s_add_u32 s48, s48, 8192\n\t"
          "s_waitcnt vmcnt(0)\n\t"
          "s_waitcnt lgkmcnt(4)\n\t"
          "v_mfma_f32_16x16x32_bf16 v[32:35], a[128:131], a[0:3], 0\n\t"
          "ds_read_b64_tr_b16 a[144:145], v6 offset:32\n\t"
          "v_mfma_f32_16x16x32_bf16 v[32:35], a[132:135], a[16:19], v[32:35]\n\t"
          "v_mfma_f32_16x16x32_bf16 v[36:39], a[132:135], a[20:23], 0\n\t"
          "ds_read_b64_tr_b16 a[146:147], v6 offset:3616\n\t"
          "buffer_load_dwordx4 a[32:35], %[vx], %[ws], s44 offen offset:0 sc1\n\t"
          "v_mfma_f32_16x16x32_bf16 v[36:39], a[128:131], a[4:7], v[36:39]\n\t"
          "v_mfma_f32_16x16x32_bf16 v[40:43], a[128:131], a[8:11], 0\n\t"
          "ds_read_b64_tr_b16 a[148:149], v6 offset:14368\n\t"
          "v_mfma_f32_16x16x32_bf16 v[40:43], a[132:135], a[24:27], v[40:43]\n\t"
          "v_mfma_f32_16x16x32_bf16 v[44:47], a[132:135], a[28:31], 0\n\t"
          "ds_read_b64_tr_b16 a[150:151], v6 offset:17952\n\t"
          "buffer_load_dwordx4 a[36:39], %[vx], %[ws], s44 offen offset:1024 sc1\n\t"
          "v_mfma_f32_16x16x32_bf16 v[44:47], a[128:131], a[12:15], v[44:47]\n\t"
          "s_waitcnt lgkmcnt(4)\n\t"
          "v_mfma_f32_16x16x32_bf16 v[156:159], a[136:139], a[12:15], 0\n\t"
          "ds_read_b64_tr_b16 a[152:153], v6 offset:7200\n\t"
          "v_mfma_f32_16x16x32_bf16 v[156:159], a[140:143], a[28:31], v[156:159]\n\t"
          "v_mfma_f32_16x16x32_bf16 v[152:155], a[140:143], a[24:27], 0\n\t"
          "ds_read_b64_tr_b16 a[154:155], v6 offset:10784\n\t"
          "v_mfma_f32_16x16x32_bf16 v[152:155], a[136:139], a[8:11], v[152:155]\n\t"
          "buffer_load_dwordx4 a[40:43], %[vx], %[ws], s44 offen offset:2048 sc1\n\t"
          "v_mfma_f32_16x16x32_bf16 v[148:151], a[136:139], a[4:7], 0\n\t"
          "ds_read_b64_tr_b16 a[156:157], v6 offset:21536\n\t"
          "v_mfma_f32_16x16x32_bf16 v[148:151], a[140:143], a[20:23], v[148:151]\n\t"
          "v_mfma_f32_16x16x32_bf16 v[144:147], a[140:143], a[16:19], 0\n\t"
          "ds_read_b64_tr_b16 a[158:159], v6 offset:25120\n\t"
          "v_mfma_f32_16x16x32_bf16 v[144:147], a[136:139], a[0:3], v[144:147]\n\t"
          "buffer_load_dwordx4 a[44:47], %[vx], %[ws], s44 offen offset:3072 sc1\n\t"
          "s_waitcnt lgkmcnt(4)\n\t"
          "v_mfma_f32_16x16x32_bf16 v[48:51], a[144:147], a[0:3], 0\n\t"
          "ds_read_b64_tr_b16 a[128:129], v6 offset:64\n\t"
          "v_mfma_f32_16x16x32_bf16 v[48:51], a[148:151], a[16:19], v[48:51]\n\t"
          "v_mfma_f32_16x16x32_bf16 v[52:55], a[148:151], a[20:23], 0\n\t"
          "ds_read_b64_tr_b16 a[130:131], v6 offset:3648\n\t"
          "v_mfma_f32_16x16x32_bf16 v[52:55], a[144:147], a[4:7], v[52:55]\n\t"
          "v_mfma_f32_16x16x32_bf16 v[56:59], a[144:147], a[8:11], 0\n\t"
          "ds_read_b64_tr_b16 a[132:133], v6 offset:14400\n\t"
          "buffer_load_dwordx4 a[48:51], %[vx], %[ws], s48 offen offset:0 sc1\n\t"
          "v_mfma_f32_16x16x32_bf16 v[56:59], a[148:151], a[24:27], v[56:59]\n\t"
          "v_mfma_f32_16x16x32_bf16 v[60:63], a[148:151], a[28:31], 0\n\t"
          "ds_read_b64_tr_b16 a[134:135], v6 offset:17984\n\t"
          "v_mfma_f32_16x16x32_bf16 v[60:63], a[144:147], a[12:15], v[60:63]\n\t"
          "s_waitcnt lgkmcnt(4)\n\t"
          "v_mfma_f32_16x16x32_bf16 v[172:175], a[152:155], a[12:15], 0\n\t"
          "ds_read_b64_tr_b16 a[136:137], v6 offset:7232\n\t"
          "buffer_load_dwordx4 a[52:55], %[vx], %[ws], s48 offen offset:1024 sc1\n\t"
          "v_mfma_f32_16x16x32_bf16 v[172:175], a[156:159], a[28:31], v[172:175]\n\t"
          "v_mfma_f32_16x16x32_bf16 v[168:171], a[156:159], a[24:27], 0\n\t"
          "ds_read_b64_tr_b16 a[138:139], v6 offset:10816\n\t"
          "v_mfma_f32_16x16x32_bf16 v[168:171], a[152:155], a[8:11], v[168:171]\n\t"
          "v_mfma_f32_16x16x32_bf16 v[164:167], a[152:155], a[4:7], 0\n\t"
          "ds_read_b64_tr_b16 a[140:141], v6 offset:21568\n\t"
          "v_mfma_f32_16x16x32_bf16 v[164:167], a[156:159], a[20:23], v[164:167]\n\t"
          "buffer_load_dwordx4 a[56:59], %[vx], %[ws], s48 offen offset:2048 sc1\n\t"
          "v_mfma_f32_16x16x32_bf16 v[160:163], a[156:159], a[16:19], 0\n\t"
          "ds_read_b64_tr_b16 a[142:143], v6 offset:25152\n\t"
          "v_mfma_f32_16x16x32_bf16 v[160:163], a[152:155], a[0:3], v[160:163]\n\t"
          "s_waitcnt lgkmcnt(4)\n\t"
          "v_mfma_f32_16x16x32_bf16 v[64:67], a[128:131], a[0:3], 0\n\t"
          "ds_read_b64_tr_b16 a[144:145], v6 offset:96\n\t"
          "v_mfma_f32_16x16x32_bf16 v[64:67], a[132:135], a[16:19], v[64:67]\n\t"
          "buffer_load_dwordx4 a[60:63], %[vx], %[ws], s48 offen offset:3072 sc1\n\t"
          "s_add_u32 s44, s44, 8192\n\t"
          "s_add_u32 s48, s48, 8192\n\t"
          "v_mfma_f32_16x16x32_bf16 v[68:71], a[132:135], a[20:23], 0\n\t"
          "ds_read_b64_tr_b16 a[146:147], v6 offset:3680\n\t"
          "v_mfma_f32_16x16x32_bf16 v[68:71], a[128:131], a[4:7], v[68:71]\n\t"
          "v_mfma_f32_16x16x32_bf16 v[72:75], a[128:131], a[8:11], 0\n\t"
          "ds_read_b64_tr_b16 a[148:149], v6 offset:14432\n\t"
          "v_mfma_f32_16x16x32_bf16 v[72:75], a[132:135], a[24:27], v[72:75]\n\t"
          "s_add_u32 m0, s46, 57344\n\t"
          "v_mfma_f32_16x16x32_bf16 v[76:79], a[132:135], a[28:31], 0\n\t"
          "ds_read_b64_tr_b16 a[150:151], v6 offset:18016\n\t"
          "buffer_load_dwordx4 v8, %[srdW1], s45 offen offset:0 lds\n\t"
          "v_mfma_f32_16x16x32_bf16 v[76:79], a[128:131], a[12:15], v[76:79]\n\t"
          "s_waitcnt lgkmcnt(4)\n\t"
          "v_mfma_f32_16x16x32_bf16 v[188:191], a[136:139], a[12:15], 0\n\t"
          "ds_read_b64_tr_b16 a[152:153], v6 offset:7264\n\t"
          "v_mfma_f32_16x16x32_bf16 v[188:191], a[140:143], a[28:31], v[188:191]\n\t"
          "v_mfma_f32_16x16x32_bf16 v[184:187], a[140:143], a[24:27], 0\n\t"
          "ds_read_b64_tr_b16 a[154:155], v6 offset:10848\n\t"
          "buffer_load_dwordx4 a[64:67], %[vx], %[ws], s44 offen offset:0 sc1\n\t"
          "v_mfma_f32_16x16x32_bf16 v[184:187], a[136:139], a[8:11], v[184:187]\n\t"
          "v_mfma_f32_16x16x32_bf16 v[180:183], a[136:139], a[4:7], 0\n\t"
          "ds_read_b64_tr_b16 a[156:157], v6 offset:21600\n\t"
          "v_mfma_f32_16x16x32_bf16 v[180:183], a[140:143], a[20:23], v[180:183]\n\t"
          "v_mfma_f32_16x16x32_bf16 v[176:179], a[140:143], a[16:19], 0\n\t"
          "ds_read_b64_tr_b16 a[158:159], v6 offset:25184\n\t"
          "v_mfma_f32_16x16x32_bf16 v[176:179], a[136:139], a[0:3], v[176:179]\n\t"
          "buffer_load_dwordx4 v9, %[srdW1], s45 offen offset:1024 lds\n\t"
          "s_waitcnt lgkmcnt(4)\n\t"
          "v_mfma_f32_16x16x32_bf16 v[80:83], a[144:147], a[0:3], 0\n\t"
          "ds_read_b64_tr_b16 a[128:129], v6 offset:128\n\t"
          "v_mfma_f32_16x16x32_bf16 v[80:83], a[148:151], a[16:19], v[80:83]\n\t"
          "v_mfma_f32_16x16x32_bf16 v[84:87], a[148:151], a[20:23], 0\n\t"
          "ds_read_b64_tr_b16 a[130:131], v6 offset:3712\n\t"
          "v_mfma_f32_16x16x32_bf16 v[84:87], a[144:147], a[4:7], v[84:87]\n\t"
          "buffer_load_dwordx4 a[68:71], %[vx], %[ws], s44 offen offset:1024 sc1\n\t"
          "v_mfma_f32_16x16x32_bf16 v[88:91], a[144:147], a[8:11], 0\n\t"
          "ds_read_b64_tr_b16 a[132:133], v6 offset:14464\n\t"
          "v_mfma_f32_16x16x32_bf16 v[88:91], a[148:151], a[24:27], v[88:91]\n\t"
          "v_mfma_f32_16x16x32_bf16 v[92:95], a[148:151], a[28:31], 0\n\t"
          "ds_read_b64_tr_b16 a[134:135], v6 offset:18048\n\t"
          "v_mfma_f32_16x16x32_bf16 v[92:95], a[144:147], a[12:15], v[92:95]\n\t"
          "s_waitcnt lgkmcnt(4)\n\t"
          "v_mfma_f32_16x16x32_bf16 v[204:207], a[152:155], a[12:15], 0\n\t"
          "ds_read_b64_tr_b16 a[136:137], v6 offset:7296\n\t"
          "buffer_load_dwordx4 v10, %[srdW1], s45 offen offset:2048 lds\n\t"
          "v_mfma_f32_16x16x32_bf16 v[204:207], a[156:159], a[28:31], v[204:207]\n\t"
          "v_mfma_f32_16x16x32_bf16 v[200:203], a[156:159], a[24:27], 0\n\t"
          "ds_read_b64_tr_b16 a[138:139], v6 offset:10880\n\t"
          "v_mfma_f32_16x16x32_bf16 v[200:203], a[152:155], a[8:11], v[200:203]\n\t"
          "v_mfma_f32_16x16x32_bf16 v[196:199], a[152:155], a[4:7], 0\n\t"
          "ds_read_b64_tr_b16 a[140:141], v6 offset:21632\n\t"
          "buffer_load_dwordx4 a[72:75], %[vx], %[ws], s44 offen offset:2048 sc1\n\t"
          "v_mfma_f32_16x16x32_bf16 v[196:199], a[156:159], a[20:23], v[196:199]\n\t"
          "v_mfma_f32_16x16x32_bf16 v[192:195], a[156:159], a[16:19], 0\n\t"
          "ds_read_b64_tr_b16 a[142:143], v6 offset:25216\n\t"
          "v_mfma_f32_16x16x32_bf16 v[192:195], a[152:155], a[0:3], v[192:195]\n\t"
          "s_waitcnt lgkmcnt(4)\n\t"
          "v_mfma_f32_16x16x32_bf16 v[96:99], a[128:131], a[0:3], 0\n\t"
          "ds_read_b64_tr_b16 a[144:145], v6 offset:160\n\t"
          "v_mfma_f32_16x16x32_bf16 v[96:99], a[132:135], a[16:19], v[96:99]\n\t"
          "buffer_load_dwordx4 v11, %[srdW1], s45 offen offset:3072 lds\n\t"
          "v_mfma_f32_16x16x32_bf16 v[100:103], a[132:135], a[20:23], 0\n\t"
          "ds_read_b64_tr_b16 a[146:147], v6 offset:3744\n\t"
          "v_mfma_f32_16x16x32_bf16 v[100:103], a[128:131], a[4:7], v[100:103]\n\t"
          "v_mfma_f32_16x16x32_bf16 v[104:107], a[128:131], a[8:11], 0\n\t"
          "ds_read_b64_tr_b16 a[148:149], v6 offset:14496\n\t"
          "v_mfma_f32_16x16x32_bf16 v[104:107], a[132:135], a[24:27], v[104:107]\n\t"
          "buffer_load_dwordx4 a[76:79], %[vx], %[ws], s44 offen offset:3072 sc1\n\t"
          "v_mfma_f32_16x16x32_bf16 v[108:111], a[132:135], a[28:31], 0\n\t"
          "ds_read_b64_tr_b16 a[150:151], v6 offset:18080\n\t"
          "v_mfma_f32_16x16x32_bf16 v[108:111], a[128:131], a[12:15], v[108:111]\n\t"
          "s_waitcnt lgkmcnt(4)\n\t"
          "v_mfma_f32_16x16x32_bf16 v[220:223], a[136:139], a[12:15], 0\n\t"
          "ds_read_b64_tr_b16 a[152:153], v6 offset:7328\n\t"
          "v_mfma_f32_16x16x32_bf16 v[220:223], a[140:143], a[28:31], v[220:223]\n\t"
          "s_add_u32 m0, s46, 61440\n\t"
          "v_mfma_f32_16x16x32_bf16 v[216:219], a[140:143], a[24:27], 0\n\t"
          "ds_read_b64_tr_b16 a[154:155], v6 offset:10912\n\t"
          "buffer_load_dwordx4 v12, %[srdW1], s45 offen offset:0 lds\n\t"
          "v_mfma_f32_16x16x32_bf16 v[216:219], a[136:139], a[8:11], v[216:219]\n\t"
          "v_mfma_f32_16x16x32_bf16 v[212:215], a[136:139], a[4:7], 0\n\t"
          "ds_read_b64_tr_b16 a[156:157], v6 offset:21664\n\t"
          "v_mfma_f32_16x16x32_bf16 v[212:215], a[140:143], a[20:23], v[212:215]\n\t"
          "v_mfma_f32_16x16x32_bf16 v[208:211], a[140:143], a[16:19], 0\n\t"
          "ds_read_b64_tr_b16 a[158:159], v6 offset:25248\n\t"
          "buffer_load_dwordx4 a[80:83], %[vx], %[ws], s48 offen offset:0 sc1\n\t"
          "v_mfma_f32_16x16x32_bf16 v[208:211], a[136:139], a[0:3], v[208:211]\n\t"
          "s_waitcnt lgkmcnt(4)\n\t"
          "v_mfma_f32_16x16x32_bf16 v[112:115], a[144:147], a[0:3], 0\n\t"
          "ds_read_b64_tr_b16 a[128:129], v6 offset:192\n\t"
          "v_mfma_f32_16x16x32_bf16 v[112:115], a[148:151], a[16:19], v[112:115]\n\t"
          "v_mfma_f32_16x16x32_bf16 v[116:119], a[148:151], a[20:23], 0\n\t"
          "ds_read_b64_tr_b16 a[130:131], v6 offset:3776\n\t"
          "v_mfma_f32_16x16x32_bf16 v[116:119], a[144:147], a[4:7], v[116:119]\n\t"
          "buffer_load_dwordx4 v13, %[srdW1], s45 offen offset:1024 lds\n\t"
          "v_mfma_f32_16x16x32_bf16 v[120:123], a[144:147], a[8:11], 0\n\t"
          "ds_read_b64_tr_b16 a[132:133], v6 offset:14528\n\t"
          "v_mfma_f32_16x16x32_bf16 v[120:123], a[148:151], a[24:27], v[120:123]\n\t"
          "v_mfma_f32_16x16x32_bf16 v[124:127], a[148:151], a[28:31], 0\n\t"
          "ds_read_b64_tr_b16 a[134:135], v6 offset:18112\n\t"
          "v_mfma_f32_16x16x32_bf16 v[124:127], a[144:147], a[12:15], v[124:127]\n\t"
          "buffer_load_dwordx4 a[84:87], %[vx], %[ws], s48 offen offset:1024 sc1\n\t"
          "s_waitcnt lgkmcnt(4)\n\t"
          "v_mfma_f32_16x16x32_bf16 v[236:239], a[152:155], a[12:15], 0\n\t"
          "ds_read_b64_tr_b16 a[136:137], v6 offset:7360\n\t"
          "v_mfma_f32_16x16x32_bf16 v[236:239], a[156:159], a[28:31], v[236:239]\n\t"
          "v_mfma_f32_16x16x32_bf16 v[232:235], a[156:159], a[24:27], 0\n\t"
          "ds_read_b64_tr_b16 a[138:139], v6 offset:10944\n\t"
          "v_mfma_f32_16x16x32_bf16 v[232:235], a[152:155], a[8:11], v[232:235]\n\t"
          "v_mfma_f32_16x16x32_bf16 v[228:231], a[152:155], a[4:7], 0\n\t"
          "ds_read_b64_tr_b16 a[140:141], v6 offset:21696\n\t"
          "buffer_load_dwordx4 v14, %[srdW1], s45 offen offset:2048 lds\n\t"
          "s_add_u32 s45, s45, 458752\n\t"
          "v_mfma_f32_16x16x32_bf16 v[228:231], a[156:159], a[20:23], v[228:231]\n\t"
          "v_mfma_f32_16x16x32_bf16 v[224:227], a[156:159], a[16:19], 0\n\t"
          "ds_read_b64_tr_b16 a[142:143], v6 offset:25280\n\t"
          "v_mfma_f32_16x16x32_bf16 v[224:227], a[152:155], a[0:3], v[224:227]\n\t"
          "s_barrier\n\t"
          "s_waitcnt lgkmcnt(4)\n\t"
          "v_mfma_f32_16x16x32_bf16 v[128:131], a[128:131], a[0:3], 0\n\t"
          "ds_read_b64_tr_b16 a[144:145], v6 offset:28672\n\t"
          "buffer_load_dwordx4 a[88:91], %[vx], %[ws], s48 offen offset:2048 sc1\n\t"
          "v_mfma_f32_16x16x32_bf16 v[128:131], a[132:135], a[16:19], v[128:131]\n\t"
          "v_mfma_f32_16x16x32_bf16 v[132:135], a[132:135], a[20:23], 0\n\t"
          "ds_read_b64_tr_b16 a[146:147], v6 offset:32256\n\t"
          "v_mfma_f32_16x16x32_bf16 v[132:135], a[128:131], a[4:7], v[132:135]\n\t"
          "v_mfma_f32_16x16x32_bf16 v[136:139], a[128:131], a[8:11], 0\n\t"
          "ds_read_b64_tr_b16 a[148:149], v6 offset:43008\n\t"
          "v_mfma_f32_16x16x32_bf16 v[136:139], a[132:135], a[24:27], v[136:139]\n\t"
          "buffer_load_dwordx4 a[92:95], %[vx], %[ws], s48 offen offset:3072 sc1\n\t"
          "s_add_u32 s44, s44, 8192\n\t"
          "s_add_u32 s48, s48, 8192\n\t"
          "v_mfma_f32_16x16x32_bf16 v[140:143], a[132:135], a[28:31], 0\n\t"
          "ds_read_b64_tr_b16 a[150:151], v6 offset:46592\n\t"
          "v_mfma_f32_16x16x32_bf16 v[140:143], a[128:131], a[12:15], v[140:143]\n\t"
          "s_waitcnt lgkmcnt(4)\n\t"
          "v_mfma_f32_16x16x32_bf16 v[252:255], a[136:139], a[12:15], 0\n\t"
          "ds_read_b64_tr_b16 a[152:153], v6 offset:35840\n\t"
          "v_mfma_f32_16x16x32_bf16 v[252:255], a[140:143], a[28:31], v[252:255]\n\t"
          "v_mfma_f32_16x16x32_bf16 v[248:251], a[140:143], a[24:27], 0\n\t"
          "ds_read_b64_tr_b16 a[154:155], v6 offset:39424\n\t"
          "v_mfma_f32_16x16x32_bf16 v[248:251], a[136:139], a[8:11], v[248:251]\n\t"
          "v_mfma_f32_16x16x32_bf16 v[244:247], a[136:139], a[4:7], 0\n\t"
          "ds_read_b64_tr_b16 a[156:157], v6 offset:50176\n\t"
          "v_mfma_f32_16x16x32_bf16 v[244:247], a[140:143], a[20:23], v[244:247]\n\t"
          "v_mfma_f32_16x16x32_bf16 v[240:243], a[140:143], a[16:19], 0\n\t"
          "ds_read_b64_tr_b16 a[158:159], v6 offset:53760\n\t"
          "v_mfma_f32_16x16x32_bf16 v[240:243], a[136:139], a[0:3], v[240:243]\n\t"
          "s_waitcnt vmcnt(15)\n\t"
          "s_waitcnt lgkmcnt(4)\n\t"
          "v_mfma_f32_16x16x32_bf16 v[32:35], a[144:147], a[32:35], v[32:35]\n\t"
          "ds_read_b64_tr_b16 a[128:129], v6 offset:28704\n\t"
          "v_mfma_f32_16x16x32_bf16 v[32:35], a[148:151], a[48:51], v[32:35]\n\t"
          "s_add_u32 m0, s46, 86016\n\t"
          "v_mfma_f32_16x16x32_bf16 v[36:39], a[148:151], a[52:55], v[36:39]\n\t"
          "ds_read_b64_tr_b16 a[130:131], v6 offset:32288\n\t"
          "buffer_load_dwordx4 v8, %[srdW1], s45 offen offset:0 lds\n\t"
          "v_mfma_f32_16x16x32_bf16 v[36:39], a[144:147], a[36:39], v[36:39]\n\t"
          "v_mfma_f32_16x16x32_bf16 v[40:43], a[144:147], a[40:43], v[40:43]\n\t"
          "ds_read_b64_tr_b16 a[132:133], v6 offset:43040\n\t"
          "v_mfma_f32_16x16x32_bf16 v[40:43], a[148:151], a[56:59], v[40:43]\n\t"
          "v_mfma_f32_16x16x32_bf16 v[44:47], a[148:151], a[60:63], v[44:47]\n\t"
          "ds_read_b64_tr_b16 a[134:135], v6 offset:46624\n\t"
          "v_mfma_f32_16x16x32_bf16 v[44:47], a[144:147], a[44:47], v[44:47]\n\t"
          "s_waitcnt lgkmcnt(4)\n\t"
          "v_mfma_f32_16x16x32_bf16 v[156:159], a[152:155], a[44:47], v[156:159]\n\t"
          "ds_read_b64_tr_b16 a[136:137], v6 offset:35872\n\t"
          "buffer_load_dwordx4 a[96:99], %[vx], %[ws], s44 offen offset:0 sc1\n\t"
          "v_mfma_f32_16x16x32_bf16 v[156:159], a[156:159], a[60:63], v[156:159]\n\t"
          "v_mfma_f32_16x16x32_bf16 v[152:155], a[156:159], a[56:59], v[152:155]\n\t"
          "ds_read_b64_tr_b16 a[138:139], v6 offset:39456\n\t"
          "v_mfma_f32_16x16x32_bf16 v[152:155], a[152:155], a[40:43], v[152:155]\n\t"
          "v_mfma_f32_16x16x32_bf16 v[148:151], a[152:155], a[36:39], v[148:151]\n\t"
          "ds_read_b64_tr_b16 a[140:141], v6 offset:50208\n\t"
          "v_mfma_f32_16x16x32_bf16 v[148:151], a[156:159], a[52:55], v[148:151]\n\t"
          "v_mfma_f32_16x16x32_bf16 v[144:147], a[156:159], a[48:51], v[144:147]\n\t"
          "ds_read_b64_tr_b16 a[142:143], v6 offset:53792\n\t"
          "v_mfma_f32_16x16x32_bf16 v[144:147], a[152:155], a[32:35], v[144:147]\n\t"
          "buffer_load_dwordx4 v9, %[srdW1], s45 offen offset:1024 lds\n\t"
          "s_waitcnt lgkmcnt(4)\n\t"
          "v_mfma_f32_16x16x32_bf16 v[48:51], a[128:131], a[32:35], v[48:51]\n\t"
          "ds_read_b64_tr_b16 a[144:145], v6 offset:28736\n\t"
          "v_mfma_f32_16x16x32_bf16 v[48:51], a[132:135], a[48:51], v[48:51]\n\t"
          "v_mfma_f32_16x16x32_bf16 v[52:55], a[132:135], a[52:55], v[52:55]\n\t"
          "ds_read_b64_tr_b16 a[146:147], v6 offset:32320\n\t"
          "v_mfma_f32_16x16x32_bf16 v[52:55], a[128:131], a[36:39], v[52:55]\n\t"
          "v_mfma_f32_16x16x32_bf16 v[56:59], a[128:131], a[40:43], v[56:59]\n\t"
          "ds_read_b64_tr_b16 a[148:149], v6 offset:43072\n\t"
          "v_mfma_f32_16x16x32_bf16 v[56:59], a[132:135], a[56:59], v[56:59]\n\t"
          "v_mfma_f32_16x16x32_bf16 v[60:63], a[132:135], a[60:63], v[60:63]\n\t"
          "ds_read_b64_tr_b16 a[150:151], v6 offset:46656\n\t"
          "buffer_load_dwordx4 a[100:103], %[vx], %[ws], s44 offen offset:1024 sc1\n\t"
          "v_mfma_f32_16x16x32_bf16 v[60:63], a[128:131], a[44:47], v[60:63]\n\t"
          "s_waitcnt lgkmcnt(4)\n\t"
          "v_mfma_f32_16x16x32_bf16 v[172:175], a[136:139], a[44:47], v[172:175]\n\t"
          "ds_read_b64_tr_b16 a[152:153], v6 offset:35904\n\t"
          "v_mfma_f32_16x16x32_bf16 v[172:175], a[140:143], a[60:63], v[172:175]\n\t"
          "v_mfma_f32_16x16x32_bf16 v[168:171], a[140:143], a[56:59], v[168:171]\n\t"
          "ds_read_b64_tr_b16 a[154:155], v6 offset:39488\n\t"
          "v_mfma_f32_16x16x32_bf16 v[168:171], a[136:139], a[40:43], v[168:171]\n\t"
          "v_mfma_f32_16x16x32_bf16 v[164:167], a[136:139], a[36:39], v[164:167]\n\t"
          "ds_read_b64_tr_b16 a[156:157], v6 offset:50240\n\t"
          "v_mfma_f32_16x16x32_bf16 v[164:167], a[140:143], a[52:55], v[164:167]\n\t"
          "buffer_load_dwordx4 v10, %[srdW1], s45 offen offset:2048 lds\n\t"
          "v_mfma_f32_16x16x32_bf16 v[160:163], a[140:143], a[48:51], v[160:163]\n\t"
          "ds_read_b64_tr_b16 a[158:159], v6 offset:53824\n\t"
          "v_mfma_f32_16x16x32_bf16 v[160:163], a[136:139], a[32:35], v[160:163]\n\t"
          "s_waitcnt lgkmcnt(4)\n\t"
          "v_mfma_f32_16x16x32_bf16 v[64:67], a[144:147], a[32:35], v[64:67]\n\t"
          "ds_read_b64_tr_b16 a[128:129], v6 offset:28768\n\t"
          "v_mfma_f32_16x16x32_bf16 v[64:67], a[148:151], a[48:51], v[64:67]\n\t"
          "v_mfma_f32_16x16x32_bf16 v[68:71], a[148:151], a[52:55], v[68:71]\n\t"
          "ds_read_b64_tr_b16 a[130:131], v6 offset:32352\n\t"
          "v_mfma_f32_16x16x32_bf16 v[68:71], a[144:147], a[36:39], v[68:71]\n\t"
          "v_mfma_f32_16x16x32_bf16 v[72:75], a[144:147], a[40:43], v[72:75]\n\t"
          "ds_read_b64_tr_b16 a[132:133], v6 offset:43104\n\t"
          "buffer_load_dwordx4 a[104:107], %[vx], %[ws], s44 offen offset:2048 sc1\n\t"
          "v_mfma_f32_16x16x32_bf16 v[72:75], a[148:151], a[56:59], v[72:75]\n\t"
          "v_mfma_f32_16x16x32_bf16 v[76:79], a[148:151], a[60:63], v[76:79]\n\t"
          "ds_read_b64_tr_b16 a[134:135], v6 offset:46688\n\t"
          "v_mfma_f32_16x16x32_bf16 v[76:79], a[144:147], a[44:47], v[76:79]\n\t"
          "s_waitcnt lgkmcnt(4)\n\t"
          "v_mfma_f32_16x16x32_bf16 v[188:191], a[152:155], a[44:47], v[188:191]\n\t"
          "ds_read_b64_tr_b16 a[136:137], v6 offset:35936\n\t"
          "v_mfma_f32_16x16x32_bf16 v[188:191], a[156:159], a[60:63], v[188:191]\n\t"
          "v_mfma_f32_16x16x32_bf16 v[184:187], a[156:159], a[56:59], v[184:187]\n\t"
          "ds_read_b64_tr_b16 a[138:139], v6 offset:39520\n\t"
          "v_mfma_f32_16x16x32_bf16 v[184:187], a[152:155], a[40:43], v[184:187]\n\t"
          "buffer_load_dwordx4 v11, %[srdW1], s45 offen offset:3072 lds\n\t"
          "v_mfma_f32_16x16x32_bf16 v[180:183], a[152:155], a[36:39], v[180:183]\n\t"
          "ds_read_b64_tr_b16 a[140:141], v6 offset:50272\n\t"
          "v_mfma_f32_16x16x32_bf16 v[180:183], a[156:159], a[52:55], v[180:183]\n\t"
          "v_mfma_f32_16x16x32_bf16 v[176:179], a[156:159], a[48:51], v[176:179]\n\t"
          "ds_read_b64_tr_b16 a[142:143], v6 offset:53856\n\t"
          "v_mfma_f32_16x16x32_bf16 v[176:179], a[152:155], a[32:35], v[176:179]\n\t"
          "s_waitcnt lgkmcnt(4)\n\t"
          "v_mfma_f32_16x16x32_bf16 v[80:83], a[128:131], a[32:35], v[80:83]\n\t"
          "ds_read_b64_tr_b16 a[144:145], v6 offset:28800\n\t"
          "v_mfma_f32_16x16x32_bf16 v[80:83], a[132:135], a[48:51], v[80:83]\n\t"
          "v_mfma_f32_16x16x32_bf16 v[84:87], a[132:135], a[52:55], v[84:87]\n\t"
          "ds_read_b64_tr_b16 a[146:147], v6 offset:32384\n\t"
          "buffer_load_dwordx4 a[108:111], %[vx], %[ws], s44 offen offset:3072 sc1\n\t"
          "v_mfma_f32_16x16x32_bf16 v[84:87], a[128:131], a[36:39], v[84:87]\n\t"
          "v_mfma_f32_16x16x32_bf16 v[88:91], a[128:131], a[40:43], v[88:91]\n\t"
          "ds_read_b64_tr_b16 a[148:149], v6 offset:43136\n\t"
          "v_mfma_f32_16x16x32_bf16 v[88:91], a[132:135], a[56:59], v[88:91]\n\t"
          "v_mfma_f32_16x16x32_bf16 v[92:95], a[132:135], a[60:63], v[92:95]\n\t"
          "ds_read_b64_tr_b16 a[150:151], v6 offset:46720\n\t"
          "v_mfma_f32_16x16x32_bf16 v[92:95], a[128:131], a[44:47], v[92:95]\n\t"
          "s_waitcnt lgkmcnt(4)\n\t"
          "v_mfma_f32_16x16x32_bf16 v[204:207], a[136:139], a[44:47], v[204:207]\n\t"
          "ds_read_b64_tr_b16 a[152:153], v6 offset:35968\n\t"
          "s_add_u32 m0, s46, 90112\n\t"
          "v_mfma_f32_16x16x32_bf16 v[204:207], a[140:143], a[60:63], v[204:207]\n\t"
          "buffer_load_dwordx4 v12, %[srdW1], s45 offen offset:0 lds\n\t"
          "v_mfma_f32_16x16x32_bf16 v[200:203], a[140:143], a[56:59], v[200:203]\n\t"
          "ds_read_b64_tr_b16 a[154:155], v6 offset:39552\n\t"
          "v_mfma_f32_16x16x32_bf16 v[200:203], a[136:139], a[40:43], v[200:203]\n\t"
          "v_mfma_f32_16x16x32_bf16 v[196:199], a[136:139], a[36:39], v[196:199]\n\t"
          "ds_read_b64_tr_b16 a[156:157], v6 offset:50304\n\t"
          "v_mfma_f32_16x16x32_bf16 v[196:199], a[140:143], a[52:55], v[196:199]\n\t"
          "v_mfma_f32_16x16x32_bf16 v[192:195], a[140:143], a[48:51], v[192:195]\n\t"
          "ds_read_b64_tr_b16 a[158:159], v6 offset:53888\n\t"
          "v_mfma_f32_16x16x32_bf16 v[192:195], a[136:139], a[32:35], v[192:195]\n\t"
          "s_waitcnt lgkmcnt(4)\n\t"
          "v_mfma_f32_16x16x32_bf16 v[96:99], a[144:147], a[32:35], v[96:99]\n\t"
          "ds_read_b64_tr_b16 a[128:129], v6 offset:28832\n\t"
          "buffer_load_dwordx4 a[112:115], %[vx], %[ws], s48 offen offset:0 sc1\n\t"
          "v_mfma_f32_16x16x32_bf16 v[96:99], a[148:151], a[48:51], v[96:99]\n\t"
          "v_mfma_f32_16x16x32_bf16 v[100:103], a[148:151], a[52:55], v[100:103]\n\t"
          "ds_read_b64_tr_b16 a[130:131], v6 offset:32416\n\t"
          "v_mfma_f32_16x16x32_bf16 v[100:103], a[144:147], a[36:39], v[100:103]\n\t"
          "v_mfma_f32_16x16x32_bf16 v[104:107], a[144:147], a[40:43], v[104:107]\n\t"
          "ds_read_b64_tr_b16 a[132:133], v6 offset:43168\n\t"
          "v_mfma_f32_16x16x32_bf16 v[104:107], a[148:151], a[56:59], v[104:107]\n\t"
          "v_mfma_f32_16x16x32_bf16 v[108:111], a[148:151], a[60:63], v[108:111]\n\t"
          "ds_read_b64_tr_b16 a[134:135], v6 offset:46752\n\t"
          "v_mfma_f32_16x16x32_bf16 v[108:111], a[144:147], a[44:47], v[108:111]\n\t"
          "buffer_load_dwordx4 v13, %[srdW1], s45 offen offset:1024 lds\n\t"
          "s_waitcnt lgkmcnt(4)\n\t"
          "v_mfma_f32_16x16x32_bf16 v[220:223], a[152:155], a[44:47], v[220:223]\n\t"
          "ds_read_b64_tr_b16 a[136:137], v6 offset:36000\n\t"
          "v_mfma_f32_16x16x32_bf16 v[220:223], a[156:159], a[60:63], v[220:223]\n\t"
          "v_mfma_f32_16x16x32_bf16 v[216:219], a[156:159], a[56:59], v[216:219]\n\t"
          "ds_read_b64_tr_b16 a[138:139], v6 offset:39584\n\t"
          "v_mfma_f32_16x16x32_bf16 v[216:219], a[152:155], a[40:43], v[216:219]\n\t"
          "v_mfma_f32_16x16x32_bf16 v[212:215], a[152:155], a[36:39], v[212:215]\n\t"
          "ds_read_b64_tr_b16 a[140:141], v6 offset:50336\n\t"
          "v_mfma_f32_16x16x32_bf16 v[212:215], a[156:159], a[52:55], v[212:215]\n\t"
          "v_mfma_f32_16x16x32_bf16 v[208:211], a[156:159], a[48:51], v[208:211]\n\t"
          "ds_read_b64_tr_b16 a[142:143], v6 offset:53920\n\t"
          "buffer_load_dwordx4 a[116:119], %[vx], %[ws], s48 offen offset:1024 sc1\n\t"
          "v_mfma_f32_16x16x32_bf16 v[208:211], a[152:155], a[32:35], v[208:211]\n\t"
          "s_waitcnt lgkmcnt(4)\n\t"
          "v_mfma_f32_16x16x32_bf16 v[112:115], a[128:131], a[32:35], v[112:115]\n\t"
          "ds_read_b64_tr_b16 a[144:145], v6 offset:28864\n\t"
          "v_mfma_f32_16x16x32_bf16 v[112:115], a[132:135], a[48:51], v[112:115]\n\t"
          "v_mfma_f32_16x16x32_bf16 v[116:119], a[132:135], a[52:55], v[116:119]\n\t"
          "ds_read_b64_tr_b16 a[146:147], v6 offset:32448\n\t"
          "v_mfma_f32_16x16x32_bf16 v[116:119], a[128:131], a[36:39], v[116:119]\n\t"
          "v_mfma_f32_16x16x32_bf16 v[120:123], a[128:131], a[40:43], v[120:123]\n\t"
          "ds_read_b64_tr_b16 a[148:149], v6 offset:43200\n\t"
          "v_mfma_f32_16x16x32_bf16 v[120:123], a[132:135], a[56:59], v[120:123]\n\t"
          "buffer_load_dwordx4 v14, %[srdW1], s45 offen offset:2048 lds\n\t"
          "s_add_u32 s45, s45, 458752\n\t"
          "v_mfma_f32_16x16x32_bf16 v[124:127], a[132:135], a[60:63], v[124:127]\n\t"
          "ds_read_b64_tr_b16 a[150:151], v6 offset:46784\n\t"
          "v_mfma_f32_16x16x32_bf16 v[124:127], a[128:131], a[44:47], v[124:127]\n\t"
          "s_waitcnt lgkmcnt(4)\n\t"
          "v_mfma_f32_16x16x32_bf16 v[236:239], a[136:139], a[44:47], v[236:239]\n\t"
          "ds_read_b64_tr_b16 a[152:153], v6 offset:36032\n\t"
          "v_mfma_f32_16x16x32_bf16 v[236:239], a[140:143], a[60:63], v[236:239]\n\t"
          "v_mfma_f32_16x16x32_bf16 v[232:235], a[140:143], a[56:59], v[232:235]\n\t"
          "ds_read_b64_tr_b16 a[154:155], v6 offset:39616\n\t"
          "v_mfma_f32_16x16x32_bf16 v[232:235], a[136:139], a[40:43], v[232:235]\n\t"
          "v_mfma_f32_16x16x32_bf16 v[228:231], a[136:139], a[36:39], v[228:231]\n\t"
          "ds_read_b64_tr_b16 a[156:157], v6 offset:50368\n\t"
          "buffer_load_dwordx4 a[120:123], %[vx], %[ws], s48 offen offset:2048 sc1\n\t"
          "v_mfma_f32_16x16x32_bf16 v[228:231], a[140:143], a[52:55], v[228:231]\n\t"
          "v_mfma_f32_16x16x32_bf16 v[224:227], a[140:143], a[48:51], v[224:227]\n\t"
          "ds_read_b64_tr_b16 a[158:159], v6 offset:53952\n\t"
          "v_mfma_f32_16x16x32_bf16 v[224:227], a[136:139], a[32:35], v[224:227]\n\t"
          "s_waitcnt vmcnt(16)\n\t"
          "s_barrier\n\t"
          "s_waitcnt lgkmcnt(4)\n\t"
          "v_mfma_f32_16x16x32_bf16 v[128:131], a[144:147], a[32:35], v[128:131]\n\t"
          "ds_read_b64_tr_b16 a[128:129], v7 offset:0\n\t"
          "v_mfma_f32_16x16x32_bf16 v[128:131], a[148:151], a[48:51], v[128:131]\n\t"
          "v_mfma_f32_16x16x32_bf16 v[132:135], a[148:151], a[52:55], v[132:135]\n\t"
          "ds_read_b64_tr_b16 a[130:131], v7 offset:3584\n\t"
          "v_mfma_f32_16x16x32_bf16 v[132:135], a[144:147], a[36:39], v[132:135]\n\t"
          "buffer_load_dwordx4 a[124:127], %[vx], %[ws], s48 offen offset:3072 sc1\n\t"
          "s_add_u32 s44, s44, 8192\n\t"
          "s_add_u32 s48, s48, 8192\n\t"
          "v_mfma_f32_16x16x32_bf16 v[136:139], a[144:147], a[40:43], v[136:139]\n\t"
          "ds_read_b64_tr_b16 a[132:133], v7 offset:14336\n\t"
          "v_mfma_f32_16x16x32_bf16 v[136:139], a[148:151], a[56:59], v[136:139]\n\t"
          "v_mfma_f32_16x16x32_bf16 v[140:143], a[148:151], a[60:63], v[140:143]\n\t"
          "ds_read_b64_tr_b16 a[134:135], v7 offset:17920\n\t"
          "v_mfma_f32_16x16x32_bf16 v[140:143], a[144:147], a[44:47], v[140:143]\n\t"
          "s_waitcnt lgkmcnt(4)\n\t"
          "v_mfma_f32_16x16x32_bf16 v[252:255], a[152:155], a[44:47], v[252:255]\n\t"
          "ds_read_b64_tr_b16 a[136:137], v7 offset:7168\n\t"
          "v_mfma_f32_16x16x32_bf16 v[252:255], a[156:159], a[60:63], v[252:255]\n\t"
          "v_mfma_f32_16x16x32_bf16 v[248:251], a[156:159], a[56:59], v[248:251]\n\t"
          "ds_read_b64_tr_b16 a[138:139], v7 offset:10752\n\t"
          "v_mfma_f32_16x16x32_bf16 v[248:251], a[152:155], a[40:43], v[248:251]\n\t"
          "v_mfma_f32_16x16x32_bf16 v[244:247], a[152:155], a[36:39], v[244:247]\n\t"
          "ds_read_b64_tr_b16 a[140:141], v7 offset:21504\n\t"
          "v_mfma_f32_16x16x32_bf16 v[244:247], a[156:159], a[52:55], v[244:247]\n\t"
          "v_mfma_f32_16x16x32_bf16 v[240:243], a[156:159], a[48:51], v[240:243]\n\t"
          "ds_read_b64_tr_b16 a[142:143], v7 offset:25088\n\t"
          "v_mfma_f32_16x16x32_bf16 v[240:243], a[152:155], a[32:35], v[240:243]\n\t"
          "1:\n\t"
          "s_waitcnt vmcnt(15)\n\t"
          "s_waitcnt lgkmcnt(4)\n\t"
          "v_mfma_f32_16x16x32_bf16 v[32:35], a[128:131], a[64:67], v[32:35]\n\t"
          "ds_read_b64_tr_b16 a[144:145], v7 offset:32\n\t"
          "v_mfma_f32_16x16x32_bf16 v[32:35], a[132:135], a[80:83], v[32:35]\n\t"
          "s_add_u32 m0, s46, 0\n\t"
          "v_mfma_f32_16x16x32_bf16 v[36:39], a[132:135], a[84:87], v[36:39]\n\t"
          "ds_read_b64_tr_b16 a[146:147], v7 offset:3616\n\t"
          "buffer_load_dwordx4 v8, %[srdW1], s45 offen offset:0 lds\n\t"
          "v_mfma_f32_16x16x32_bf16 v[36:39], a[128:131], a[68:71], v[36:39]\n\t"
          "v_mfma_f32_16x16x32_bf16 v[40:43], a[128:131], a[72:75], v[40:43]\n\t"
          "ds_read_b64_tr_b16 a[148:149], v7 offset:14368\n\t"
          "v_mfma_f32_16x16x32_bf16 v[40:43], a[132:135], a[88:91], v[40:43]\n\t"
          "v_mfma_f32_16x16x32_bf16 v[44:47], a[132:135], a[92:95], v[44:47]\n\t"
          "ds_read_b64_tr_b16 a[150:151], v7 offset:17952\n\t"
          "v_mfma_f32_16x16x32_bf16 v[44:47], a[128:131], a[76:79], v[44:47]\n\t"
          "s_waitcnt lgkmcnt(4)\n\t"
          "v_mfma_f32_16x16x32_bf16 v[156:159], a[136:139], a[76:79], v[156:159]\n\t"
          "ds_read_b64_tr_b16 a[152:153], v7 offset:7200\n\t"
          "buffer_load_dwordx4 a[0:3], %[vx], %[ws], s44 offen offset:0 sc1\n\t"
          "v_mfma_f32_16x16x32_bf16 v[156:159], a[140:143], a[92:95], v[156:159]\n\t"
          "v_mfma_f32_16x16x32_bf16 v[152:155], a[140:143], a[88:91], v[152:155]\n\t"
          "ds_read_b64_tr_b16 a[154:155], v7 offset:10784\n\t"
          "v_mfma_f32_16x16x32_bf16 v[152:155], a[136:139], a[72:75], v[152:155]\n\t"
          "v_mfma_f32_16x16x32_bf16 v[148:151], a[136:139], a[68:71], v[148:151]\n\t"
          "ds_read_b64_tr_b16 a[156:157], v7 offset:21536\n\t"
          "v_mfma_f32_16x16x32_bf16 v[148:151], a[140:143], a[84:87], v[148:151]\n\t"
          "v_mfma_f32_16x16x32_bf16 v[144:147], a[140:143], a[80:83], v[144:147]\n\t"
          "ds_read_b64_tr_b16 a[158:159], v7 offset:25120\n\t"
          "v_mfma_f32_16x16x32_bf16 v[144:147], a[136:139], a[64:67], v[144:147]\n\t"
          "buffer_load_dwordx4 v9, %[srdW1], s45 offen offset:1024 lds\n\t"
          "s_waitcnt lgkmcnt(4)\n\t"
          "v_mfma_f32_16x16x32_bf16 v[48:51], a[144:147], a[64:67], v[48:51]\n\t"
          "ds_read_b64_tr_b16 a[128:129], v7 offset:64\n\t"
          "v_mfma_f32_16x16x32_bf16 v[48:51], a[148:151], a[80:83], v[48:51]\n\t"
          "v_mfma_f32_16x16x32_bf16 v[52:55], a[148:151], a[84:87], v[52:55]\n\t"
          "ds_read_b64_tr_b16 a[130:131], v7 offset:3648\n\t"
          "v_mfma_f32_16x16x32_bf16 v[52:55], a[144:147], a[68:71], v[52:55]\n\t"
          "v_mfma_f32_16x16x32_bf16 v[56:59], a[144:147], a[72:75], v[56:59]\n\t"
          "ds_read_b64_tr_b16 a[132:133], v7 offset:14400\n\t"
          "v_mfma_f32_16x16x32_bf16 v[56:59], a[148:151], a[88:91], v[56:59]\n\t"
          "v_mfma_f32_16x16x32_bf16 v[60:63], a[148:151], a[92:95], v[60:63]\n\t"
          "ds_read_b64_tr_b16 a[134:135], v7 offset:17984\n\t"
          "buffer_load_dwordx4 a[4:7], %[vx], %[ws], s44 offen offset:1024 sc1\n\t"
          "v_mfma_f32_16x16x32_bf16 v[60:63], a[144:147], a[76:79], v[60:63]\n\t"
          "s_waitcnt lgkmcnt(4)\n\t"
          "v_mfma_f32_16x16x32_bf16 v[172:175], a[152:155], a[76:79], v[172:175]\n\t"
          "ds_read_b64_tr_b16 a[136:137], v7 offset:7232\n\t"
          "v_mfma_f32_16x16x32_bf16 v[172:175], a[156:159], a[92:95], v[172:175]\n\t"
          "v_mfma_f32_16x16x32_bf16 v[168:171], a[156:159], a[88:91], v[168:171]\n\t"
          "ds_read_b64_tr_b16 a[138:139], v7 offset:10816\n\t"
          "v_mfma_f32_16x16x32_bf16 v[168:171], a[152:155], a[72:75], v[168:171]\n\t"
          "v_mfma_f32_16x16x32_bf16 v[164:167], a[152:155], a[68:71], v[164:167]\n\t"
          "ds_read_b64_tr_b16 a[140:141], v7 offset:21568\n\t"
          "v_mfma_f32_16x16x32_bf16 v[164:167], a[156:159], a[84:87], v[164:167]\n\t"
          "buffer_load_dwordx4 v10, %[srdW1], s45 offen offset:2048 lds\n\t"
          "v_mfma_f32_16x16x32_bf16 v[160:163], a[156:159], a[80:83], v[160:163]\n\t"
          "ds_read_b64_tr_b16 a[142:143], v7 offset:25152\n\t"
          "v_mfma_f32_16x16x32_bf16 v[160:163], a[152:155], a[64:67], v[160:163]\n\t"
          "s_waitcnt lgkmcnt(4)\n\t"
          "v_mfma_f32_16x16x32_bf16 v[64:67], a[128:131], a[64:67], v[64:67]\n\t"
          "ds_read_b64_tr_b16 a[144:145], v7 offset:96\n\t"
          "v_mfma_f32_16x16x32_bf16 v[64:67], a[132:135], a[80:83], v[64:67]\n\t"
          "v_mfma_f32_16x16x32_bf16 v[68:71], a[132:135], a[84:87], v[68:71]\n\t"
          "ds_read_b64_tr_b16 a[146:147], v7 offset:3680\n\t"
          "v_mfma_f32_16x16x32_bf16 v[68:71], a[128:131], a[68:71], v[68:71]\n\t"
          "v_mfma_f32_16x16x32_bf16 v[72:75], a[128:131], a[72:75], v[72:75]\n\t"
          "ds_read_b64_tr_b16 a[148:149], v7 offset:14432\n\t"
          "buffer_load_dwordx4 a[8:11], %[vx], %[ws], s44 offen offset:2048 sc1\n\t"
          "v_mfma_f32_16x16x32_bf16 v[72:75], a[132:135], a[88:91], v[72:75]\n\t"
          "v_mfma_f32_16x16x32_bf16 v[76:79], a[132:135], a[92:95], v[76:79]\n\t"
          "ds_read_b64_tr_b16 a[150:151], v7 offset:18016\n\t"
          "v_mfma_f32_16x16x32_bf16 v[76:79], a[128:131], a[76:79], v[76:79]\n\t"
          "s_waitcnt lgkmcnt(4)\n\t"
          "v_mfma_f32_16x16x32_bf16 v[188:191], a[136:139], a[76:79], v[188:191]\n\t"
          "ds_read_b64_tr_b16 a[152:153], v7 offset:7264\n\t"
          "v_mfma_f32_16x16x32_bf16 v[188:191], a[140:143], a[92:95], v[188:191]\n\t"
          "v_mfma_f32_16x16x32_bf16 v[184:187], a[140:143], a[88:91], v[184:187]\n\t"
          "ds_read_b64_tr_b16 a[154:155], v7 offset:10848\n\t"
          "v_mfma_f32_16x16x32_bf16 v[184:187], a[136:139], a[72:75], v[184:187]\n\t"
          "buffer_load_dwordx4 v11, %[srdW1], s45 offen offset:3072 lds\n\t"
          "v_mfma_f32_16x16x32_bf16 v[180:183], a[136:139], a[68:71], v[180:183]\n\t"
          "ds_read_b64_tr_b16 a[156:157], v7 offset:21600\n\t"
          "v_mfma_f32_16x16x32_bf16 v[180:183], a[140:143], a[84:87], v[180:183]\n\t"
          "v_mfma_f32_16x16x32_bf16 v[176:179], a[140:143], a[80:83], v[176:179]\n\t"
          "ds_read_b64_tr_b16 a[158:159], v7 offset:25184\n\t"
          "v_mfma_f32_16x16x32_bf16 v[176:179], a[136:139], a[64:67], v[176:179]\n\t"
          "s_waitcnt lgkmcnt(4)\n\t"
          "v_mfma_f32_16x16x32_bf16 v[80:83], a[144:147], a[64:67], v[80:83]\n\t"
          "ds_read_b64_tr_b16 a[128:129], v7 offset:128\n\t"
          "v_mfma_f32_16x16x32_bf16 v[80:83], a[148:151], a[80:83], v[80:83]\n\t"
          "v_mfma_f32_16x16x32_bf16 v[84:87], a[148:151], a[84:87], v[84:87]\n\t"
          "ds_read_b64_tr_b16 a[130:131], v7 offset:3712\n\t"
          "buffer_load_dwordx4 a[12:15], %[vx], %[ws], s44 offen offset:3072 sc1\n\t"
          "v_mfma_f32_16x16x32_bf16 v[84:87], a[144:147], a[68:71], v[84:87]\n\t"
          "v_mfma_f32_16x16x32_bf16 v[88:91], a[144:147], a[72:75], v[88:91]\n\t"
          "ds_read_b64_tr_b16 a[132:133], v7 offset:14464\n\t"
          "v_mfma_f32_16x16x32_bf16 v[88:91], a[148:151], a[88:91], v[88:91]\n\t"
          "v_mfma_f32_16x16x32_bf16 v[92:95], a[148:151], a[92:95], v[92:95]\n\t"
          "ds_read_b64_tr_b16 a[134:135], v7 offset:18048\n\t"
          "v_mfma_f32_16x16x32_bf16 v[92:95], a[144:147], a[76:79], v[92:95]\n\t"
          "s_waitcnt lgkmcnt(4)\n\t"
          "v_mfma_f32_16x16x32_bf16 v[204:207], a[152:155], a[76:79], v[204:207]\n\t"
          "ds_read_b64_tr_b16 a[136:137], v7 offset:7296\n\t"
          "s_add_u32 m0, s46, 4096\n\t"
          "v_mfma_f32_16x16x32_bf16 v[204:207], a[156:159], a[92:95], v[204:207]\n\t"
          "buffer_load_dwordx4 v12, %[srdW1], s45 offen offset:0 lds\n\t"
          "v_mfma_f32_16x16x32_bf16 v[200:203], a[156:159], a[88:91], v[200:203]\n\t"
          "ds_read_b64_tr_b16 a[138:139], v7 offset:10880\n\t"
          "v_mfma_f32_16x16x32_bf16 v[200:203], a[152:155], a[72:75], v[200:203]\n\t"
          "v_mfma_f32_16x16x32_bf16 v[196:199], a[152:155], a[68:71], v[196:199]\n\t"
          "ds_read_b64_tr_b16 a[140:141], v7 offset:21632\n\t"
          "v_mfma_f32_16x16x32_bf16 v[196:199], a[156:159], a[84:87], v[196:199]\n\t"
          "v_mfma_f32_16x16x32_bf16 v[192:195], a[156:159], a[80:83], v[192:195]\n\t"
          "ds_read_b64_tr_b16 a[142:143], v7 offset:25216\n\t"
          "v_mfma_f32_16x16x32_bf16 v[192:195], a[152:155], a[64:67], v[192:195]\n\t"
          "s_waitcnt lgkmcnt(4)\n\t"
          "v_mfma_f32_16x16x32_bf16 v[96:99], a[128:131], a[64:67], v[96:99]\n\t"
          "ds_read_b64_tr_b16 a[144:145], v7 offset:160\n\t"
          "buffer_load_dwordx4 a[16:19], %[vx], %[ws], s48 offen offset:0 sc1\n\t"
          "v_mfma_f32_16x16x32_bf16 v[96:99], a[132:135], a[80:83], v[96:99]\n\t"
          "v_mfma_f32_16x16x32_bf16 v[100:103], a[132:135], a[84:87], v[100:103]\n\t"
          "ds_read_b64_tr_b16 a[146:147], v7 offset:3744\n\t"
          "v_mfma_f32_16x16x32_bf16 v[100:103], a[128:131], a[68:71], v[100:103]\n\t"
          "v_mfma_f32_16x16x32_bf16 v[104:107], a[128:131], a[72:75], v[104:107]\n\t"
          "ds_read_b64_tr_b16 a[148:149], v7 offset:14496\n\t"
          "v_mfma_f32_16x16x32_bf16 v[104:107], a[132:135], a[88:91], v[104:107]\n\t"
          "v_mfma_f32_16x16x32_bf16 v[108:111], a[132:135], a[92:95], v[108:111]\n\t"
          "ds_read_b64_tr_b16 a[150:151], v7 offset:18080\n\t"
          "v_mfma_f32_16x16x32_bf16 v[108:111], a[128:131], a[76:79], v[108:111]\n\t"
          "buffer_load_dwordx4 v13, %[srdW1], s45 offen offset:1024 lds\n\t"
          "s_waitcnt lgkmcnt(4)\n\t"
          "v_mfma_f32_16x16x32_bf16 v[220:223], a[136:139], a[76:79], v[220:223]\n\t"
          "ds_read_b64_tr_b16 a[152:153], v7 offset:7328\n\t"
          "v_mfma_f32_16x16x32_bf16 v[220:223], a[140:143], a[92:95], v[220:223]\n\t"
          "v_mfma_f32_16x16x32_bf16 v[216:219], a[140:143], a[88:91], v[216:219]\n\t"
          "ds_read_b64_tr_b16 a[154:155], v7 offset:10912\n\t"
          "v_mfma_f32_16x16x32_bf16 v[216:219], a[136:139], a[72:75], v[216:219]\n\t"
          "v_mfma_f32_16x16x32_bf16 v[212:215], a[136:139], a[68:71], v[212:215]\n\t"
          "ds_read_b64_tr_b16 a[156:157], v7 offset:21664\n\t"
          "v_mfma_f32_16x16x32_bf16 v[212:215], a[140:143], a[84:87], v[212:215]\n\t"
          "v_mfma_f32_16x16x32_bf16 v[208:211], a[140:143], a[80:83], v[208:211]\n\t"
          "ds_read_b64_tr_b16 a[158:159], v7 offset:25248\n\t"
          "buffer_load_dwordx4 a[20:23], %[vx], %[ws], s48 offen offset:1024 sc1\n\t"
          "v_mfma_f32_16x16x32_bf16 v[208:211], a[136:139], a[64:67], v[208:211]\n\t"
          "s_waitcnt lgkmcnt(4)\n\t"
          "v_mfma_f32_16x16x32_bf16 v[112:115], a[144:147], a[64:67], v[112:115]\n\t"
          "ds_read_b64_tr_b16 a[128:129], v7 offset:192\n\t"
          "v_mfma_f32_16x16x32_bf16 v[112:115], a[148:151], a[80:83], v[112:115]\n\t"
          "v_mfma_f32_16x16x32_bf16 v[116:119], a[148:151], a[84:87], v[116:119]\n\t"
          "ds_read_b64_tr_b16 a[130:131], v7 offset:3776\n\t"
          "v_mfma_f32_16x16x32_bf16 v[116:119], a[144:147], a[68:71], v[116:119]\n\t"
          "v_mfma_f32_16x16x32_bf16 v[120:123], a[144:147], a[72:75], v[120:123]\n\t"
          "ds_read_b64_tr_b16 a[132:133], v7 offset:14528\n\t"
          "v_mfma_f32_16x16x32_bf16 v[120:123], a[148:151], a[88:91], v[120:123]\n\t"
          "buffer_load_dwordx4 v14, %[srdW1], s45 offen offset:2048 lds\n\t"
          "s_add_u32 s45, s45, 458752\n\t"
          "v_mfma_f32_16x16x32_bf16 v[124:127], a[148:151], a[92:95], v[124:127]\n\t"
          "ds_read_b64_tr_b16 a[134:135], v7 offset:18112\n\t"
          "v_mfma_f32_16x16x32_bf16 v[124:127], a[144:147], a[76:79], v[124:127]\n\t"
          "s_waitcnt lgkmcnt(4)\n\t"
          "v_mfma_f32_16x16x32_bf16 v[236:239], a[152:155], a[76:79], v[236:239]\n\t"
          "ds_read_b64_tr_b16 a[136:137], v7 offset:7360\n\t"
          "v_mfma_f32_16x16x32_bf16 v[236:239], a[156:159], a[92:95], v[236:239]\n\t"
          "v_mfma_f32_16x16x32_bf16 v[232:235], a[156:159], a[88:91], v[232:235]\n\t"
          "ds_read_b64_tr_b16 a[138:139], v7 offset:10944\n\t"
          "v_mfma_f32_16x16x32_bf16 v[232:235], a[152:155], a[72:75], v[232:235]\n\t"
          "v_mfma_f32_16x16x32_bf16 v[228:231], a[152:155], a[68:71], v[228:231]\n\t"
          "ds_read_b64_tr_b16 a[140:141], v7 offset:21696\n\t"
          "buffer_load_dwordx4 a[24:27], %[vx], %[ws], s48 offen offset:2048 sc1\n\t"
          "v_mfma_f32_16x16x32_bf16 v[228:231], a[156:159], a[84:87], v[228:231]\n\t"
          "v_mfma_f32_16x16x32_bf16 v[224:227], a[156:159], a[80:83], v[224:227]\n\t"
          "ds_read_b64_tr_b16 a[142:143], v7 offset:25280\n\t"
          "v_mfma_f32_16x16x32_bf16 v[224:227], a[152:155], a[64:67], v[224:227]\n\t"
          "s_waitcnt vmcnt(16)\n\t"
          "s_barrier\n\t"
          "s_waitcnt lgkmcnt(4)\n\t"
          "v_mfma_f32_16x16x32_bf16 v[128:131], a[128:131], a[64:67], v[128:131]\n\t"
          "ds_read_b64_tr_b16 a[144:145], v7 offset:28672\n\t"
          "v_mfma_f32_16x16x32_bf16 v[128:131], a[132:135], a[80:83], v[128:131]\n\t"
          "v_mfma_f32_16x16x32_bf16 v[132:135], a[132:135], a[84:87], v[132:135]\n\t"
          "ds_read_b64_tr_b16 a[146:147], v7 offset:32256\n\t"
          "v_mfma_f32_16x16x32_bf16 v[132:135], a[128:131], a[68:71], v[132:135]\n\t"
          "buffer_load_dwordx4 a[28:31], %[vx], %[ws], s48 offen offset:3072 sc1\n\t"
          "s_add_u32 s44, s44, 8192\n\t"
          "s_add_u32 s48, s48, 8192\n\t"
          "v_mfma_f32_16x16x32_bf16 v[136:139], a[128:131], a[72:75], v[136:139]\n\t"
          "ds_read_b64_tr_b16 a[148:149], v7 offset:43008\n\t"
          "v_mfma_f32_16x16x32_bf16 v[136:139], a[132:135], a[88:91], v[136:139]\n\t"
          "v_mfma_f32_16x16x32_bf16 v[140:143], a[132:135], a[92:95], v[140:143]\n\t"
          "ds_read_b64_tr_b16 a[150:151], v7 offset:46592\n\t"
          "v_mfma_f32_16x16x32_bf16 v[140:143], a[128:131], a[76:79], v[140:143]\n\t"
          "s_waitcnt lgkmcnt(4)\n\t"
          "v_mfma_f32_16x16x32_bf16 v[252:255], a[136:139], a[76:79], v[252:255]\n\t"
          "ds_read_b64_tr_b16 a[152:153], v7 offset:35840\n\t"
          "v_mfma_f32_16x16x32_bf16 v[252:255], a[140:143], a[92:95], v[252:255]\n\t"
          "v_mfma_f32_16x16x32_bf16 v[248:251], a[140:143], a[88:91], v[248:251]\n\t"
          "ds_read_b64_tr_b16 a[154:155], v7 offset:39424\n\t"
          "v_mfma_f32_16x16x32_bf16 v[248:251], a[136:139], a[72:75], v[248:251]\n\t"
          "v_mfma_f32_16x16x32_bf16 v[244:247], a[136:139], a[68:71], v[244:247]\n\t"
          "ds_read_b64_tr_b16 a[156:157], v7 offset:50176\n\t"
          "v_mfma_f32_16x16x32_bf16 v[244:247], a[140:143], a[84:87], v[244:247]\n\t"
          "v_mfma_f32_16x16x32_bf16 v[240:243], a[140:143], a[80:83], v[240:243]\n\t"
          "ds_read_b64_tr_b16 a[158:159], v7 offset:53760\n\t"
          "v_mfma_f32_16x16x32_bf16 v[240:243], a[136:139], a[64:67], v[240:243]\n\t"
          "s_waitcnt vmcnt(15)\n\t"
          "s_waitcnt lgkmcnt(4)\n\t"
          "v_mfma_f32_16x16x32_bf16 v[32:35], a[144:147], a[96:99], v[32:35]\n\t"
          "ds_read_b64_tr_b16 a[128:129], v7 offset:28704\n\t"
          "v_mfma_f32_16x16x32_bf16 v[32:35], a[148:151], a[112:115], v[32:35]\n\t"
          "s_add_u32 m0, s46, 28672\n\t"
          "v_mfma_f32_16x16x32_bf16 v[36:39], a[148:151], a[116:119], v[36:39]\n\t"
          "ds_read_b64_tr_b16 a[130:131], v7 offset:32288\n\t"
          "buffer_load_dwordx4 v8, %[srdW1], s45 offen offset:0 lds\n\t"
          "v_mfma_f32_16x16x32_bf16 v[36:39], a[144:147], a[100:103], v[36:39]\n\t"
          "v_mfma_f32_16x16x32_bf16 v[40:43], a[144:147], a[104:107], v[40:43]\n\t"
          "ds_read_b64_tr_b16 a[132:133], v7 offset:43040\n\t"
          "v_mfma_f32_16x16x32_bf16 v[40:43], a[148:151], a[120:123], v[40:43]\n\t"
          "v_mfma_f32_16x16x32_bf16 v[44:47], a[148:151], a[124:127], v[44:47]\n\t"
          "ds_read_b64_tr_b16 a[134:135], v7 offset:46624\n\t"
          "v_mfma_f32_16x16x32_bf16 v[44:47], a[144:147], a[108:111], v[44:47]\n\t"
          "s_waitcnt lgkmcnt(4)\n\t"
          "v_mfma_f32_16x16x32_bf16 v[156:159], a[152:155], a[108:111], v[156:159]\n\t"
          "ds_read_b64_tr_b16 a[136:137], v7 offset:35872\n\t"
          "buffer_load_dwordx4 a[32:35], %[vx], %[ws], s44 offen offset:0 sc1\n\t"
          "v_mfma_f32_16x16x32_bf16 v[156:159], a[156:159], a[124:127], v[156:159]\n\t"
          "v_mfma_f32_16x16x32_bf16 v[152:155], a[156:159], a[120:123], v[152:155]\n\t"
          "ds_read_b64_tr_b16 a[138:139], v7 offset:39456\n\t"
          "v_mfma_f32_16x16x32_bf16 v[152:155], a[152:155], a[104:107], v[152:155]\n\t"
          "v_mfma_f32_16x16x32_bf16 v[148:151], a[152:155], a[100:103], v[148:151]\n\t"
          "ds_read_b64_tr_b16 a[140:141], v7 offset:50208\n\t"
          "v_mfma_f32_16x16x32_bf16 v[148:151], a[156:159], a[116:119], v[148:151]\n\t"
          "v_mfma_f32_16x16x32_bf16 v[144:147], a[156:159], a[112:115], v[144:147]\n\t"
          "ds_read_b64_tr_b16 a[142:143], v7 offset:53792\n\t"
          "v_mfma_f32_16x16x32_bf16 v[144:147], a[152:155], a[96:99], v[144:147]\n\t"
          "buffer_load_dwordx4 v9, %[srdW1], s45 offen offset:1024 lds\n\t"
          "s_waitcnt lgkmcnt(4)\n\t"
          "v_mfma_f32_16x16x32_bf16 v[48:51], a[128:131], a[96:99], v[48:51]\n\t"
          "ds_read_b64_tr_b16 a[144:145], v7 offset:28736\n\t"
          "v_mfma_f32_16x16x32_bf16 v[48:51], a[132:135], a[112:115], v[48:51]\n\t"
          "v_mfma_f32_16x16x32_bf16 v[52:55], a[132:135], a[116:119], v[52:55]\n\t"
          "ds_read_b64_tr_b16 a[146:147], v7 offset:32320\n\t"
          "v_mfma_f32_16x16x32_bf16 v[52:55], a[128:131], a[100:103], v[52:55]\n\t"
          "v_mfma_f32_16x16x32_bf16 v[56:59], a[128:131], a[104:107], v[56:59]\n\t"
          "ds_read_b64_tr_b16 a[148:149], v7 offset:43072\n\t"
          "v_mfma_f32_16x16x32_bf16 v[56:59], a[132:135], a[120:123], v[56:59]\n\t"
          "v_mfma_f32_16x16x32_bf16 v[60:63], a[132:135], a[124:127], v[60:63]\n\t"
          "ds_read_b64_tr_b16 a[150:151], v7 offset:46656\n\t"
          "buffer_load_dwordx4 a[36:39], %[vx], %[ws], s44 offen offset:1024 sc1\n\t"
          "v_mfma_f32_16x16x32_bf16 v[60:63], a[128:131], a[108:111], v[60:63]\n\t"
          "s_waitcnt lgkmcnt(4)\n\t"
          "v_mfma_f32_16x16x32_bf16 v[172:175], a[136:139], a[108:111], v[172:175]\n\t"
          "ds_read_b64_tr_b16 a[152:153], v7 offset:35904\n\t"
          "v_mfma_f32_16x16x32_bf16 v[172:175], a[140:143], a[124:127], v[172:175]\n\t"
          "v_mfma_f32_16x16x32_bf16 v[168:171], a[140:143], a[120:123], v[168:171]\n\t"
          "ds_read_b64_tr_b16 a[154:155], v7 offset:39488\n\t"
          "v_mfma_f32_16x16x32_bf16 v[168:171], a[136:139], a[104:107], v[168:171]\n\t"
          "v_mfma_f32_16x16x32_bf16 v[164:167], a[136:139], a[100:103], v[164:167]\n\t"
          "ds_read_b64_tr_b16 a[156:157], v7 offset:50240\n\t"
          "v_mfma_f32_16x16x32_bf16 v[164:167], a[140:143], a[116:119], v[164:167]\n\t"
          "buffer_load_dwordx4 v10, %[srdW1], s45 offen offset:2048 lds\n\t"
          "v_mfma_f32_16x16x32_bf16 v[160:163], a[140:143], a[112:115], v[160:163]\n\t"
          "ds_read_b64_tr_b16 a[158:159], v7 offset:53824\n\t"
          "v_mfma_f32_16x16x32_bf16 v[160:163], a[136:139], a[96:99], v[160:163]\n\t"
          "s_waitcnt lgkmcnt(4)\n\t"
          "v_mfma_f32_16x16x32_bf16 v[64:67], a[144:147], a[96:99], v[64:67]\n\t"
          "ds_read_b64_tr_b16 a[128:129], v7 offset:28768\n\t"
          "v_mfma_f32_16x16x32_bf16 v[64:67], a[148:151], a[112:115], v[64:67]\n\t"
          "v_mfma_f32_16x16x32_bf16 v[68:71], a[148:151], a[116:119], v[68:71]\n\t"
          "ds_read_b64_tr_b16 a[130:131], v7 offset:32352\n\t"
          "v_mfma_f32_16x16x32_bf16 v[68:71], a[144:147], a[100:103], v[68:71]\n\t"
          "v_mfma_f32_16x16x32_bf16 v[72:75], a[144:147], a[104:107], v[72:75]\n\t"
          "ds_read_b64_tr_b16 a[132:133], v7 offset:43104\n\t"
          "buffer_load_dwordx4 a[40:43], %[vx], %[ws], s44 offen offset:2048 sc1\n\t"
          "v_mfma_f32_16x16x32_bf16 v[72:75], a[148:151], a[120:123], v[72:75]\n\t"
          "v_mfma_f32_16x16x32_bf16 v[76:79], a[148:151], a[124:127], v[76:79]\n\t"
          "ds_read_b64_tr_b16 a[134:135], v7 offset:46688\n\t"
          "v_mfma_f32_16x16x32_bf16 v[76:79], a[144:147], a[108:111], v[76:79]\n\t"
          "s_waitcnt lgkmcnt(4)\n\t"
          "v_mfma_f32_16x16x32_bf16 v[188:191], a[152:155], a[108:111], v[188:191]\n\t"
          "ds_read_b64_tr_b16 a[136:137], v7 offset:35936\n\t"
          "v_mfma_f32_16x16x32_bf16 v[188:191], a[156:159], a[124:127], v[188:191]\n\t"
          "v_mfma_f32_16x16x32_bf16 v[184:187], a[156:159], a[120:123], v[184:187]\n\t"
          "ds_read_b64_tr_b16 a[138:139], v7 offset:39520\n\t"
          "v_mfma_f32_16x16x32_bf16 v[184:187], a[152:155], a[104:107], v[184:187]\n\t"
          "buffer_load_dwordx4 v11, %[srdW1], s45 offen offset:3072 lds\n\t"
          "v_mfma_f32_16x16x32_bf16 v[180:183], a[152:155], a[100:103], v[180:183]\n\t"
          "ds_read_b64_tr_b16 a[140:141], v7 offset:50272\n\t"
          "v_mfma_f32_16x16x32_bf16 v[180:183], a[156:159], a[116:119], v[180:183]\n\t"
          "v_mfma_f32_16x16x32_bf16 v[176:179], a[156:159], a[112:115], v[176:179]\n\t"
          "ds_read_b64_tr_b16 a[142:143], v7 offset:53856\n\t"
          "v_mfma_f32_16x16x32_bf16 v[176:179], a[152:155], a[96:99], v[176:179]\n\t"
          "s_waitcnt lgkmcnt(4)\n\t"
          "v_mfma_f32_16x16x32_bf16 v[80:83], a[128:131], a[96:99], v[80:83]\n\t"
          "ds_read_b64_tr_b16 a[144:145], v7 offset:28800\n\t"
          "v_mfma_f32_16x16x32_bf16 v[80:83], a[132:135], a[112:115], v[80:83]\n\t"
          "v_mfma_f32_16x16x32_bf16 v[84:87], a[132:135], a[116:119], v[84:87]\n\t"
          "ds_read_b64_tr_b16 a[146:147], v7 offset:32384\n\t"
          "buffer_load_dwordx4 a[44:47], %[vx], %[ws], s44 offen offset:3072 sc1\n\t"
          "v_mfma_f32_16x16x32_bf16 v[84:87], a[128:131], a[100:103], v[84:87]\n\t"
          "v_mfma_f32_16x16x32_bf16 v[88:91], a[128:131], a[104:107], v[88:91]\n\t"
          "ds_read_b64_tr_b16 a[148:149], v7 offset:43136\n\t"
          "v_mfma_f32_16x16x32_bf16 v[88:91], a[132:135], a[120:123], v[88:91]\n\t"
          "v_mfma_f32_16x16x32_bf16 v[92:95], a[132:135], a[124:127], v[92:95]\n\t"
          "ds_read_b64_tr_b16 a[150:151], v7 offset:46720\n\t"
          "v_mfma_f32_16x16x32_bf16 v[92:95], a[128:131], a[108:111], v[92:95]\n\t"
          "s_waitcnt lgkmcnt(4)\n\t"
          "v_mfma_f32_16x16x32_bf16 v[204:207], a[136:139], a[108:111], v[204:207]\n\t"
          "ds_read_b64_tr_b16 a[152:153], v7 offset:35968\n\t"
          "s_add_u32 m0, s46, 32768\n\t"
          "v_mfma_f32_16x16x32_bf16 v[204:207], a[140:143], a[124:127], v[204:207]\n\t"
          "buffer_load_dwordx4 v12, %[srdW1], s45 offen offset:0 lds\n\t"
          "v_mfma_f32_16x16x32_bf16 v[200:203], a[140:143], a[120:123], v[200:203]\n\t"
          "ds_read_b64_tr_b16 a[154:155], v7 offset:39552\n\t"
          "v_mfma_f32_16x16x32_bf16 v[200:203], a[136:139], a[104:107], v[200:203]\n\t"
          "v_mfma_f32_16x16x32_bf16 v[196:199], a[136:139], a[100:103], v[196:199]\n\t"
          "ds_read_b64_tr_b16 a[156:157], v7 offset:50304\n\t"
          "v_mfma_f32_16x16x32_bf16 v[196:199], a[140:143], a[116:119], v[196:199]\n\t"
          "v_mfma_f32_16x16x32_bf16 v[192:195], a[140:143], a[112:115], v[192:195]\n\t"
          "ds_read_b64_tr_b16 a[158:159], v7 offset:53888\n\t"
          "v_mfma_f32_16x16x32_bf16 v[192:195], a[136:139], a[96:99], v[192:195]\n\t"
          "s_waitcnt lgkmcnt(4)\n\t"
          "v_mfma_f32_16x16x32_bf16 v[96:99], a[144:147], a[96:99], v[96:99]\n\t"
          "ds_read_b64_tr_b16 a[128:129], v7 offset:28832\n\t"
          "buffer_load_dwordx4 a[48:51], %[vx], %[ws], s48 offen offset:0 sc1\n\t"
          "v_mfma_f32_16x16x32_bf16 v[96:99], a[148:151], a[112:115], v[96:99]\n\t"
          "v_mfma_f32_16x16x32_bf16 v[100:103], a[148:151], a[116:119], v[100:103]\n\t"
          "ds_read_b64_tr_b16 a[130:131], v7 offset:32416\n\t"
          "v_mfma_f32_16x16x32_bf16 v[100:103], a[144:147], a[100:103], v[100:103]\n\t"
          "v_mfma_f32_16x16x32_bf16 v[104:107], a[144:147], a[104:107], v[104:107]\n\t"
          "ds_read_b64_tr_b16 a[132:133], v7 offset:43168\n\t"
          "v_mfma_f32_16x16x32_bf16 v[104:107], a[148:151], a[120:123], v[104:107]\n\t"
          "v_mfma_f32_16x16x32_bf16 v[108:111], a[148:151], a[124:127], v[108:111]\n\t"
          "ds_read_b64_tr_b16 a[134:135], v7 offset:46752\n\t"
          "v_mfma_f32_16x16x32_bf16 v[108:111], a[144:147], a[108:111], v[108:111]\n\t"
          "buffer_load_dwordx4 v13, %[srdW1], s45 offen offset:1024 lds\n\t"
          "s_waitcnt lgkmcnt(4)\n\t"
          "v_mfma_f32_16x16x32_bf16 v[220:223], a[152:155], a[108:111], v[220:223]\n\t"
          "ds_read_b64_tr_b16 a[136:137], v7 offset:36000\n\t"
          "v_mfma_f32_16x16x32_bf16 v[220:223], a[156:159], a[124:127], v[220:223]\n\t"
          "v_mfma_f32_16x16x32_bf16 v[216:219], a[156:159], a[120:123], v[216:219]\n\t"
          "ds_read_b64_tr_b16 a[138:139], v7 offset:39584\n\t"
          "v_mfma_f32_16x16x32_bf16 v[216:219], a[152:155], a[104:107], v[216:219]\n\t"
          "v_mfma_f32_16x16x32_bf16 v[212:215], a[152:155], a[100:103], v[212:215]\n\t"
          "ds_read_b64_tr_b16 a[140:141], v7 offset:50336\n\t"
          "v_mfma_f32_16x16x32_bf16 v[212:215], a[156:159], a[116:119], v[212:215]\n\t"
          "v_mfma_f32_16x16x32_bf16 v[208:211], a[156:159], a[112:115], v[208:211]\n\t"
          "ds_read_b64_tr_b16 a[142:143], v7 offset:53920\n\t"
          "buffer_load_dwordx4 a[52:55], %[vx], %[ws], s48 offen offset:1024 sc1\n\t"
          "v_mfma_f32_16x16x32_bf16 v[208:211], a[152:155], a[96:99], v[208:211]\n\t"
          "s_waitcnt lgkmcnt(4)\n\t"
          "v_mfma_f32_16x16x32_bf16 v[112:115], a[128:131], a[96:99], v[112:115]\n\t"
          "ds_read_b64_tr_b16 a[144:145], v7 offset:28864\n\t"
          "v_mfma_f32_16x16x32_bf16 v[112:115], a[132:135], a[112:115], v[112:115]\n\t"
          "v_mfma_f32_16x16x32_bf16 v[116:119], a[132:135], a[116:119], v[116:119]\n\t"
          "ds_read_b64_tr_b16 a[146:147], v7 offset:32448\n\t"
          "v_mfma_f32_16x16x32_bf16 v[116:119], a[128:131], a[100:103], v[116:119]\n\t"
          "v_mfma_f32_16x16x32_bf16 v[120:123], a[128:131], a[104:107], v[120:123]\n\t"
          "ds_read_b64_tr_b16 a[148:149], v7 offset:43200\n\t"
          "v_mfma_f32_16x16x32_bf16 v[120:123], a[132:135], a[120:123], v[120:123]\n\t"
          "buffer_load_dwordx4 v14, %[srdW1], s45 offen offset:2048 lds\n\t"
          "s_add_u32 s45, s45, 458752\n\t"
          "v_mfma_f32_16x16x32_bf16 v[124:127], a[132:135], a[124:127], v[124:127]\n\t"
          "ds_read_b64_tr_b16 a[150:151], v7 offset:46784\n\t"
          "v_mfma_f32_16x16x32_bf16 v[124:127], a[128:131], a[108:111], v[124:127]\n\t"
          "s_waitcnt lgkmcnt(4)\n\t"
          "v_mfma_f32_16x16x32_bf16 v[236:239], a[136:139], a[108:111], v[236:239]\n\t"
          "ds_read_b64_tr_b16 a[152:153], v7 offset:36032\n\t"
          "v_mfma_f32_16x16x32_bf16 v[236:239], a[140:143], a[124:127], v[236:239]\n\t"
          "v_mfma_f32_16x16x32_bf16 v[232:235], a[140:143], a[120:123], v[232:235]\n\t"
          "ds_read_b64_tr_b16 a[154:155], v7 offset:39616\n\t"
          "v_mfma_f32_16x16x32_bf16 v[232:235], a[136:139], a[104:107], v[232:235]\n\t"
          "v_mfma_f32_16x16x32_bf16 v[228:231], a[136:139], a[100:103], v[228:231]\n\t"
          "ds_read_b64_tr_b16 a[156:157], v7 offset:50368\n\t"
          "buffer_load_dwordx4 a[56:59], %[vx], %[ws], s48 offen offset:2048 sc1\n\t"
          "v_mfma_f32_16x16x32_bf16 v[228:231], a[140:143], a[116:119], v[228:231]\n\t"
          "v_mfma_f32_16x16x32_bf16 v[224:227], a[140:143], a[112:115], v[224:227]\n\t"
          "ds_read_b64_tr_b16 a[158:159], v7 offset:53952\n\t"
          "v_mfma_f32_16x16x32_bf16 v[224:227], a[136:139], a[96:99], v[224:227]\n\t"
          "s_waitcnt vmcnt(16)\n\t"
          "s_barrier\n\t"
          "s_waitcnt lgkmcnt(4)\n\t"
          "v_mfma_f32_16x16x32_bf16 v[128:131], a[144:147], a[96:99], v[128:131]\n\t"
          "ds_read_b64_tr_b16 a[128:129], v6 offset:0\n\t"
          "v_mfma_f32_16x16x32_bf16 v[128:131], a[148:151], a[112:115], v[128:131]\n\t"
          "v_mfma_f32_16x16x32_bf16 v[132:135], a[148:151], a[116:119], v[132:135]\n\t"
          "ds_read_b64_tr_b16 a[130:131], v6 offset:3584\n\t"
          "v_mfma_f32_16x16x32_bf16 v[132:135], a[144:147], a[100:103], v[132:135]\n\t"
          "buffer_load_dwordx4 a[60:63], %[vx], %[ws], s48 offen offset:3072 sc1\n\t"
          "s_add_u32 s44, s44, 8192\n\t"
          "s_add_u32 s48, s48, 8192\n\t"
          "v_mfma_f32_16x16x32_bf16 v[136:139], a[144:147], a[104:107], v[136:139]\n\t"
          "ds_read_b64_tr_b16 a[132:133], v6 offset:14336\n\t"
          "v_mfma_f32_16x16x32_bf16 v[136:139], a[148:151], a[120:123], v[136:139]\n\t"
          "v_mfma_f32_16x16x32_bf16 v[140:143], a[148:151], a[124:127], v[140:143]\n\t"
          "ds_read_b64_tr_b16 a[134:135], v6 offset:17920\n\t"
          "v_mfma_f32_16x16x32_bf16 v[140:143], a[144:147], a[108:111], v[140:143]\n\t"
          "s_waitcnt lgkmcnt(4)\n\t"
          "v_mfma_f32_16x16x32_bf16 v[252:255], a[152:155], a[108:111], v[252:255]\n\t"
          "ds_read_b64_tr_b16 a[136:137], v6 offset:7168\n\t"
          "v_mfma_f32_16x16x32_bf16 v[252:255], a[156:159], a[124:127], v[252:255]\n\t"
          "v_mfma_f32_16x16x32_bf16 v[248:251], a[156:159], a[120:123], v[248:251]\n\t"
          "ds_read_b64_tr_b16 a[138:139], v6 offset:10752\n\t"
          "v_mfma_f32_16x16x32_bf16 v[248:251], a[152:155], a[104:107], v[248:251]\n\t"
          "v_mfma_f32_16x16x32_bf16 v[244:247], a[152:155], a[100:103], v[244:247]\n\t"
          "ds_read_b64_tr_b16 a[140:141], v6 offset:21504\n\t"
          "v_mfma_f32_16x16x32_bf16 v[244:247], a[156:159], a[116:119], v[244:247]\n\t"
          "v_mfma_f32_16x16x32_bf16 v[240:243], a[156:159], a[112:115], v[240:243]\n\t"
          "ds_read_b64_tr_b16 a[142:143], v6 offset:25088\n\t"
          "v_mfma_f32_16x16x32_bf16 v[240:243], a[152:155], a[96:99], v[240:243]\n\t"
          "s_waitcnt vmcnt(15)\n\t"
          "s_waitcnt lgkmcnt(4)\n\t"
          "v_mfma_f32_16x16x32_bf16 v[32:35], a[128:131], a[0:3], v[32:35]\n\t"
          "ds_read_b64_tr_b16 a[144:145], v6 offset:32\n\t"
          "v_mfma_f32_16x16x32_bf16 v[32:35], a[132:135], a[16:19], v[32:35]\n\t"
          "s_add_u32 m0, s46, 57344\n\t"
          "v_mfma_f32_16x16x32_bf16 v[36:39], a[132:135], a[20:23], v[36:39]\n\t"
          "ds_read_b64_tr_b16 a[146:147], v6 offset:3616\n\t"
          "buffer_load_dwordx4 v8, %[srdW1], s45 offen offset:0 lds\n\t"
          "v_mfma_f32_16x16x32_bf16 v[36:39], a[128:131], a[4:7], v[36:39]\n\t"
          "v_mfma_f32_16x16x32_bf16 v[40:43], a[128:131], a[8:11], v[40:43]\n\t"
          "ds_read_b64_tr_b16 a[148:149], v6 offset:14368\n\t"
          "v_mfma_f32_16x16x32_bf16 v[40:43], a[132:135], a[24:27], v[40:43]\n\t"
          "v_mfma_f32_16x16x32_bf16 v[44:47], a[132:135], a[28:31], v[44:47]\n\t"
          "ds_read_b64_tr_b16 a[150:151], v6 offset:17952\n\t"
          "v_mfma_f32_16x16x32_bf16 v[44:47], a[128:131], a[12:15], v[44:47]\n\t"
          "s_waitcnt lgkmcnt(4)\n\t"
          "v_mfma_f32_16x16x32_bf16 v[156:159], a[136:139], a[12:15], v[156:159]\n\t"
          "ds_read_b64_tr_b16 a[152:153], v6 offset:7200\n\t"
          "buffer_load_dwordx4 a[64:67], %[vx], %[ws], s44 offen offset:0 sc1\n\t"
          "v_mfma_f32_16x16x32_bf16 v[156:159], a[140:143], a[28:31], v[156:159]\n\t"
          "v_mfma_f32_16x16x32_bf16 v[152:155], a[140:143], a[24:27], v[152:155]\n\t"
          "ds_read_b64_tr_b16 a[154:155], v6 offset:10784\n\t"
          "v_mfma_f32_16x16x32_bf16 v[152:155], a[136:139], a[8:11], v[152:155]\n\t"
          "v_mfma_f32_16x16x32_bf16 v[148:151], a[136:139], a[4:7], v[148:151]\n\t"
          "ds_read_b64_tr_b16 a[156:157], v6 offset:21536\n\t"
          "v_mfma_f32_16x16x32_bf16 v[148:151], a[140:143], a[20:23], v[148:151]\n\t"
          "v_mfma_f32_16x16x32_bf16 v[144:147], a[140:143], a[16:19], v[144:147]\n\t"
          "ds_read_b64_tr_b16 a[158:159], v6 offset:25120\n\t"
          "v_mfma_f32_16x16x32_bf16 v[144:147], a[136:139], a[0:3], v[144:147]\n\t"
          "buffer_load_dwordx4 v9, %[srdW1], s45 offen offset:1024 lds\n\t"
          "s_waitcnt lgkmcnt(4)\n\t"
          "v_mfma_f32_16x16x32_bf16 v[48:51], a[144:147], a[0:3], v[48:51]\n\t"
          "ds_read_b64_tr_b16 a[128:129], v6 offset:64\n\t"
          "v_mfma_f32_16x16x32_bf16 v[48:51], a[148:151], a[16:19], v[48:51]\n\t"
          "v_mfma_f32_16x16x32_bf16 v[52:55], a[148:151], a[20:23], v[52:55]\n\t"
          "ds_read_b64_tr_b16 a[130:131], v6 offset:3648\n\t"
          "v_mfma_f32_16x16x32_bf16 v[52:55], a[144:147], a[4:7], v[52:55]\n\t"
          "v_mfma_f32_16x16x32_bf16 v[56:59], a[144:147], a[8:11], v[56:59]\n\t"
          "ds_read_b64_tr_b16 a[132:133], v6 offset:14400\n\t"
          "v_mfma_f32_16x16x32_bf16 v[56:59], a[148:151], a[24:27], v[56:59]\n\t"
          "v_mfma_f32_16x16x32_bf16 v[60:63], a[148:151], a[28:31], v[60:63]\n\t"
          "ds_read_b64_tr_b16 a[134:135], v6 offset:17984\n\t"
          "buffer_load_dwordx4 a[68:71], %[vx], %[ws], s44 offen offset:1024 sc1\n\t"
          "v_mfma_f32_16x16x32_bf16 v[60:63], a[144:147], a[12:15], v[60:63]\n\t"
          "s_waitcnt lgkmcnt(4)\n\t"
          "v_mfma_f32_16x16x32_bf16 v[172:175], a[152:155], a[12:15], v[172:175]\n\t"
          "ds_read_b64_tr_b16 a[136:137], v6 offset:7232\n\t"
          "v_mfma_f32_16x16x32_bf16 v[172:175], a[156:159], a[28:31], v[172:175]\n\t"
          "v_mfma_f32_16x16x32_bf16 v[168:171], a[156:159], a[24:27], v[168:171]\n\t"
          "ds_read_b64_tr_b16 a[138:139], v6 offset:10816\n\t"
          "v_mfma_f32_16x16x32_bf16 v[168:171], a[152:155], a[8:11], v[168:171]\n\t"
          "v_mfma_f32_16x16x32_bf16 v[164:167], a[152:155], a[4:7], v[164:167]\n\t"
          "ds_read_b64_tr_b16 a[140:141], v6 offset:21568\n\t"
          "v_mfma_f32_16x16x32_bf16 v[164:167], a[156:159], a[20:23], v[164:167]\n\t"
          "buffer_load_dwordx4 v10, %[srdW1], s45 offen offset:2048 lds\n\t"
          "v_mfma_f32_16x16x32_bf16 v[160:163], a[156:159], a[16:19], v[160:163]\n\t"
          "ds_read_b64_tr_b16 a[142:143], v6 offset:25152\n\t"
          "v_mfma_f32_16x16x32_bf16 v[160:163], a[152:155], a[0:3], v[160:163]\n\t"
          "s_waitcnt lgkmcnt(4)\n\t"
          "v_mfma_f32_16x16x32_bf16 v[64:67], a[128:131], a[0:3], v[64:67]\n\t"
          "ds_read_b64_tr_b16 a[144:145], v6 offset:96\n\t"
          "v_mfma_f32_16x16x32_bf16 v[64:67], a[132:135], a[16:19], v[64:67]\n\t"
          "v_mfma_f32_16x16x32_bf16 v[68:71], a[132:135], a[20:23], v[68:71]\n\t"
          "ds_read_b64_tr_b16 a[146:147], v6 offset:3680\n\t"
          "v_mfma_f32_16x16x32_bf16 v[68:71], a[128:131], a[4:7], v[68:71]\n\t"
          "v_mfma_f32_16x16x32_bf16 v[72:75], a[128:131], a[8:11], v[72:75]\n\t"
          "ds_read_b64_tr_b16 a[148:149], v6 offset:14432\n\t"
          "buffer_load_dwordx4 a[72:75], %[vx], %[ws], s44 offen offset:2048 sc1\n\t"
          "v_mfma_f32_16x16x32_bf16 v[72:75], a[132:135], a[24:27], v[72:75]\n\t"
          "v_mfma_f32_16x16x32_bf16 v[76:79], a[132:135], a[28:31], v[76:79]\n\t"
          "ds_read_b64_tr_b16 a[150:151], v6 offset:18016\n\t"
          "v_mfma_f32_16x16x32_bf16 v[76:79], a[128:131], a[12:15], v[76:79]\n\t"
          "s_waitcnt lgkmcnt(4)\n\t"
          "v_mfma_f32_16x16x32_bf16 v[188:191], a[136:139], a[12:15], v[188:191]\n\t"
          "ds_read_b64_tr_b16 a[152:153], v6 offset:7264\n\t"
          "v_mfma_f32_16x16x32_bf16 v[188:191], a[140:143], a[28:31], v[188:191]\n\t"
          "v_mfma_f32_16x16x32_bf16 v[184:187], a[140:143], a[24:27], v[184:187]\n\t"
          "ds_read_b64_tr_b16 a[154:155], v6 offset:10848\n\t"
          "v_mfma_f32_16x16x32_bf16 v[184:187], a[136:139], a[8:11], v[184:187]\n\t"
          "buffer_load_dwordx4 v11, %[srdW1], s45 offen offset:3072 lds\n\t"
          "v_mfma_f32_16x16x32_bf16 v[180:183], a[136:139], a[4:7], v[180:183]\n\t"
          "ds_read_b64_tr_b16 a[156:157], v6 offset:21600\n\t"
          "v_mfma_f32_16x16x32_bf16 v[180:183], a[140:143], a[20:23], v[180:183]\n\t"
          "v_mfma_f32_16x16x32_bf16 v[176:179], a[140:143], a[16:19], v[176:179]\n\t"
          "ds_read_b64_tr_b16 a[158:159], v6 offset:25184\n\t"
          "v_mfma_f32_16x16x32_bf16 v[176:179], a[136:139], a[0:3], v[176:179]\n\t"
          "s_waitcnt lgkmcnt(4)\n\t"
          "v_mfma_f32_16x16x32_bf16 v[80:83], a[144:147], a[0:3], v[80:83]\n\t"
          "ds_read_b64_tr_b16 a[128:129], v6 offset:128\n\t"
          "v_mfma_f32_16x16x32_bf16 v[80:83], a[148:151], a[16:19], v[80:83]\n\t"
          "v_mfma_f32_16x16x32_bf16 v[84:87], a[148:151], a[20:23], v[84:87]\n\t"
          "ds_read_b64_tr_b16 a[130:131], v6 offset:3712\n\t"
          "buffer_load_dwordx4 a[76:79], %[vx], %[ws], s44 offen offset:3072 sc1\n\t"
          "v_mfma_f32_16x16x32_bf16 v[84:87], a[144:147], a[4:7], v[84:87]\n\t"
          "v_mfma_f32_16x16x32_bf16 v[88:91], a[144:147], a[8:11], v[88:91]\n\t"
          "ds_read_b64_tr_b16 a[132:133], v6 offset:14464\n\t"
          "v_mfma_f32_16x16x32_bf16 v[88:91], a[148:151], a[24:27], v[88:91]\n\t"
          "v_mfma_f32_16x16x32_bf16 v[92:95], a[148:151], a[28:31], v[92:95]\n\t"
          "ds_read_b64_tr_b16 a[134:135], v6 offset:18048\n\t"
          "v_mfma_f32_16x16x32_bf16 v[92:95], a[144:147], a[12:15], v[92:95]\n\t"
          "s_waitcnt lgkmcnt(4)\n\t"
          "v_mfma_f32_16x16x32_bf16 v[204:207], a[152:155], a[12:15], v[204:207]\n\t"
          "ds_read_b64_tr_b16 a[136:137], v6 offset:7296\n\t"
          "s_add_u32 m0, s46, 61440\n\t"
          "v_mfma_f32_16x16x32_bf16 v[204:207], a[156:159], a[28:31], v[204:207]\n\t"
          "buffer_load_dwordx4 v12, %[srdW1], s45 offen offset:0 lds\n\t"
          "v_mfma_f32_16x16x32_bf16 v[200:203], a[156:159], a[24:27], v[200:203]\n\t"
          "ds_read_b64_tr_b16 a[138:139], v6 offset:10880\n\t"
          "v_mfma_f32_16x16x32_bf16 v[200:203], a[152:155], a[8:11], v[200:203]\n\t"
          "v_mfma_f32_16x16x32_bf16 v[196:199], a[152:155], a[4:7], v[196:199]\n\t"
          "ds_read_b64_tr_b16 a[140:141], v6 offset:21632\n\t"
          "v_mfma_f32_16x16x32_bf16 v[196:199], a[156:159], a[20:23], v[196:199]\n\t"
          "v_mfma_f32_16x16x32_bf16 v[192:195], a[156:159], a[16:19], v[192:195]\n\t"
          "ds_read_b64_tr_b16 a[142:143], v6 offset:25216\n\t"
          "v_mfma_f32_16x16x32_bf16 v[192:195], a[152:155], a[0:3], v[192:195]\n\t"
          "s_waitcnt lgkmcnt(4)\n\t"
          "v_mfma_f32_16x16x32_bf16 v[96:99], a[128:131], a[0:3], v[96:99]\n\t"
          "ds_read_b64_tr_b16 a[144:145], v6 offset:160\n\t"
          "buffer_load_dwordx4 a[80:83], %[vx], %[ws], s48 offen offset:0 sc1\n\t"
          "v_mfma_f32_16x16x32_bf16 v[96:99], a[132:135], a[16:19], v[96:99]\n\t"
          "v_mfma_f32_16x16x32_bf16 v[100:103], a[132:135], a[20:23], v[100:103]\n\t"
          "ds_read_b64_tr_b16 a[146:147], v6 offset:3744\n\t"
          "v_mfma_f32_16x16x32_bf16 v[100:103], a[128:131], a[4:7], v[100:103]\n\t"
          "v_mfma_f32_16x16x32_bf16 v[104:107], a[128:131], a[8:11], v[104:107]\n\t"
          "ds_read_b64_tr_b16 a[148:149], v6 offset:14496\n\t"
          "v_mfma_f32_16x16x32_bf16 v[104:107], a[132:135], a[24:27], v[104:107]\n\t"
          "v_mfma_f32_16x16x32_bf16 v[108:111], a[132:135], a[28:31], v[108:111]\n\t"
          "ds_read_b64_tr_b16 a[150:151], v6 offset:18080\n\t"
          "v_mfma_f32_16x16x32_bf16 v[108:111], a[128:131], a[12:15], v[108:111]\n\t"
          "buffer_load_dwordx4 v13, %[srdW1], s45 offen offset:1024 lds\n\t"
          "s_waitcnt lgkmcnt(4)\n\t"
          "v_mfma_f32_16x16x32_bf16 v[220:223], a[136:139], a[12:15], v[220:223]\n\t"
          "ds_read_b64_tr_b16 a[152:153], v6 offset:7328\n\t"
          "v_mfma_f32_16x16x32_bf16 v[220:223], a[140:143], a[28:31], v[220:223]\n\t"
          "v_mfma_f32_16x16x32_bf16 v[216:219], a[140:143], a[24:27], v[216:219]\n\t"
          "ds_read_b64_tr_b16 a[154:155], v6 offset:10912\n\t"
          "v_mfma_f32_16x16x32_bf16 v[216:219], a[136:139], a[8:11], v[216:219]\n\t"
          "v_mfma_f32_16x16x32_bf16 v[212:215], a[136:139], a[4:7], v[212:215]\n\t"
          "ds_read_b64_tr_b16 a[156:157], v6 offset:21664\n\t"
          "v_mfma_f32_16x16x32_bf16 v[212:215], a[140:143], a[20:23], v[212:215]\n\t"
          "v_mfma_f32_16x16x32_bf16 v[208:211], a[140:143], a[16:19], v[208:211]\n\t"
          "ds_read_b64_tr_b16 a[158:159], v6 offset:25248\n\t"
          "buffer_load_dwordx4 a[84:87], %[vx], %[ws], s48 offen offset:1024 sc1\n\t"
          "v_mfma_f32_16x16x32_bf16 v[208:211], a[136:139], a[0:3], v[208:211]\n\t"
          "s_waitcnt lgkmcnt(4)\n\t"
          "v_mfma_f32_16x16x32_bf16 v[112:115], a[144:147], a[0:3], v[112:115]\n\t"
          "ds_read_b64_tr_b16 a[128:129], v6 offset:192\n\t"
          "v_mfma_f32_16x16x32_bf16 v[112:115], a[148:151], a[16:19], v[112:115]\n\t"
          "v_mfma_f32_16x16x32_bf16 v[116:119], a[148:151], a[20:23], v[116:119]\n\t"
          "ds_read_b64_tr_b16 a[130:131], v6 offset:3776\n\t"
          "v_mfma_f32_16x16x32_bf16 v[116:119], a[144:147], a[4:7], v[116:119]\n\t"
          "v_mfma_f32_16x16x32_bf16 v[120:123], a[144:147], a[8:11], v[120:123]\n\t"
          "ds_read_b64_tr_b16 a[132:133], v6 offset:14528\n\t"
          "v_mfma_f32_16x16x32_bf16 v[120:123], a[148:151], a[24:27], v[120:123]\n\t"
          "buffer_load_dwordx4 v14, %[srdW1], s45 offen offset:2048 lds\n\t"
          "s_add_u32 s45, s45, 458752\n\t"
          "v_mfma_f32_16x16x32_bf16 v[124:127], a[148:151], a[28:31], v[124:127]\n\t"
          "ds_read_b64_tr_b16 a[134:135], v6 offset:18112\n\t"
          "v_mfma_f32_16x16x32_bf16 v[124:127], a[144:147], a[12:15], v[124:127]\n\t"
          "s_waitcnt lgkmcnt(4)\n\t"
          "v_mfma_f32_16x16x32_bf16 v[236:239], a[152:155], a[12:15], v[236:239]\n\t"
          "ds_read_b64_tr_b16 a[136:137], v6 offset:7360\n\t"
          "v_mfma_f32_16x16x32_bf16 v[236:239], a[156:159], a[28:31], v[236:239]\n\t"
          "v_mfma_f32_16x16x32_bf16 v[232:235], a[156:159], a[24:27], v[232:235]\n\t"
          "ds_read_b64_tr_b16 a[138:139], v6 offset:10944\n\t"
          "v_mfma_f32_16x16x32_bf16 v[232:235], a[152:155], a[8:11], v[232:235]\n\t"
          "v_mfma_f32_16x16x32_bf16 v[228:231], a[152:155], a[4:7], v[228:231]\n\t"
          "ds_read_b64_tr_b16 a[140:141], v6 offset:21696\n\t"
          "buffer_load_dwordx4 a[88:91], %[vx], %[ws], s48 offen offset:2048 sc1\n\t"
          "v_mfma_f32_16x16x32_bf16 v[228:231], a[156:159], a[20:23], v[228:231]\n\t"
          "v_mfma_f32_16x16x32_bf16 v[224:227], a[156:159], a[16:19], v[224:227]\n\t"
          "ds_read_b64_tr_b16 a[142:143], v6 offset:25280\n\t"
          "v_mfma_f32_16x16x32_bf16 v[224:227], a[152:155], a[0:3], v[224:227]\n\t"
          "s_waitcnt vmcnt(16)\n\t"
          "s_barrier\n\t"
          "s_waitcnt lgkmcnt(4)\n\t"
          "v_mfma_f32_16x16x32_bf16 v[128:131], a[128:131], a[0:3], v[128:131]\n\t"
          "ds_read_b64_tr_b16 a[144:145], v6 offset:28672\n\t"
          "v_mfma_f32_16x16x32_bf16 v[128:131], a[132:135], a[16:19], v[128:131]\n\t"
          "v_mfma_f32_16x16x32_bf16 v[132:135], a[132:135], a[20:23], v[132:135]\n\t"
          "ds_read_b64_tr_b16 a[146:147], v6 offset:32256\n\t"
          "v_mfma_f32_16x16x32_bf16 v[132:135], a[128:131], a[4:7], v[132:135]\n\t"
          "buffer_load_dwordx4 a[92:95], %[vx], %[ws], s48 offen offset:3072 sc1\n\t"
          "s_add_u32 s44, s44, 8192\n\t"
          "s_add_u32 s48, s48, 8192\n\t"
          "v_mfma_f32_16x16x32_bf16 v[136:139], a[128:131], a[8:11], v[136:139]\n\t"
          "ds_read_b64_tr_b16 a[148:149], v6 offset:43008\n\t"
          "v_mfma_f32_16x16x32_bf16 v[136:139], a[132:135], a[24:27], v[136:139]\n\t"
          "v_mfma_f32_16x16x32_bf16 v[140:143], a[132:135], a[28:31], v[140:143]\n\t"
          "ds_read_b64_tr_b16 a[150:151], v6 offset:46592\n\t"
          "v_mfma_f32_16x16x32_bf16 v[140:143], a[128:131], a[12:15], v[140:143]\n\t"
          "s_waitcnt lgkmcnt(4)\n\t"
          "v_mfma_f32_16x16x32_bf16 v[252:255], a[136:139], a[12:15], v[252:255]\n\t"
          "ds_read_b64_tr_b16 a[152:153], v6 offset:35840\n\t"
          "v_mfma_f32_16x16x32_bf16 v[252:255], a[140:143], a[28:31], v[252:255]\n\t"
          "v_mfma_f32_16x16x32_bf16 v[248:251], a[140:143], a[24:27], v[248:251]\n\t"
          "ds_read_b64_tr_b16 a[154:155], v6 offset:39424\n\t"
          "v_mfma_f32_16x16x32_bf16 v[248:251], a[136:139], a[8:11], v[248:251]\n\t"
          "v_mfma_f32_16x16x32_bf16 v[244:247], a[136:139], a[4:7], v[244:247]\n\t"
          "ds_read_b64_tr_b16 a[156:157], v6 offset:50176\n\t"
          "v_mfma_f32_16x16x32_bf16 v[244:247], a[140:143], a[20:23], v[244:247]\n\t"
          "v_mfma_f32_16x16x32_bf16 v[240:243], a[140:143], a[16:19], v[240:243]\n\t"
          "ds_read_b64_tr_b16 a[158:159], v6 offset:53760\n\t"
          "v_mfma_f32_16x16x32_bf16 v[240:243], a[136:139], a[0:3], v[240:243]\n\t"
          "s_waitcnt vmcnt(15)\n\t"
          "s_waitcnt lgkmcnt(4)\n\t"
          "v_mfma_f32_16x16x32_bf16 v[32:35], a[144:147], a[32:35], v[32:35]\n\t"
          "ds_read_b64_tr_b16 a[128:129], v6 offset:28704\n\t"
          "v_mfma_f32_16x16x32_bf16 v[32:35], a[148:151], a[48:51], v[32:35]\n\t"
          "s_add_u32 m0, s46, 86016\n\t"
          "v_mfma_f32_16x16x32_bf16 v[36:39], a[148:151], a[52:55], v[36:39]\n\t"
          "ds_read_b64_tr_b16 a[130:131], v6 offset:32288\n\t"
          "buffer_load_dwordx4 v8, %[srdW1], s45 offen offset:0 lds\n\t"
          "v_mfma_f32_16x16x32_bf16 v[36:39], a[144:147], a[36:39], v[36:39]\n\t"
          "v_mfma_f32_16x16x32_bf16 v[40:43], a[144:147], a[40:43], v[40:43]\n\t"
          "ds_read_b64_tr_b16 a[132:133], v6 offset:43040\n\t"
          "v_mfma_f32_16x16x32_bf16 v[40:43], a[148:151], a[56:59], v[40:43]\n\t"
          "v_mfma_f32_16x16x32_bf16 v[44:47], a[148:151], a[60:63], v[44:47]\n\t"
          "ds_read_b64_tr_b16 a[134:135], v6 offset:46624\n\t"
          "v_mfma_f32_16x16x32_bf16 v[44:47], a[144:147], a[44:47], v[44:47]\n\t"
          "s_waitcnt lgkmcnt(4)\n\t"
          "v_mfma_f32_16x16x32_bf16 v[156:159], a[152:155], a[44:47], v[156:159]\n\t"
          "ds_read_b64_tr_b16 a[136:137], v6 offset:35872\n\t"
          "buffer_load_dwordx4 a[96:99], %[vx], %[ws], s44 offen offset:0 sc1\n\t"
          "v_mfma_f32_16x16x32_bf16 v[156:159], a[156:159], a[60:63], v[156:159]\n\t"
          "v_mfma_f32_16x16x32_bf16 v[152:155], a[156:159], a[56:59], v[152:155]\n\t"
          "ds_read_b64_tr_b16 a[138:139], v6 offset:39456\n\t"
          "v_mfma_f32_16x16x32_bf16 v[152:155], a[152:155], a[40:43], v[152:155]\n\t"
          "v_mfma_f32_16x16x32_bf16 v[148:151], a[152:155], a[36:39], v[148:151]\n\t"
          "ds_read_b64_tr_b16 a[140:141], v6 offset:50208\n\t"
          "v_mfma_f32_16x16x32_bf16 v[148:151], a[156:159], a[52:55], v[148:151]\n\t"
          "v_mfma_f32_16x16x32_bf16 v[144:147], a[156:159], a[48:51], v[144:147]\n\t"
          "ds_read_b64_tr_b16 a[142:143], v6 offset:53792\n\t"
          "v_mfma_f32_16x16x32_bf16 v[144:147], a[152:155], a[32:35], v[144:147]\n\t"
          "buffer_load_dwordx4 v9, %[srdW1], s45 offen offset:1024 lds\n\t"
          "s_waitcnt lgkmcnt(4)\n\t"
          "v_mfma_f32_16x16x32_bf16 v[48:51], a[128:131], a[32:35], v[48:51]\n\t"
          "ds_read_b64_tr_b16 a[144:145], v6 offset:28736\n\t"
          "v_mfma_f32_16x16x32_bf16 v[48:51], a[132:135], a[48:51], v[48:51]\n\t"
          "v_mfma_f32_16x16x32_bf16 v[52:55], a[132:135], a[52:55], v[52:55]\n\t"
          "ds_read_b64_tr_b16 a[146:147], v6 offset:32320\n\t"
          "v_mfma_f32_16x16x32_bf16 v[52:55], a[128:131], a[36:39], v[52:55]\n\t"
          "v_mfma_f32_16x16x32_bf16 v[56:59], a[128:131], a[40:43], v[56:59]\n\t"
          "ds_read_b64_tr_b16 a[148:149], v6 offset:43072\n\t"
          "v_mfma_f32_16x16x32_bf16 v[56:59], a[132:135], a[56:59], v[56:59]\n\t"
          "v_mfma_f32_16x16x32_bf16 v[60:63], a[132:135], a[60:63], v[60:63]\n\t"
          "ds_read_b64_tr_b16 a[150:151], v6 offset:46656\n\t"
          "buffer_load_dwordx4 a[100:103], %[vx], %[ws], s44 offen offset:1024 sc1\n\t"
          "v_mfma_f32_16x16x32_bf16 v[60:63], a[128:131], a[44:47], v[60:63]\n\t"
          "s_waitcnt lgkmcnt(4)\n\t"
          "v_mfma_f32_16x16x32_bf16 v[172:175], a[136:139], a[44:47], v[172:175]\n\t"
          "ds_read_b64_tr_b16 a[152:153], v6 offset:35904\n\t"
          "v_mfma_f32_16x16x32_bf16 v[172:175], a[140:143], a[60:63], v[172:175]\n\t"
          "v_mfma_f32_16x16x32_bf16 v[168:171], a[140:143], a[56:59], v[168:171]\n\t"
          "ds_read_b64_tr_b16 a[154:155], v6 offset:39488\n\t"
          "v_mfma_f32_16x16x32_bf16 v[168:171], a[136:139], a[40:43], v[168:171]\n\t"
          "v_mfma_f32_16x16x32_bf16 v[164:167], a[136:139], a[36:39], v[164:167]\n\t"
          "ds_read_b64_tr_b16 a[156:157], v6 offset:50240\n\t"
          "v_mfma_f32_16x16x32_bf16 v[164:167], a[140:143], a[52:55], v[164:167]\n\t"
          "buffer_load_dwordx4 v10, %[srdW1], s45 offen offset:2048 lds\n\t"
          "v_mfma_f32_16x16x32_bf16 v[160:163], a[140:143], a[48:51], v[160:163]\n\t"
          "ds_read_b64_tr_b16 a[158:159], v6 offset:53824\n\t"
          "v_mfma_f32_16x16x32_bf16 v[160:163], a[136:139], a[32:35], v[160:163]\n\t"
          "s_waitcnt lgkmcnt(4)\n\t"
          "v_mfma_f32_16x16x32_bf16 v[64:67], a[144:147], a[32:35], v[64:67]\n\t"
          "ds_read_b64_tr_b16 a[128:129], v6 offset:28768\n\t"
          "v_mfma_f32_16x16x32_bf16 v[64:67], a[148:151], a[48:51], v[64:67]\n\t"
          "v_mfma_f32_16x16x32_bf16 v[68:71], a[148:151], a[52:55], v[68:71]\n\t"
          "ds_read_b64_tr_b16 a[130:131], v6 offset:32352\n\t"
          "v_mfma_f32_16x16x32_bf16 v[68:71], a[144:147], a[36:39], v[68:71]\n\t"
          "v_mfma_f32_16x16x32_bf16 v[72:75], a[144:147], a[40:43], v[72:75]\n\t"
          "ds_read_b64_tr_b16 a[132:133], v6 offset:43104\n\t"
          "buffer_load_dwordx4 a[104:107], %[vx], %[ws], s44 offen offset:2048 sc1\n\t"
          "v_mfma_f32_16x16x32_bf16 v[72:75], a[148:151], a[56:59], v[72:75]\n\t"
          "v_mfma_f32_16x16x32_bf16 v[76:79], a[148:151], a[60:63], v[76:79]\n\t"
          "ds_read_b64_tr_b16 a[134:135], v6 offset:46688\n\t"
          "v_mfma_f32_16x16x32_bf16 v[76:79], a[144:147], a[44:47], v[76:79]\n\t"
          "s_waitcnt lgkmcnt(4)\n\t"
          "v_mfma_f32_16x16x32_bf16 v[188:191], a[152:155], a[44:47], v[188:191]\n\t"
          "ds_read_b64_tr_b16 a[136:137], v6 offset:35936\n\t"
          "v_mfma_f32_16x16x32_bf16 v[188:191], a[156:159], a[60:63], v[188:191]\n\t"
          "v_mfma_f32_16x16x32_bf16 v[184:187], a[156:159], a[56:59], v[184:187]\n\t"
          "ds_read_b64_tr_b16 a[138:139], v6 offset:39520\n\t"
          "v_mfma_f32_16x16x32_bf16 v[184:187], a[152:155], a[40:43], v[184:187]\n\t"
          "buffer_load_dwordx4 v11, %[srdW1], s45 offen offset:3072 lds\n\t"
          "v_mfma_f32_16x16x32_bf16 v[180:183], a[152:155], a[36:39], v[180:183]\n\t"
          "ds_read_b64_tr_b16 a[140:141], v6 offset:50272\n\t"
          "v_mfma_f32_16x16x32_bf16 v[180:183], a[156:159], a[52:55], v[180:183]\n\t"
          "v_mfma_f32_16x16x32_bf16 v[176:179], a[156:159], a[48:51], v[176:179]\n\t"
          "ds_read_b64_tr_b16 a[142:143], v6 offset:53856\n\t"
          "v_mfma_f32_16x16x32_bf16 v[176:179], a[152:155], a[32:35], v[176:179]\n\t"
          "s_waitcnt lgkmcnt(4)\n\t"
          "v_mfma_f32_16x16x32_bf16 v[80:83], a[128:131], a[32:35], v[80:83]\n\t"
          "ds_read_b64_tr_b16 a[144:145], v6 offset:28800\n\t"
          "v_mfma_f32_16x16x32_bf16 v[80:83], a[132:135], a[48:51], v[80:83]\n\t"
          "v_mfma_f32_16x16x32_bf16 v[84:87], a[132:135], a[52:55], v[84:87]\n\t"
          "ds_read_b64_tr_b16 a[146:147], v6 offset:32384\n\t"
          "buffer_load_dwordx4 a[108:111], %[vx], %[ws], s44 offen offset:3072 sc1\n\t"
          "v_mfma_f32_16x16x32_bf16 v[84:87], a[128:131], a[36:39], v[84:87]\n\t"
          "v_mfma_f32_16x16x32_bf16 v[88:91], a[128:131], a[40:43], v[88:91]\n\t"
          "ds_read_b64_tr_b16 a[148:149], v6 offset:43136\n\t"
          "v_mfma_f32_16x16x32_bf16 v[88:91], a[132:135], a[56:59], v[88:91]\n\t"
          "v_mfma_f32_16x16x32_bf16 v[92:95], a[132:135], a[60:63], v[92:95]\n\t"
          "ds_read_b64_tr_b16 a[150:151], v6 offset:46720\n\t"
          "v_mfma_f32_16x16x32_bf16 v[92:95], a[128:131], a[44:47], v[92:95]\n\t"
          "s_waitcnt lgkmcnt(4)\n\t"
          "v_mfma_f32_16x16x32_bf16 v[204:207], a[136:139], a[44:47], v[204:207]\n\t"
          "ds_read_b64_tr_b16 a[152:153], v6 offset:35968\n\t"
          "s_add_u32 m0, s46, 90112\n\t"
          "v_mfma_f32_16x16x32_bf16 v[204:207], a[140:143], a[60:63], v[204:207]\n\t"
          "buffer_load_dwordx4 v12, %[srdW1], s45 offen offset:0 lds\n\t"
          "v_mfma_f32_16x16x32_bf16 v[200:203], a[140:143], a[56:59], v[200:203]\n\t"
          "ds_read_b64_tr_b16 a[154:155], v6 offset:39552\n\t"
          "v_mfma_f32_16x16x32_bf16 v[200:203], a[136:139], a[40:43], v[200:203]\n\t"
          "v_mfma_f32_16x16x32_bf16 v[196:199], a[136:139], a[36:39], v[196:199]\n\t"
          "ds_read_b64_tr_b16 a[156:157], v6 offset:50304\n\t"
          "v_mfma_f32_16x16x32_bf16 v[196:199], a[140:143], a[52:55], v[196:199]\n\t"
          "v_mfma_f32_16x16x32_bf16 v[192:195], a[140:143], a[48:51], v[192:195]\n\t"
          "ds_read_b64_tr_b16 a[158:159], v6 offset:53888\n\t"
          "v_mfma_f32_16x16x32_bf16 v[192:195], a[136:139], a[32:35], v[192:195]\n\t"
          "s_waitcnt lgkmcnt(4)\n\t"
          "v_mfma_f32_16x16x32_bf16 v[96:99], a[144:147], a[32:35], v[96:99]\n\t"
          "ds_read_b64_tr_b16 a[128:129], v6 offset:28832\n\t"
          "buffer_load_dwordx4 a[112:115], %[vx], %[ws], s48 offen offset:0 sc1\n\t"
          "v_mfma_f32_16x16x32_bf16 v[96:99], a[148:151], a[48:51], v[96:99]\n\t"
          "v_mfma_f32_16x16x32_bf16 v[100:103], a[148:151], a[52:55], v[100:103]\n\t"
          "ds_read_b64_tr_b16 a[130:131], v6 offset:32416\n\t"
          "v_mfma_f32_16x16x32_bf16 v[100:103], a[144:147], a[36:39], v[100:103]\n\t"
          "v_mfma_f32_16x16x32_bf16 v[104:107], a[144:147], a[40:43], v[104:107]\n\t"
          "ds_read_b64_tr_b16 a[132:133], v6 offset:43168\n\t"
          "v_mfma_f32_16x16x32_bf16 v[104:107], a[148:151], a[56:59], v[104:107]\n\t"
          "v_mfma_f32_16x16x32_bf16 v[108:111], a[148:151], a[60:63], v[108:111]\n\t"
          "ds_read_b64_tr_b16 a[134:135], v6 offset:46752\n\t"
          "v_mfma_f32_16x16x32_bf16 v[108:111], a[144:147], a[44:47], v[108:111]\n\t"
          "buffer_load_dwordx4 v13, %[srdW1], s45 offen offset:1024 lds\n\t"
          "s_waitcnt lgkmcnt(4)\n\t"
          "v_mfma_f32_16x16x32_bf16 v[220:223], a[152:155], a[44:47], v[220:223]\n\t"
          "ds_read_b64_tr_b16 a[136:137], v6 offset:36000\n\t"
          "v_mfma_f32_16x16x32_bf16 v[220:223], a[156:159], a[60:63], v[220:223]\n\t"
          "v_mfma_f32_16x16x32_bf16 v[216:219], a[156:159], a[56:59], v[216:219]\n\t"
          "ds_read_b64_tr_b16 a[138:139], v6 offset:39584\n\t"
          "v_mfma_f32_16x16x32_bf16 v[216:219], a[152:155], a[40:43], v[216:219]\n\t"
          "v_mfma_f32_16x16x32_bf16 v[212:215], a[152:155], a[36:39], v[212:215]\n\t"
          "ds_read_b64_tr_b16 a[140:141], v6 offset:50336\n\t"
          "v_mfma_f32_16x16x32_bf16 v[212:215], a[156:159], a[52:55], v[212:215]\n\t"
          "v_mfma_f32_16x16x32_bf16 v[208:211], a[156:159], a[48:51], v[208:211]\n\t"
          "ds_read_b64_tr_b16 a[142:143], v6 offset:53920\n\t"
          "buffer_load_dwordx4 a[116:119], %[vx], %[ws], s48 offen offset:1024 sc1\n\t"
          "v_mfma_f32_16x16x32_bf16 v[208:211], a[152:155], a[32:35], v[208:211]\n\t"
          "s_waitcnt lgkmcnt(4)\n\t"
          "v_mfma_f32_16x16x32_bf16 v[112:115], a[128:131], a[32:35], v[112:115]\n\t"
          "ds_read_b64_tr_b16 a[144:145], v6 offset:28864\n\t"
          "v_mfma_f32_16x16x32_bf16 v[112:115], a[132:135], a[48:51], v[112:115]\n\t"
          "v_mfma_f32_16x16x32_bf16 v[116:119], a[132:135], a[52:55], v[116:119]\n\t"
          "ds_read_b64_tr_b16 a[146:147], v6 offset:32448\n\t"
          "v_mfma_f32_16x16x32_bf16 v[116:119], a[128:131], a[36:39], v[116:119]\n\t"
          "v_mfma_f32_16x16x32_bf16 v[120:123], a[128:131], a[40:43], v[120:123]\n\t"
          "ds_read_b64_tr_b16 a[148:149], v6 offset:43200\n\t"
          "v_mfma_f32_16x16x32_bf16 v[120:123], a[132:135], a[56:59], v[120:123]\n\t"
          "buffer_load_dwordx4 v14, %[srdW1], s45 offen offset:2048 lds\n\t"
          "s_add_u32 s45, s45, 458752\n\t"
          "v_mfma_f32_16x16x32_bf16 v[124:127], a[132:135], a[60:63], v[124:127]\n\t"
          "ds_read_b64_tr_b16 a[150:151], v6 offset:46784\n\t"
          "v_mfma_f32_16x16x32_bf16 v[124:127], a[128:131], a[44:47], v[124:127]\n\t"
          "s_waitcnt lgkmcnt(4)\n\t"
          "v_mfma_f32_16x16x32_bf16 v[236:239], a[136:139], a[44:47], v[236:239]\n\t"
          "ds_read_b64_tr_b16 a[152:153], v6 offset:36032\n\t"
          "v_mfma_f32_16x16x32_bf16 v[236:239], a[140:143], a[60:63], v[236:239]\n\t"
          "v_mfma_f32_16x16x32_bf16 v[232:235], a[140:143], a[56:59], v[232:235]\n\t"
          "ds_read_b64_tr_b16 a[154:155], v6 offset:39616\n\t"
          "v_mfma_f32_16x16x32_bf16 v[232:235], a[136:139], a[40:43], v[232:235]\n\t"
          "v_mfma_f32_16x16x32_bf16 v[228:231], a[136:139], a[36:39], v[228:231]\n\t"
          "ds_read_b64_tr_b16 a[156:157], v6 offset:50368\n\t"
          "buffer_load_dwordx4 a[120:123], %[vx], %[ws], s48 offen offset:2048 sc1\n\t"
          "v_mfma_f32_16x16x32_bf16 v[228:231], a[140:143], a[52:55], v[228:231]\n\t"
          "v_mfma_f32_16x16x32_bf16 v[224:227], a[140:143], a[48:51], v[224:227]\n\t"
          "ds_read_b64_tr_b16 a[158:159], v6 offset:53952\n\t"
          "v_mfma_f32_16x16x32_bf16 v[224:227], a[136:139], a[32:35], v[224:227]\n\t"
          "s_waitcnt vmcnt(16)\n\t"
          "s_barrier\n\t"
          "s_waitcnt lgkmcnt(4)\n\t"
          "v_mfma_f32_16x16x32_bf16 v[128:131], a[144:147], a[32:35], v[128:131]\n\t"
          "ds_read_b64_tr_b16 a[128:129], v7 offset:0\n\t"
          "v_mfma_f32_16x16x32_bf16 v[128:131], a[148:151], a[48:51], v[128:131]\n\t"
          "v_mfma_f32_16x16x32_bf16 v[132:135], a[148:151], a[52:55], v[132:135]\n\t"
          "ds_read_b64_tr_b16 a[130:131], v7 offset:3584\n\t"
          "v_mfma_f32_16x16x32_bf16 v[132:135], a[144:147], a[36:39], v[132:135]\n\t"
          "buffer_load_dwordx4 a[124:127], %[vx], %[ws], s48 offen offset:3072 sc1\n\t"
          "s_add_u32 s44, s44, 8192\n\t"
          "s_add_u32 s48, s48, 8192\n\t"
          "v_mfma_f32_16x16x32_bf16 v[136:139], a[144:147], a[40:43], v[136:139]\n\t"
          "ds_read_b64_tr_b16 a[132:133], v7 offset:14336\n\t"
          "v_mfma_f32_16x16x32_bf16 v[136:139], a[148:151], a[56:59], v[136:139]\n\t"
          "v_mfma_f32_16x16x32_bf16 v[140:143], a[148:151], a[60:63], v[140:143]\n\t"
          "ds_read_b64_tr_b16 a[134:135], v7 offset:17920\n\t"
          "v_mfma_f32_16x16x32_bf16 v[140:143], a[144:147], a[44:47], v[140:143]\n\t"
          "s_waitcnt lgkmcnt(4)\n\t"
          "v_mfma_f32_16x16x32_bf16 v[252:255], a[152:155], a[44:47], v[252:255]\n\t"
          "ds_read_b64_tr_b16 a[136:137], v7 offset:7168\n\t"
          "v_mfma_f32_16x16x32_bf16 v[252:255], a[156:159], a[60:63], v[252:255]\n\t"
          "v_mfma_f32_16x16x32_bf16 v[248:251], a[156:159], a[56:59], v[248:251]\n\t"
          "ds_read_b64_tr_b16 a[138:139], v7 offset:10752\n\t"
          "v_mfma_f32_16x16x32_bf16 v[248:251], a[152:155], a[40:43], v[248:251]\n\t"
          "v_mfma_f32_16x16x32_bf16 v[244:247], a[152:155], a[36:39], v[244:247]\n\t"
          "ds_read_b64_tr_b16 a[140:141], v7 offset:21504\n\t"
          "v_mfma_f32_16x16x32_bf16 v[244:247], a[156:159], a[52:55], v[244:247]\n\t"
          "v_mfma_f32_16x16x32_bf16 v[240:243], a[156:159], a[48:51], v[240:243]\n\t"
          "ds_read_b64_tr_b16 a[142:143], v7 offset:25088\n\t"
          "v_mfma_f32_16x16x32_bf16 v[240:243], a[152:155], a[32:35], v[240:243]\n\t"
          "s_sub_u32 s47, s47, 1\n\t"
          "s_cmp_lg_u32 s47, 0\n\t"
          "s_cbranch_scc1 1b\n\t"
          "s_waitcnt vmcnt(15)\n\t"
          "s_waitcnt lgkmcnt(4)\n\t"
          "v_mfma_f32_16x16x32_bf16 v[32:35], a[128:131], a[64:67], v[32:35]\n\t"
          "ds_read_b64_tr_b16 a[144:145], v7 offset:32\n\t"
          "v_mfma_f32_16x16x32_bf16 v[32:35], a[132:135], a[80:83], v[32:35]\n\t"
          "v_mfma_f32_16x16x32_bf16 v[36:39], a[132:135], a[84:87], v[36:39]\n\t"
          "ds_read_b64_tr_b16 a[146:147], v7 offset:3616\n\t"
          "v_mfma_f32_16x16x32_bf16 v[36:39], a[128:131], a[68:71], v[36:39]\n\t"
          "v_mfma_f32_16x16x32_bf16 v[40:43], a[128:131], a[72:75], v[40:43]\n\t"
          "ds_read_b64_tr_b16 a[148:149], v7 offset:14368\n\t"
          "v_mfma_f32_16x16x32_bf16 v[40:43], a[132:135], a[88:91], v[40:43]\n\t"
          "v_mfma_f32_16x16x32_bf16 v[44:47], a[132:135], a[92:95], v[44:47]\n\t"
          "ds_read_b64_tr_b16 a[150:151], v7 offset:17952\n\t"
          "v_mfma_f32_16x16x32_bf16 v[44:47], a[128:131], a[76:79], v[44:47]\n\t"
          "s_waitcnt lgkmcnt(4)\n\t"
          "v_mfma_f32_16x16x32_bf16 v[156:159], a[136:139], a[76:79], v[156:159]\n\t"
          "ds_read_b64_tr_b16 a[152:153], v7 offset:7200\n\t"
          "v_mfma_f32_16x16x32_bf16 v[156:159], a[140:143], a[92:95], v[156:159]\n\t"
          "v_mfma_f32_16x16x32_bf16 v[152:155], a[140:143], a[88:91], v[152:155]\n\t"
          "ds_read_b64_tr_b16 a[154:155], v7 offset:10784\n\t"
          "v_mfma_f32_16x16x32_bf16 v[152:155], a[136:139], a[72:75], v[152:155]\n\t"
          "v_mfma_f32_16x16x32_bf16 v[148:151], a[136:139], a[68:71], v[148:151]\n\t"
          "ds_read_b64_tr_b16 a[156:157], v7 offset:21536\n\t"
          "v_mfma_f32_16x16x32_bf16 v[148:151], a[140:143], a[84:87], v[148:151]\n\t"
          "v_mfma_f32_16x16x32_bf16 v[144:147], a[140:143], a[80:83], v[144:147]\n\t"
          "ds_read_b64_tr_b16 a[158:159], v7 offset:25120\n\t"
          "v_mfma_f32_16x16x32_bf16 v[144:147], a[136:139], a[64:67], v[144:147]\n\t"
          "s_waitcnt lgkmcnt(4)\n\t"
          "v_mfma_f32_16x16x32_bf16 v[48:51], a[144:147], a[64:67], v[48:51]\n\t"
          "ds_read_b64_tr_b16 a[128:129], v7 offset:64\n\t"
          "v_mfma_f32_16x16x32_bf16 v[48:51], a[148:151], a[80:83], v[48:51]\n\t"
          "v_mfma_f32_16x16x32_bf16 v[52:55], a[148:151], a[84:87], v[52:55]\n\t"
          "ds_read_b64_tr_b16 a[130:131], v7 offset:3648\n\t"
          "v_mfma_f32_16x16x32_bf16 v[52:55], a[144:147], a[68:71], v[52:55]\n\t"
          "v_mfma_f32_16x16x32_bf16 v[56:59], a[144:147], a[72:75], v[56:59]\n\t"
          "ds_read_b64_tr_b16 a[132:133], v7 offset:14400\n\t"
          "v_mfma_f32_16x16x32_bf16 v[56:59], a[148:151], a[88:91], v[56:59]\n\t"
          "v_mfma_f32_16x16x32_bf16 v[60:63], a[148:151], a[92:95], v[60:63]\n\t"
          "ds_read_b64_tr_b16 a[134:135], v7 offset:17984\n\t"
          "v_mfma_f32_16x16x32_bf16 v[60:63], a[144:147], a[76:79], v[60:63]\n\t"
          "s_waitcnt lgkmcnt(4)\n\t"
          "v_mfma_f32_16x16x32_bf16 v[172:175], a[152:155], a[76:79], v[172:175]\n\t"
          "ds_read_b64_tr_b16 a[136:137], v7 offset:7232\n\t"
          "v_mfma_f32_16x16x32_bf16 v[172:175], a[156:159], a[92:95], v[172:175]\n\t"
          "v_mfma_f32_16x16x32_bf16 v[168:171], a[156:159], a[88:91], v[168:171]\n\t"
          "ds_read_b64_tr_b16 a[138:139], v7 offset:10816\n\t"
          "v_mfma_f32_16x16x32_bf16 v[168:171], a[152:155], a[72:75], v[168:171]\n\t"
          "v_mfma_f32_16x16x32_bf16 v[164:167], a[152:155], a[68:71], v[164:167]\n\t"
          "ds_read_b64_tr_b16 a[140:141], v7 offset:21568\n\t"
          "v_mfma_f32_16x16x32_bf16 v[164:167], a[156:159], a[84:87], v[164:167]\n\t"
          "v_mfma_f32_16x16x32_bf16 v[160:163], a[156:159], a[80:83], v[160:163]\n\t"
          "ds_read_b64_tr_b16 a[142:143], v7 offset:25152\n\t"
          "v_mfma_f32_16x16x32_bf16 v[160:163], a[152:155], a[64:67], v[160:163]\n\t"
          "s_waitcnt lgkmcnt(4)\n\t"
          "v_mfma_f32_16x16x32_bf16 v[64:67], a[128:131], a[64:67], v[64:67]\n\t"
          "ds_read_b64_tr_b16 a[144:145], v7 offset:96\n\t"
          "v_mfma_f32_16x16x32_bf16 v[64:67], a[132:135], a[80:83], v[64:67]\n\t"
          "v_mfma_f32_16x16x32_bf16 v[68:71], a[132:135], a[84:87], v[68:71]\n\t"
          "ds_read_b64_tr_b16 a[146:147], v7 offset:3680\n\t"
          "v_mfma_f32_16x16x32_bf16 v[68:71], a[128:131], a[68:71], v[68:71]\n\t"
          "v_mfma_f32_16x16x32_bf16 v[72:75], a[128:131], a[72:75], v[72:75]\n\t"
          "ds_read_b64_tr_b16 a[148:149], v7 offset:14432\n\t"
          "v_mfma_f32_16x16x32_bf16 v[72:75], a[132:135], a[88:91], v[72:75]\n\t"
          "v_mfma_f32_16x16x32_bf16 v[76:79], a[132:135], a[92:95], v[76:79]\n\t"
          "ds_read_b64_tr_b16 a[150:151], v7 offset:18016\n\t"
          "v_mfma_f32_16x16x32_bf16 v[76:79], a[128:131], a[76:79], v[76:79]\n\t"
          "s_waitcnt lgkmcnt(4)\n\t"
          "v_mfma_f32_16x16x32_bf16 v[188:191], a[136:139], a[76:79], v[188:191]\n\t"
          "ds_read_b64_tr_b16 a[152:153], v7 offset:7264\n\t"
          "v_mfma_f32_16x16x32_bf16 v[188:191], a[140:143], a[92:95], v[188:191]\n\t"
          "v_mfma_f32_16x16x32_bf16 v[184:187], a[140:143], a[88:91], v[184:187]\n\t"
          "ds_read_b64_tr_b16 a[154:155], v7 offset:10848\n\t"
          "v_mfma_f32_16x16x32_bf16 v[184:187], a[136:139], a[72:75], v[184:187]\n\t"
          "v_mfma_f32_16x16x32_bf16 v[180:183], a[136:139], a[68:71], v[180:183]\n\t"
          "ds_read_b64_tr_b16 a[156:157], v7 offset:21600\n\t"
          "v_mfma_f32_16x16x32_bf16 v[180:183], a[140:143], a[84:87], v[180:183]\n\t"
          "v_mfma_f32_16x16x32_bf16 v[176:179], a[140:143], a[80:83], v[176:179]\n\t"
          "ds_read_b64_tr_b16 a[158:159], v7 offset:25184\n\t"
          "v_mfma_f32_16x16x32_bf16 v[176:179], a[136:139], a[64:67], v[176:179]\n\t"
          "s_waitcnt lgkmcnt(4)\n\t"
          "v_mfma_f32_16x16x32_bf16 v[80:83], a[144:147], a[64:67], v[80:83]\n\t"
          "ds_read_b64_tr_b16 a[128:129], v7 offset:128\n\t"
          "v_mfma_f32_16x16x32_bf16 v[80:83], a[148:151], a[80:83], v[80:83]\n\t"
          "v_mfma_f32_16x16x32_bf16 v[84:87], a[148:151], a[84:87], v[84:87]\n\t"
          "ds_read_b64_tr_b16 a[130:131], v7 offset:3712\n\t"
          "v_mfma_f32_16x16x32_bf16 v[84:87], a[144:147], a[68:71], v[84:87]\n\t"
          "v_mfma_f32_16x16x32_bf16 v[88:91], a[144:147], a[72:75], v[88:91]\n\t"
          "ds_read_b64_tr_b16 a[132:133], v7 offset:14464\n\t"
          "v_mfma_f32_16x16x32_bf16 v[88:91], a[148:151], a[88:91], v[88:91]\n\t"
          "v_mfma_f32_16x16x32_bf16 v[92:95], a[148:151], a[92:95], v[92:95]\n\t"
          "ds_read_b64_tr_b16 a[134:135], v7 offset:18048\n\t"
          "v_mfma_f32_16x16x32_bf16 v[92:95], a[144:147], a[76:79], v[92:95]\n\t"
          "s_lshl_b32 s59, %[w], 13\n\t"
          "s_mov_b32 s60, %[w2]\n\t"
          "s_waitcnt lgkmcnt(4)\n\t"
          "v_mfma_f32_16x16x32_bf16 v[204:207], a[152:155], a[76:79], v[204:207]\n\t"
          "ds_read_b64_tr_b16 a[136:137], v7 offset:7296\n\t"
          "v_mfma_f32_16x16x32_bf16 v[204:207], a[156:159], a[92:95], v[204:207]\n\t"
          "v_mfma_f32_16x16x32_bf16 v[200:203], a[156:159], a[88:91], v[200:203]\n\t"
          "ds_read_b64_tr_b16 a[138:139], v7 offset:10880\n\t"
          "v_mfma_f32_16x16x32_bf16 v[200:203], a[152:155], a[72:75], v[200:203]\n\t"
          "v_mfma_f32_16x16x32_bf16 v[196:199], a[152:155], a[68:71], v[196:199]\n\t"
          "ds_read_b64_tr_b16 a[140:141], v7 offset:21632\n\t"
          "v_mfma_f32_16x16x32_bf16 v[196:199], a[156:159], a[84:87], v[196:199]\n\t"
          "v_mfma_f32_16x16x32_bf16 v[192:195], a[156:159], a[80:83], v[192:195]\n\t"
          "ds_read_b64_tr_b16 a[142:143], v7 offset:25216\n\t"
          "v_mfma_f32_16x16x32_bf16 v[192:195], a[152:155], a[64:67], v[192:195]\n\t"
          "s_add_u32 s61, %[w2], 4096\n\t"
          "s_waitcnt lgkmcnt(4)\n\t"
          "v_mfma_f32_16x16x32_bf16 v[96:99], a[128:131], a[64:67], v[96:99]\n\t"
          "ds_read_b64_tr_b16 a[144:145], v7 offset:160\n\t"
          "v_mfma_f32_16x16x32_bf16 v[96:99], a[132:135], a[80:83], v[96:99]\n\t"
          "v_mfma_f32_16x16x32_bf16 v[100:103], a[132:135], a[84:87], v[100:103]\n\t"
          "ds_read_b64_tr_b16 a[146:147], v7 offset:3744\n\t"
          "v_mfma_f32_16x16x32_bf16 v[100:103], a[128:131], a[68:71], v[100:103]\n\t"
          "v_mfma_f32_16x16x32_bf16 v[104:107], a[128:131], a[72:75], v[104:107]\n\t"
          "ds_read_b64_tr_b16 a[148:149], v7 offset:14496\n\t"
          "v_mfma_f32_16x16x32_bf16 v[104:107], a[132:135], a[88:91], v[104:107]\n\t"
          "v_mfma_f32_16x16x32_bf16 v[108:111], a[132:135], a[92:95], v[108:111]\n\t"
          "ds_read_b64_tr_b16 a[150:151], v7 offset:18080\n\t"
          "v_mfma_f32_16x16x32_bf16 v[108:111], a[128:131], a[76:79], v[108:111]\n\t"
          "s_waitcnt lgkmcnt(4)\n\t"
          "v_mfma_f32_16x16x32_bf16 v[220:223], a[136:139], a[76:79], v[220:223]\n\t"
          "ds_read_b64_tr_b16 a[152:153], v7 offset:7328\n\t"
          "v_mfma_f32_16x16x32_bf16 v[220:223], a[140:143], a[92:95], v[220:223]\n\t"
          "v_mfma_f32_16x16x32_bf16 v[216:219], a[140:143], a[88:91], v[216:219]\n\t"
          "ds_read_b64_tr_b16 a[154:155], v7 offset:10912\n\t"
          "v_mfma_f32_16x16x32_bf16 v[216:219], a[136:139], a[72:75], v[216:219]\n\t"
          "v_mfma_f32_16x16x32_bf16 v[212:215], a[136:139], a[68:71], v[212:215]\n\t"
          "ds_read_b64_tr_b16 a[156:157], v7 offset:21664\n\t"
          "v_mfma_f32_16x16x32_bf16 v[212:215], a[140:143], a[84:87], v[212:215]\n\t"
          "v_mfma_f32_16x16x32_bf16 v[208:211], a[140:143], a[80:83], v[208:211]\n\t"
          "ds_read_b64_tr_b16 a[158:159], v7 offset:25248\n\t"
          "v_mfma_f32_16x16x32_bf16 v[208:211], a[136:139], a[64:67], v[208:211]\n\t"
          "s_waitcnt lgkmcnt(4)\n\t"
          "v_mfma_f32_16x16x32_bf16 v[112:115], a[144:147], a[64:67], v[112:115]\n\t"
          "ds_read_b64_tr_b16 a[128:129], v7 offset:192\n\t"
          "v_mfma_f32_16x16x32_bf16 v[112:115], a[148:151], a[80:83], v[112:115]\n\t"
          "v_mfma_f32_16x16x32_bf16 v[116:119], a[148:151], a[84:87], v[116:119]\n\t"
          "ds_read_b64_tr_b16 a[130:131], v7 offset:3776\n\t"
          "v_mfma_f32_16x16x32_bf16 v[116:119], a[144:147], a[68:71], v[116:119]\n\t"
          "v_mfma_f32_16x16x32_bf16 v[120:123], a[144:147], a[72:75], v[120:123]\n\t"
          "ds_read_b64_tr_b16 a[132:133], v7 offset:14528\n\t"
          "v_mfma_f32_16x16x32_bf16 v[120:123], a[148:151], a[88:91], v[120:123]\n\t"
          "v_mfma_f32_16x16x32_bf16 v[124:127], a[148:151], a[92:95], v[124:127]\n\t"
          "ds_read_b64_tr_b16 a[134:135], v7 offset:18112\n\t"
          "v_mfma_f32_16x16x32_bf16 v[124:127], a[144:147], a[76:79], v[124:127]\n\t"
          "s_waitcnt lgkmcnt(4)\n\t"
          "v_mfma_f32_16x16x32_bf16 v[236:239], a[152:155], a[76:79], v[236:239]\n\t"
          "ds_read_b64_tr_b16 a[136:137], v7 offset:7360\n\t"
          "v_mfma_f32_16x16x32_bf16 v[236:239], a[156:159], a[92:95], v[236:239]\n\t"
          "v_mfma_f32_16x16x32_bf16 v[232:235], a[156:159], a[88:91], v[232:235]\n\t"
          "ds_read_b64_tr_b16 a[138:139], v7 offset:10944\n\t"
          "v_mfma_f32_16x16x32_bf16 v[232:235], a[152:155], a[72:75], v[232:235]\n\t"
          "v_mfma_f32_16x16x32_bf16 v[228:231], a[152:155], a[68:71], v[228:231]\n\t"
          "ds_read_b64_tr_b16 a[140:141], v7 offset:21696\n\t"
          "v_mfma_f32_16x16x32_bf16 v[228:231], a[156:159], a[84:87], v[228:231]\n\t"
          "v_mfma_f32_16x16x32_bf16 v[224:227], a[156:159], a[80:83], v[224:227]\n\t"
          "ds_read_b64_tr_b16 a[142:143], v7 offset:25280\n\t"
          "v_mfma_f32_16x16x32_bf16 v[224:227], a[152:155], a[64:67], v[224:227]\n\t"
          "s_waitcnt vmcnt(2)\n\t"
          "s_barrier\n\t"
          "s_waitcnt lgkmcnt(4)\n\t"
          "v_mfma_f32_16x16x32_bf16 v[128:131], a[128:131], a[64:67], v[128:131]\n\t"
          "ds_read_b64_tr_b16 a[144:145], v7 offset:28672\n\t"
          "v_mfma_f32_16x16x32_bf16 v[128:131], a[132:135], a[80:83], v[128:131]\n\t"
          "v_mfma_f32_16x16x32_bf16 v[132:135], a[132:135], a[84:87], v[132:135]\n\t"
          "ds_read_b64_tr_b16 a[146:147], v7 offset:32256\n\t"
          "v_mfma_f32_16x16x32_bf16 v[132:135], a[128:131], a[68:71], v[132:135]\n\t"
          "v_mfma_f32_16x16x32_bf16 v[136:139], a[128:131], a[72:75], v[136:139]\n\t"
          "ds_read_b64_tr_b16 a[148:149], v7 offset:43008\n\t"
          "v_mfma_f32_16x16x32_bf16 v[136:139], a[132:135], a[88:91], v[136:139]\n\t"
          "v_mfma_f32_16x16x32_bf16 v[140:143], a[132:135], a[92:95], v[140:143]\n\t"
          "ds_read_b64_tr_b16 a[150:151], v7 offset:46592\n\t"
          "v_mfma_f32_16x16x32_bf16 v[140:143], a[128:131], a[76:79], v[140:143]\n\t"
          "s_waitcnt lgkmcnt(4)\n\t"
          "v_mfma_f32_16x16x32_bf16 v[252:255], a[136:139], a[76:79], v[252:255]\n\t"
          "ds_read_b64_tr_b16 a[152:153], v7 offset:35840\n\t"
          "v_mfma_f32_16x16x32_bf16 v[252:255], a[140:143], a[92:95], v[252:255]\n\t"
          "v_mfma_f32_16x16x32_bf16 v[248:251], a[140:143], a[88:91], v[248:251]\n\t"
          "ds_read_b64_tr_b16 a[154:155], v7 offset:39424\n\t"
          "v_mfma_f32_16x16x32_bf16 v[248:251], a[136:139], a[72:75], v[248:251]\n\t"
          "v_mfma_f32_16x16x32_bf16 v[244:247], a[136:139], a[68:71], v[244:247]\n\t"
          "ds_read_b64_tr_b16 a[156:157], v7 offset:50176\n\t"
          "v_mfma_f32_16x16x32_bf16 v[244:247], a[140:143], a[84:87], v[244:247]\n\t"
          "v_mfma_f32_16x16x32_bf16 v[240:243], a[140:143], a[80:83], v[240:243]\n\t"
          "ds_read_b64_tr_b16 a[158:159], v7 offset:53760\n\t"
          "v_mfma_f32_16x16x32_bf16 v[240:243], a[136:139], a[64:67], v[240:243]\n\t"
          "s_waitcnt vmcnt(0)\n\t"
          "s_waitcnt lgkmcnt(4)\n\t"
          "v_mfma_f32_16x16x32_bf16 v[32:35], a[144:147], a[96:99], v[32:35]\n\t"
          "ds_read_b64_tr_b16 a[128:129], v7 offset:28704\n\t"
          "v_mfma_f32_16x16x32_bf16 v[32:35], a[148:151], a[112:115], v[32:35]\n\t"
          "s_add_u32 m0, s59, 0\n\t"
          "v_mfma_f32_16x16x32_bf16 v[36:39], a[148:151], a[116:119], v[36:39]\n\t"
          "ds_read_b64_tr_b16 a[130:131], v7 offset:32288\n\t"
          "buffer_load_dwordx4 %[vx], %[ws], s60 offen offset:0 sc1 lds\n\t"
          "v_mfma_f32_16x16x32_bf16 v[36:39], a[144:147], a[100:103], v[36:39]\n\t"
          "v_mfma_f32_16x16x32_bf16 v[40:43], a[144:147], a[104:107], v[40:43]\n\t"
          "ds_read_b64_tr_b16 a[132:133], v7 offset:43040\n\t"
          "v_mfma_f32_16x16x32_bf16 v[40:43], a[148:151], a[120:123], v[40:43]\n\t"
          "v_mfma_f32_16x16x32_bf16 v[44:47], a[148:151], a[124:127], v[44:47]\n\t"
          "ds_read_b64_tr_b16 a[134:135], v7 offset:46624\n\t"
          "v_mfma_f32_16x16x32_bf16 v[44:47], a[144:147], a[108:111], v[44:47]\n\t"
          "s_waitcnt lgkmcnt(4)\n\t"
          "v_mfma_f32_16x16x32_bf16 v[156:159], a[152:155], a[108:111], v[156:159]\n\t"
          "ds_read_b64_tr_b16 a[136:137], v7 offset:35872\n\t"
          "buffer_load_dwordx4 %[vx], %[ws], s60 offen offset:1024 sc1 lds\n\t"
          "v_mfma_f32_16x16x32_bf16 v[156:159], a[156:159], a[124:127], v[156:159]\n\t"
          "v_mfma_f32_16x16x32_bf16 v[152:155], a[156:159], a[120:123], v[152:155]\n\t"
          "ds_read_b64_tr_b16 a[138:139], v7 offset:39456\n\t"
          "v_mfma_f32_16x16x32_bf16 v[152:155], a[152:155], a[104:107], v[152:155]\n\t"
          "v_mfma_f32_16x16x32_bf16 v[148:151], a[152:155], a[100:103], v[148:151]\n\t"
          "ds_read_b64_tr_b16 a[140:141], v7 offset:50208\n\t"
          "v_mfma_f32_16x16x32_bf16 v[148:151], a[156:159], a[116:119], v[148:151]\n\t"
          "v_mfma_f32_16x16x32_bf16 v[144:147], a[156:159], a[112:115], v[144:147]\n\t"
          "ds_read_b64_tr_b16 a[142:143], v7 offset:53792\n\t"
          "v_mfma_f32_16x16x32_bf16 v[144:147], a[152:155], a[96:99], v[144:147]\n\t"
          "buffer_load_dwordx4 %[vx], %[ws], s60 offen offset:2048 sc1 lds\n\t"
          "s_waitcnt lgkmcnt(4)\n\t"
          "v_mfma_f32_16x16x32_bf16 v[48:51], a[128:131], a[96:99], v[48:51]\n\t"
          "ds_read_b64_tr_b16 a[144:145], v7 offset:28736\n\t"
          "v_mfma_f32_16x16x32_bf16 v[48:51], a[132:135], a[112:115], v[48:51]\n\t"
          "v_mfma_f32_16x16x32_bf16 v[52:55], a[132:135], a[116:119], v[52:55]\n\t"
          "ds_read_b64_tr_b16 a[146:147], v7 offset:32320\n\t"
          "v_mfma_f32_16x16x32_bf16 v[52:55], a[128:131], a[100:103], v[52:55]\n\t"
          "v_mfma_f32_16x16x32_bf16 v[56:59], a[128:131], a[104:107], v[56:59]\n\t"
          "ds_read_b64_tr_b16 a[148:149], v7 offset:43072\n\t"
          "v_mfma_f32_16x16x32_bf16 v[56:59], a[132:135], a[120:123], v[56:59]\n\t"
          "buffer_load_dwordx4 %[vx], %[ws], s60 offen offset:3072 sc1 lds\n\t"
          "v_mfma_f32_16x16x32_bf16 v[60:63], a[132:135], a[124:127], v[60:63]\n\t"
          "ds_read_b64_tr_b16 a[150:151], v7 offset:46656\n\t"
          "v_mfma_f32_16x16x32_bf16 v[60:63], a[128:131], a[108:111], v[60:63]\n\t"
          "s_waitcnt lgkmcnt(4)\n\t"
          "v_mfma_f32_16x16x32_bf16 v[172:175], a[136:139], a[108:111], v[172:175]\n\t"
          "ds_read_b64_tr_b16 a[152:153], v7 offset:35904\n\t"
          "v_mfma_f32_16x16x32_bf16 v[172:175], a[140:143], a[124:127], v[172:175]\n\t"
          "v_mfma_f32_16x16x32_bf16 v[168:171], a[140:143], a[120:123], v[168:171]\n\t"
          "ds_read_b64_tr_b16 a[154:155], v7 offset:39488\n\t"
          "v_mfma_f32_16x16x32_bf16 v[168:171], a[136:139], a[104:107], v[168:171]\n\t"
          "s_add_u32 m0, s59, 4096\n\t"
          "v_mfma_f32_16x16x32_bf16 v[164:167], a[136:139], a[100:103], v[164:167]\n\t"
          "ds_read_b64_tr_b16 a[156:157], v7 offset:50240\n\t"
          "buffer_load_dwordx4 %[vx], %[ws], s61 offen offset:0 sc1 lds\n\t"
          "v_mfma_f32_16x16x32_bf16 v[164:167], a[140:143], a[116:119], v[164:167]\n\t"
          "v_mfma_f32_16x16x32_bf16 v[160:163], a[140:143], a[112:115], v[160:163]\n\t"
          "ds_read_b64_tr_b16 a[158:159], v7 offset:53824\n\t"
          "v_mfma_f32_16x16x32_bf16 v[160:163], a[136:139], a[96:99], v[160:163]\n\t"
          "s_waitcnt lgkmcnt(4)\n\t"
          "v_mfma_f32_16x16x32_bf16 v[64:67], a[144:147], a[96:99], v[64:67]\n\t"
          "ds_read_b64_tr_b16 a[128:129], v7 offset:28768\n\t"
          "v_mfma_f32_16x16x32_bf16 v[64:67], a[148:151], a[112:115], v[64:67]\n\t"
          "v_mfma_f32_16x16x32_bf16 v[68:71], a[148:151], a[116:119], v[68:71]\n\t"
          "ds_read_b64_tr_b16 a[130:131], v7 offset:32352\n\t"
          "buffer_load_dwordx4 %[vx], %[ws], s61 offen offset:1024 sc1 lds\n\t"
          "v_mfma_f32_16x16x32_bf16 v[68:71], a[144:147], a[100:103], v[68:71]\n\t"
          "v_mfma_f32_16x16x32_bf16 v[72:75], a[144:147], a[104:107], v[72:75]\n\t"
          "ds_read_b64_tr_b16 a[132:133], v7 offset:43104\n\t"
          "v_mfma_f32_16x16x32_bf16 v[72:75], a[148:151], a[120:123], v[72:75]\n\t"
          "v_mfma_f32_16x16x32_bf16 v[76:79], a[148:151], a[124:127], v[76:79]\n\t"
          "ds_read_b64_tr_b16 a[134:135], v7 offset:46688\n\t"
          "v_mfma_f32_16x16x32_bf16 v[76:79], a[144:147], a[108:111], v[76:79]\n\t"
          "s_waitcnt lgkmcnt(4)\n\t"
          "v_mfma_f32_16x16x32_bf16 v[188:191], a[152:155], a[108:111], v[188:191]\n\t"
          "ds_read_b64_tr_b16 a[136:137], v7 offset:35936\n\t"
          "v_mfma_f32_16x16x32_bf16 v[188:191], a[156:159], a[124:127], v[188:191]\n\t"
          "buffer_load_dwordx4 %[vx], %[ws], s61 offen offset:2048 sc1 lds\n\t"
          "v_mfma_f32_16x16x32_bf16 v[184:187], a[156:159], a[120:123], v[184:187]\n\t"
          "ds_read_b64_tr_b16 a[138:139], v7 offset:39520\n\t"
          "v_mfma_f32_16x16x32_bf16 v[184:187], a[152:155], a[104:107], v[184:187]\n\t"
          "v_mfma_f32_16x16x32_bf16 v[180:183], a[152:155], a[100:103], v[180:183]\n\t"
          "ds_read_b64_tr_b16 a[140:141], v7 offset:50272\n\t"
          "v_mfma_f32_16x16x32_bf16 v[180:183], a[156:159], a[116:119], v[180:183]\n\t"
          "v_mfma_f32_16x16x32_bf16 v[176:179], a[156:159], a[112:115], v[176:179]\n\t"
          "ds_read_b64_tr_b16 a[142:143], v7 offset:53856\n\t"
          "v_mfma_f32_16x16x32_bf16 v[176:179], a[152:155], a[96:99], v[176:179]\n\t"
          "buffer_load_dwordx4 %[vx], %[ws], s61 offen offset:3072 sc1 lds\n\t"
          "s_add_u32 s60, s60, 16384\n\t"
          "s_add_u32 s61, s61, 16384\n\t"
          "s_waitcnt lgkmcnt(4)\n\t"
          "v_mfma_f32_16x16x32_bf16 v[80:83], a[128:131], a[96:99], v[80:83]\n\t"
          "ds_read_b64_tr_b16 a[144:145], v7 offset:28800\n\t"
          "v_mfma_f32_16x16x32_bf16 v[80:83], a[132:135], a[112:115], v[80:83]\n\t"
          "v_mfma_f32_16x16x32_bf16 v[84:87], a[132:135], a[116:119], v[84:87]\n\t"
          "ds_read_b64_tr_b16 a[146:147], v7 offset:32384\n\t"
          "v_mfma_f32_16x16x32_bf16 v[84:87], a[128:131], a[100:103], v[84:87]\n\t"
          "v_mfma_f32_16x16x32_bf16 v[88:91], a[128:131], a[104:107], v[88:91]\n\t"
          "ds_read_b64_tr_b16 a[148:149], v7 offset:43136\n\t"
          "v_mfma_f32_16x16x32_bf16 v[88:91], a[132:135], a[120:123], v[88:91]\n\t"
          "s_add_u32 m0, s59, 32768\n\t"
          "v_mfma_f32_16x16x32_bf16 v[92:95], a[132:135], a[124:127], v[92:95]\n\t"
          "ds_read_b64_tr_b16 a[150:151], v7 offset:46720\n\t"
          "buffer_load_dwordx4 %[vx], %[ws], s60 offen offset:0 sc1 lds\n\t"
          "v_mfma_f32_16x16x32_bf16 v[92:95], a[128:131], a[108:111], v[92:95]\n\t"
          "s_waitcnt lgkmcnt(4)\n\t"
          "v_mfma_f32_16x16x32_bf16 v[204:207], a[136:139], a[108:111], v[204:207]\n\t"
          "ds_read_b64_tr_b16 a[152:153], v7 offset:35968\n\t"
          "v_mfma_f32_16x16x32_bf16 v[204:207], a[140:143], a[124:127], v[204:207]\n\t"
          "v_mfma_f32_16x16x32_bf16 v[200:203], a[140:143], a[120:123], v[200:203]\n\t"
          "ds_read_b64_tr_b16 a[154:155], v7 offset:39552\n\t"
          "v_mfma_f32_16x16x32_bf16 v[200:203], a[136:139], a[104:107], v[200:203]\n\t"
          "v_mfma_f32_16x16x32_bf16 v[196:199], a[136:139], a[100:103], v[196:199]\n\t"
          "ds_read_b64_tr_b16 a[156:157], v7 offset:50304\n\t"
          "buffer_load_dwordx4 %[vx], %[ws], s60 offen offset:1024 sc1 lds\n\t"
          "v_mfma_f32_16x16x32_bf16 v[196:199], a[140:143], a[116:119], v[196:199]\n\t"
          "v_mfma_f32_16x16x32_bf16 v[192:195], a[140:143], a[112:115], v[192:195]\n\t"
          "ds_read_b64_tr_b16 a[158:159], v7 offset:53888\n\t"
          "v_mfma_f32_16x16x32_bf16 v[192:195], a[136:139], a[96:99], v[192:195]\n\t"
          "s_waitcnt lgkmcnt(4)\n\t"
          "v_mfma_f32_16x16x32_bf16 v[96:99], a[144:147], a[96:99], v[96:99]\n\t"
          "ds_read_b64_tr_b16 a[128:129], v7 offset:28832\n\t"
          "v_mfma_f32_16x16x32_bf16 v[96:99], a[148:151], a[112:115], v[96:99]\n\t"
          "v_mfma_f32_16x16x32_bf16 v[100:103], a[148:151], a[116:119], v[100:103]\n\t"
          "ds_read_b64_tr_b16 a[130:131], v7 offset:32416\n\t"
          "v_mfma_f32_16x16x32_bf16 v[100:103], a[144:147], a[100:103], v[100:103]\n\t"
          "buffer_load_dwordx4 %[vx], %[ws], s60 offen offset:2048 sc1 lds\n\t"
          "v_mfma_f32_16x16x32_bf16 v[104:107], a[144:147], a[104:107], v[104:107]\n\t"
          "ds_read_b64_tr_b16 a[132:133], v7 offset:43168\n\t"
          "v_mfma_f32_16x16x32_bf16 v[104:107], a[148:151], a[120:123], v[104:107]\n\t"
          "v_mfma_f32_16x16x32_bf16 v[108:111], a[148:151], a[124:127], v[108:111]\n\t"
          "ds_read_b64_tr_b16 a[134:135], v7 offset:46752\n\t"
          "v_mfma_f32_16x16x32_bf16 v[108:111], a[144:147], a[108:111], v[108:111]\n\t"
          "s_waitcnt lgkmcnt(4)\n\t"
          "v_mfma_f32_16x16x32_bf16 v[220:223], a[152:155], a[108:111], v[220:223]\n\t"
          "ds_read_b64_tr_b16 a[136:137], v7 offset:36000\n\t"
          "v_mfma_f32_16x16x32_bf16 v[220:223], a[156:159], a[124:127], v[220:223]\n\t"
          "buffer_load_dwordx4 %[vx], %[ws], s60 offen offset:3072 sc1 lds\n\t"
          "v_mfma_f32_16x16x32_bf16 v[216:219], a[156:159], a[120:123], v[216:219]\n\t"
          "ds_read_b64_tr_b16 a[138:139], v7 offset:39584\n\t"
          "v_mfma_f32_16x16x32_bf16 v[216:219], a[152:155], a[104:107], v[216:219]\n\t"
          "v_mfma_f32_16x16x32_bf16 v[212:215], a[152:155], a[100:103], v[212:215]\n\t"
          "ds_read_b64_tr_b16 a[140:141], v7 offset:50336\n\t"
          "v_mfma_f32_16x16x32_bf16 v[212:215], a[156:159], a[116:119], v[212:215]\n\t"
          "v_mfma_f32_16x16x32_bf16 v[208:211], a[156:159], a[112:115], v[208:211]\n\t"
          "ds_read_b64_tr_b16 a[142:143], v7 offset:53920\n\t"
          "v_mfma_f32_16x16x32_bf16 v[208:211], a[152:155], a[96:99], v[208:211]\n\t"
          "s_add_u32 m0, s59, 36864\n\t"
          "s_waitcnt lgkmcnt(4)\n\t"
          "v_mfma_f32_16x16x32_bf16 v[112:115], a[128:131], a[96:99], v[112:115]\n\t"
          "ds_read_b64_tr_b16 a[144:145], v7 offset:28864\n\t"
          "buffer_load_dwordx4 %[vx], %[ws], s61 offen offset:0 sc1 lds\n\t"
          "v_mfma_f32_16x16x32_bf16 v[112:115], a[132:135], a[112:115], v[112:115]\n\t"
          "v_mfma_f32_16x16x32_bf16 v[116:119], a[132:135], a[116:119], v[116:119]\n\t"
          "ds_read_b64_tr_b16 a[146:147], v7 offset:32448\n\t"
          "v_mfma_f32_16x16x32_bf16 v[116:119], a[128:131], a[100:103], v[116:119]\n\t"
          "v_mfma_f32_16x16x32_bf16 v[120:123], a[128:131], a[104:107], v[120:123]\n\t"
          "ds_read_b64_tr_b16 a[148:149], v7 offset:43200\n\t"
          "v_mfma_f32_16x16x32_bf16 v[120:123], a[132:135], a[120:123], v[120:123]\n\t"
          "v_mfma_f32_16x16x32_bf16 v[124:127], a[132:135], a[124:127], v[124:127]\n\t"
          "ds_read_b64_tr_b16 a[150:151], v7 offset:46784\n\t"
          "buffer_load_dwordx4 %[vx], %[ws], s61 offen offset:1024 sc1 lds\n\t"
          "v_mfma_f32_16x16x32_bf16 v[124:127], a[128:131], a[108:111], v[124:127]\n\t"
          "s_waitcnt lgkmcnt(4)\n\t"
          "v_mfma_f32_16x16x32_bf16 v[236:239], a[136:139], a[108:111], v[236:239]\n\t"
          "ds_read_b64_tr_b16 a[152:153], v7 offset:36032\n\t"
          "v_mfma_f32_16x16x32_bf16 v[236:239], a[140:143], a[124:127], v[236:239]\n\t"
          "v_mfma_f32_16x16x32_bf16 v[232:235], a[140:143], a[120:123], v[232:235]\n\t"
          "ds_read_b64_tr_b16 a[154:155], v7 offset:39616\n\t"
          "v_mfma_f32_16x16x32_bf16 v[232:235], a[136:139], a[104:107], v[232:235]\n\t"
          "v_mfma_f32_16x16x32_bf16 v[228:231], a[136:139], a[100:103], v[228:231]\n\t"
          "ds_read_b64_tr_b16 a[156:157], v7 offset:50368\n\t"
          "v_mfma_f32_16x16x32_bf16 v[228:231], a[140:143], a[116:119], v[228:231]\n\t"
          "buffer_load_dwordx4 %[vx], %[ws], s61 offen offset:2048 sc1 lds\n\t"
          "v_mfma_f32_16x16x32_bf16 v[224:227], a[140:143], a[112:115], v[224:227]\n\t"
          "ds_read_b64_tr_b16 a[158:159], v7 offset:53952\n\t"
          "v_mfma_f32_16x16x32_bf16 v[224:227], a[136:139], a[96:99], v[224:227]\n\t"
          "s_waitcnt lgkmcnt(4)\n\t"
          "v_mfma_f32_16x16x32_bf16 v[128:131], a[144:147], a[96:99], v[128:131]\n\t"
          "v_mfma_f32_16x16x32_bf16 v[128:131], a[148:151], a[112:115], v[128:131]\n\t"
          "v_mfma_f32_16x16x32_bf16 v[132:135], a[148:151], a[116:119], v[132:135]\n\t"
          "v_mfma_f32_16x16x32_bf16 v[132:135], a[144:147], a[100:103], v[132:135]\n\t"
          "buffer_load_dwordx4 %[vx], %[ws], s61 offen offset:3072 sc1 lds\n\t"
          "s_add_u32 s60, s60, 16384\n\t"
          "s_add_u32 s61, s61, 16384\n\t"
          "v_mfma_f32_16x16x32_bf16 v[136:139], a[144:147], a[104:107], v[136:139]\n\t"
          "v_mfma_f32_16x16x32_bf16 v[136:139], a[148:151], a[120:123], v[136:139]\n\t"
          "v_mfma_f32_16x16x32_bf16 v[140:143], a[148:151], a[124:127], v[140:143]\n\t"
          "v_mfma_f32_16x16x32_bf16 v[140:143], a[144:147], a[108:111], v[140:143]\n\t"
          "s_waitcnt lgkmcnt(0)\n\t"
          "v_mfma_f32_16x16x32_bf16 v[252:255], a[152:155], a[108:111], v[252:255]\n\t"
          "v_mfma_f32_16x16x32_bf16 v[252:255], a[156:159], a[124:127], v[252:255]\n\t"
          "v_mfma_f32_16x16x32_bf16 v[248:251], a[156:159], a[120:123], v[248:251]\n\t"
          "v_mfma_f32_16x16x32_bf16 v[248:251], a[152:155], a[104:107], v[248:251]\n\t"
          "v_mfma_f32_16x16x32_bf16 v[244:247], a[152:155], a[100:103], v[244:247]\n\t"
          "v_mfma_f32_16x16x32_bf16 v[244:247], a[156:159], a[116:119], v[244:247]\n\t"
          "v_mfma_f32_16x16x32_bf16 v[240:243], a[156:159], a[112:115], v[240:243]\n\t"
          "v_mfma_f32_16x16x32_bf16 v[240:243], a[152:155], a[96:99], v[240:243]\n\t"
          "s_nop 7\n\t"
          "s_mov_b32 s52, 0xbdd2d3e8\n\t"
          "s_mov_b32 s53, 0xc0135761\n\t"
          "s_lshl_b32 s54, %[rot], 9\n\t"
          "s_add_u32 s54, s54, %[hb]\n\t"
          "s_add_u32 s55, s54, 4096\n\t"
          "s_add_u32 s56, s54, 8192\n\t"
          "s_lshl_b32 s57, %[rot], 4\n\t"
          "s_add_u32 s57, s57, %[hb]\n\t"
          "s_add_u32 s57, s57, 12288\n\t"
          "v_pk_mul_f32 v[16:17], v[32:33], v[32:33]\n\t"
          "v_pk_mul_f32 v[18:19], v[34:35], v[34:35]\n\t"
          "v_pk_mul_f32 v[20:21], v[48:49], v[48:49]\n\t"
          "v_pk_mul_f32 v[22:23], v[50:51], v[50:51]\n\t"
          "v_pk_fma_f32 v[16:17], v[16:17], s[52:53], s[52:53] op_sel:[0,0,1] op_sel_hi:[1,0,1]\n\t"
          "v_pk_fma_f32 v[18:19], v[18:19], s[52:53], s[52:53] op_sel:[0,0,1] op_sel_hi:[1,0,1]\n\t"
          "v_pk_fma_f32 v[20:21], v[20:21], s[52:53], s[52:53] op_sel:[0,0,1] op_sel_hi:[1,0,1]\n\t"
          "v_pk_fma_f32 v[22:23], v[22:23], s[52:53], s[52:53] op_sel:[0,0,1] op_sel_hi:[1,0,1]\n\t"
          "v_pk_mul_f32 v[16:17], v[16:17], v[32:33]\n\t"
          "v_pk_mul_f32 v[18:19], v[18:19], v[34:35]\n\t"
          "v_pk_mul_f32 v[20:21], v[20:21], v[48:49]\n\t"
          "v_pk_mul_f32 v[22:23], v[22:23], v[50:51]\n\t"
          "v_exp_f32 v16, v16\n\t"
          "v_exp_f32 v17, v17\n\t"
          "v_exp_f32 v18, v18\n\t"
          "v_exp_f32 v19, v19\n\t"
          "v_exp_f32 v20, v20\n\t"
          "v_exp_f32 v21, v21\n\t"
          "v_exp_f32 v22, v22\n\t"
          "v_exp_f32 v23, v23\n\t"
          "v_pk_mul_f32 v[144:145], v[32:33], v[144:145]\n\t"
          "v_pk_mul_f32 v[146:147], v[34:35], v[146:147]\n\t"
          "v_pk_mul_f32 v[160:161], v[48:49], v[160:161]\n\t"
          "v_pk_mul_f32 v[162:163], v[50:51], v[162:163]\n\t"
          "v_pk_add_f32 v[16:17], v[16:17], 1.0 op_sel_hi:[1,0]\n\t"
          "v_pk_add_f32 v[18:19], v[18:19], 1.0 op_sel_hi:[1,0]\n\t"
          "v_pk_add_f32 v[20:21], v[20:21], 1.0 op_sel_hi:[1,0]\n\t"
          "v_pk_add_f32 v[22:23], v[22:23], 1.0 op_sel_hi:[1,0]\n\t"
          "v_rcp_f32 v16, v16\n\t"
          "v_rcp_f32 v17, v17\n\t"
          "v_rcp_f32 v18, v18\n\t"
          "v_rcp_f32 v19, v19\n\t"
          "v_rcp_f32 v20, v20\n\t"
          "v_rcp_f32 v21, v21\n\t"
          "v_rcp_f32 v22, v22\n\t"
          "v_rcp_f32 v23, v23\n\t"
          "v_pk_mul_f32 v[144:145], v[144:145], v[16:17]\n\t"
          "v_pk_mul_f32 v[146:147], v[146:147], v[18:19]\n\t"
          "v_pk_mul_f32 v[160:161], v[160:161], v[20:21]\n\t"
          "v_pk_mul_f32 v[162:163], v[162:163], v[22:23]\n\t"
          "v_cvt_pk_bf16_f32 v24, v144, v145\n\t"
          "v_cvt_pk_bf16_f32 v25, v146, v147\n\t"
          "v_cvt_pk_bf16_f32 v26, v160, v161\n\t"
          "v_cvt_pk_bf16_f32 v27, v162, v163\n\t"
          "buffer_store_dwordx4 v[24:27], %[vx], %[ws], s54 offen offset:0 sc1\n\t"
          "v_pk_mul_f32 v[16:17], v[36:37], v[36:37]\n\t"
          "v_pk_mul_f32 v[18:19], v[38:39], v[38:39]\n\t"
          "v_pk_mul_f32 v[20:21], v[52:53], v[52:53]\n\t"
          "v_pk_mul_f32 v[22:23], v[54:55], v[54:55]\n\t"
          "v_pk_fma_f32 v[16:17], v[16:17], s[52:53], s[52:53] op_sel:[0,0,1] op_sel_hi:[1,0,1]\n\t"
          "v_pk_fma_f32 v[18:19], v[18:19], s[52:53], s[52:53] op_sel:[0,0,1] op_sel_hi:[1,0,1]\n\t"
          "v_pk_fma_f32 v[20:21], v[20:21], s[52:53], s[52:53] op_sel:[0,0,1] op_sel_hi:[1,0,1]\n\t"
          "v_pk_fma_f32 v[22:23], v[22:23], s[52:53], s[52:53] op_sel:[0,0,1] op_sel_hi:[1,0,1]\n\t"
          "v_pk_mul_f32 v[16:17], v[16:17], v[36:37]\n\t"
          "v_pk_mul_f32 v[18:19], v[18:19], v[38:39]\n\t"
          "v_pk_mul_f32 v[20:21], v[20:21], v[52:53]\n\t"
          "v_pk_mul_f32 v[22:23], v[22:23], v[54:55]\n\t"
          "v_exp_f32 v16, v16\n\t"
          "v_exp_f32 v17, v17\n\t"
          "v_exp_f32 v18, v18\n\t"
          "v_exp_f32 v19, v19\n\t"
          "v_exp_f32 v20, v20\n\t"
          "v_exp_f32 v21, v21\n\t"
          "v_exp_f32 v22, v22\n\t"
          "v_exp_f32 v23, v23\n\t"
          "v_pk_mul_f32 v[148:149], v[36:37], v[148:149]\n\t"
          "v_pk_mul_f32 v[150:151], v[38:39], v[150:151]\n\t"
          "v_pk_mul_f32 v[164:165], v[52:53], v[164:165]\n\t"
          "v_pk_mul_f32 v[166:167], v[54:55], v[166:167]\n\t"
          "v_pk_add_f32 v[16:17], v[16:17], 1.0 op_sel_hi:[1,0]\n\t"
          "v_pk_add_f32 v[18:19], v[18:19], 1.0 op_sel_hi:[1,0]\n\t"
          "v_pk_add_f32 v[20:21], v[20:21], 1.0 op_sel_hi:[1,0]\n\t"
          "v_pk_add_f32 v[22:23], v[22:23], 1.0 op_sel_hi:[1,0]\n\t"
          "v_rcp_f32 v16, v16\n\t"
          "v_rcp_f32 v17, v17\n\t"
          "v_rcp_f32 v18, v18\n\t"
          "v_rcp_f32 v19, v19\n\t"
          "v_rcp_f32 v20, v20\n\t"
          "v_rcp_f32 v21, v21\n\t"
          "v_rcp_f32 v22, v22\n\t"
          "v_rcp_f32 v23, v23\n\t"
          "v_pk_mul_f32 v[148:149], v[148:149], v[16:17]\n\t"
          "v_pk_mul_f32 v[150:151], v[150:151], v[18:19]\n\t"
          "v_pk_mul_f32 v[164:165], v[164:165], v[20:21]\n\t"
          "v_pk_mul_f32 v[166:167], v[166:167], v[22:23]\n\t"
          "v_cvt_pk_bf16_f32 v28, v148, v149\n\t"
          "v_cvt_pk_bf16_f32 v29, v150, v151\n\t"
          "v_cvt_pk_bf16_f32 v30, v164, v165\n\t"
          "v_cvt_pk_bf16_f32 v31, v166, v167\n\t"
          "buffer_store_dwordx4 v[28:31], %[vx], %[ws], s54 offen offset:1024 sc1\n\t"
          "v_pk_mul_f32 v[16:17], v[40:41], v[40:41]\n\t"
          "v_pk_mul_f32 v[18:19], v[42:43], v[42:43]\n\t"
          "v_pk_mul_f32 v[20:21], v[56:57], v[56:57]\n\t"
          "v_pk_mul_f32 v[22:23], v[58:59], v[58:59]\n\t"
          "v_pk_fma_f32 v[16:17], v[16:17], s[52:53], s[52:53] op_sel:[0,0,1] op_sel_hi:[1,0,1]\n\t"
          "v_pk_fma_f32 v[18:19], v[18:19], s[52:53], s[52:53] op_sel:[0,0,1] op_sel_hi:[1,0,1]\n\t"
          "v_pk_fma_f32 v[20:21], v[20:21], s[52:53], s[52:53] op_sel:[0,0,1] op_sel_hi:[1,0,1]\n\t"
          "v_pk_fma_f32 v[22:23], v[22:23], s[52:53], s[52:53] op_sel:[0,0,1] op_sel_hi:[1,0,1]\n\t"
          "v_pk_mul_f32 v[16:17], v[16:17], v[40:41]\n\t"
          "v_pk_mul_f32 v[18:19], v[18:19], v[42:43]\n\t"
          "v_pk_mul_f32 v[20:21], v[20:21], v[56:57]\n\t"
          "v_pk_mul_f32 v[22:23], v[22:23], v[58:59]\n\t"
          "v_exp_f32 v16, v16\n\t"
          "v_exp_f32 v17, v17\n\t"
          "v_exp_f32 v18, v18\n\t"
          "v_exp_f32 v19, v19\n\t"
          "v_exp_f32 v20, v20\n\t"
          "v_exp_f32 v21, v21\n\t"
          "v_exp_f32 v22, v22\n\t"
          "v_exp_f32 v23, v23\n\t"
          "v_pk_mul_f32 v[152:153], v[40:41], v[152:153]\n\t"
          "v_pk_mul_f32 v[154:155], v[42:43], v[154:155]\n\t"
          "v_pk_mul_f32 v[168:169], v[56:57], v[168:169]\n\t"
          "v_pk_mul_f32 v[170:171], v[58:59], v[170:171]\n\t"
          "v_pk_add_f32 v[16:17], v[16:17], 1.0 op_sel_hi:[1,0]\n\t"
          "v_pk_add_f32 v[18:19], v[18:19], 1.0 op_sel_hi:[1,0]\n\t"
          "v_pk_add_f32 v[20:21], v[20:21], 1.0 op_sel_hi:[1,0]\n\t"
          "v_pk_add_f32 v[22:23], v[22:23], 1.0 op_sel_hi:[1,0]\n\t"
          "v_rcp_f32 v16, v16\n\t"
          "v_rcp_f32 v17, v17\n\t"
          "v_rcp_f32 v18, v18\n\t"
          "v_rcp_f32 v19, v19\n\t"
          "v_rcp_f32 v20, v20\n\t"
          "v_rcp_f32 v21, v21\n\t"
          "v_rcp_f32 v22, v22\n\t"
          "v_rcp_f32 v23, v23\n\t"
          "v_pk_mul_f32 v[152:153], v[152:153], v[16:17]\n\t"
          "v_pk_mul_f32 v[154:155], v[154:155], v[18:19]\n\t"
          "v_pk_mul_f32 v[168:169], v[168:169], v[20:21]\n\t"
          "v_pk_mul_f32 v[170:171], v[170:171], v[22:23]\n\t"
          "v_cvt_pk_bf16_f32 v24, v152, v153\n\t"
          "v_cvt_pk_bf16_f32 v25, v154, v155\n\t"
          "v_cvt_pk_bf16_f32 v26, v168, v169\n\t"
          "v_cvt_pk_bf16_f32 v27, v170, v171\n\t"
          "buffer_store_dwordx4 v[24:27], %[vx], %[ws], s54 offen offset:2048 sc1\n\t"
          "v_pk_mul_f32 v[16:17], v[44:45], v[44:45]\n\t"
          "v_pk_mul_f32 v[18:19], v[46:47], v[46:47]\n\t"
          "v_pk_mul_f32 v[20:21], v[60:61], v[60:61]\n\t"
          "v_pk_mul_f32 v[22:23], v[62:63], v[62:63]\n\t"
          "v_pk_fma_f32 v[16:17], v[16:17], s[52:53], s[52:53] op_sel:[0,0,1] op_sel_hi:[1,0,1]\n\t"
          "v_pk_fma_f32 v[18:19], v[18:19], s[52:53], s[52:53] op_sel:[0,0,1] op_sel_hi:[1,0,1]\n\t"
          "v_pk_fma_f32 v[20:21], v[20:21], s[52:53], s[52:53] op_sel:[0,0,1] op_sel_hi:[1,0,1]\n\t"
          "v_pk_fma_f32 v[22:23], v[22:23], s[52:53], s[52:53] op_sel:[0,0,1] op_sel_hi:[1,0,1]\n\t"
          "v_pk_mul_f32 v[16:17], v[16:17], v[44:45]\n\t"
          "v_pk_mul_f32 v[18:19], v[18:19], v[46:47]\n\t"
          "v_pk_mul_f32 v[20:21], v[20:21], v[60:61]\n\t"
          "v_pk_mul_f32 v[22:23], v[22:23], v[62:63]\n\t"
          "v_exp_f32 v16, v16\n\t"
          "v_exp_f32 v17, v17\n\t"
          "v_exp_f32 v18, v18\n\t"
          "v_exp_f32 v19, v19\n\t"
          "v_exp_f32 v20, v20\n\t"
          "v_exp_f32 v21, v21\n\t"
          "v_exp_f32 v22, v22\n\t"
          "v_exp_f32 v23, v23\n\t"
          "v_pk_mul_f32 v[156:157], v[44:45], v[156:157]\n\t"
          "v_pk_mul_f32 v[158:159], v[46:47], v[158:159]\n\t"
          "v_pk_mul_f32 v[172:173], v[60:61], v[172:173]\n\t"
          "v_pk_mul_f32 v[174:175], v[62:63], v[174:175]\n\t"
          "v_pk_add_f32 v[16:17], v[16:17], 1.0 op_sel_hi:[1,0]\n\t"
          "v_pk_add_f32 v[18:19], v[18:19], 1.0 op_sel_hi:[1,0]\n\t"
          "v_pk_add_f32 v[20:21], v[20:21], 1.0 op_sel_hi:[1,0]\n\t"
          "v_pk_add_f32 v[22:23], v[22:23], 1.0 op_sel_hi:[1,0]\n\t"
          "v_rcp_f32 v16, v16\n\t"
          "v_rcp_f32 v17, v17\n\t"
          "v_rcp_f32 v18, v18\n\t"
          "v_rcp_f32 v19, v19\n\t"
          "v_rcp_f32 v20, v20\n\t"
          "v_rcp_f32 v21, v21\n\t"
          "v_rcp_f32 v22, v22\n\t"
          "v_rcp_f32 v23, v23\n\t"
          "v_pk_mul_f32 v[156:157], v[156:157], v[16:17]\n\t"
          "v_pk_mul_f32 v[158:159], v[158:159], v[18:19]\n\t"
          "v_pk_mul_f32 v[172:173], v[172:173], v[20:21]\n\t"
          "v_pk_mul_f32 v[174:175], v[174:175], v[22:23]\n\t"
          "v_cvt_pk_bf16_f32 v28, v156, v157\n\t"
          "v_cvt_pk_bf16_f32 v29, v158, v159\n\t"
          "v_cvt_pk_bf16_f32 v30, v172, v173\n\t"
          "v_cvt_pk_bf16_f32 v31, v174, v175\n\t"
          "buffer_store_dwordx4 v[28:31], %[vx], %[ws], s54 offen offset:3072 sc1\n\t"
          "v_pk_mul_f32 v[16:17], v[64:65], v[64:65]\n\t"
          "v_pk_mul_f32 v[18:19], v[66:67], v[66:67]\n\t"
          "v_pk_mul_f32 v[20:21], v[80:81], v[80:81]\n\t"
          "v_pk_mul_f32 v[22:23], v[82:83], v[82:83]\n\t"
          "v_pk_fma_f32 v[16:17], v[16:17], s[52:53], s[52:53] op_sel:[0,0,1] op_sel_hi:[1,0,1]\n\t"
          "v_pk_fma_f32 v[18:19], v[18:19], s[52:53], s[52:53] op_sel:[0,0,1] op_sel_hi:[1,0,1]\n\t"
          "v_pk_fma_f32 v[20:21], v[20:21], s[52:53], s[52:53] op_sel:[0,0,1] op_sel_hi:[1,0,1]\n\t"
          "v_pk_fma_f32 v[22:23], v[22:23], s[52:53], s[52:53] op_sel:[0,0,1] op_sel_hi:[1,0,1]\n\t"
          "v_pk_mul_f32 v[16:17], v[16:17], v[64:65]\n\t"
          "v_pk_mul_f32 v[18:19], v[18:19], v[66:67]\n\t"
          "v_pk_mul_f32 v[20:21], v[20:21], v[80:81]\n\t"
          "v_pk_mul_f32 v[22:23], v[22:23], v[82:83]\n\t"
          "v_exp_f32 v16, v16\n\t"
          "v_exp_f32 v17, v17\n\t"
          "v_exp_f32 v18, v18\n\t"
          "v_exp_f32 v19, v19\n\t"
          "v_exp_f32 v20, v20\n\t"
          "v_exp_f32 v21, v21\n\t"
          "v_exp_f32 v22, v22\n\t"
          "v_exp_f32 v23, v23\n\t"
          "v_pk_mul_f32 v[176:177], v[64:65], v[176:177]\n\t"
          "v_pk_mul_f32 v[178:179], v[66:67], v[178:179]\n\t"
          "v_pk_mul_f32 v[192:193], v[80:81], v[192:193]\n\t"
          "v_pk_mul_f32 v[194:195], v[82:83], v[194:195]\n\t"
          "v_pk_add_f32 v[16:17], v[16:17], 1.0 op_sel_hi:[1,0]\n\t"
          "v_pk_add_f32 v[18:19], v[18:19], 1.0 op_sel_hi:[1,0]\n\t"
          "v_pk_add_f32 v[20:21], v[20:21], 1.0 op_sel_hi:[1,0]\n\t"
          "v_pk_add_f32 v[22:23], v[22:23], 1.0 op_sel_hi:[1,0]\n\t"
          "v_rcp_f32 v16, v16\n\t"
          "v_rcp_f32 v17, v17\n\t"
          "v_rcp_f32 v18, v18\n\t"
          "v_rcp_f32 v19, v19\n\t"
          "v_rcp_f32 v20, v20\n\t"
          "v_rcp_f32 v21, v21\n\t"
          "v_rcp_f32 v22, v22\n\t"
          "v_rcp_f32 v23, v23\n\t"
          "v_pk_mul_f32 v[176:177], v[176:177], v[16:17]\n\t"
          "v_pk_mul_f32 v[178:179], v[178:179], v[18:19]\n\t"
          "v_pk_mul_f32 v[192:193], v[192:193], v[20:21]\n\t"
          "v_pk_mul_f32 v[194:195], v[194:195], v[22:23]\n\t"
          "v_cvt_pk_bf16_f32 v24, v176, v177\n\t"
          "v_cvt_pk_bf16_f32 v25, v178, v179\n\t"
          "v_cvt_pk_bf16_f32 v26, v192, v193\n\t"
          "v_cvt_pk_bf16_f32 v27, v194, v195\n\t"
          "buffer_store_dwordx4 v[24:27], %[vx], %[ws], s55 offen offset:0 sc1\n\t"
          "v_pk_mul_f32 v[16:17], v[68:69], v[68:69]\n\t"
          "v_pk_mul_f32 v[18:19], v[70:71], v[70:71]\n\t"
          "v_pk_mul_f32 v[20:21], v[84:85], v[84:85]\n\t"
          "v_pk_mul_f32 v[22:23], v[86:87], v[86:87]\n\t"
          "v_pk_fma_f32 v[16:17], v[16:17], s[52:53], s[52:53] op_sel:[0,0,1] op_sel_hi:[1,0,1]\n\t"
          "v_pk_fma_f32 v[18:19], v[18:19], s[52:53], s[52:53] op_sel:[0,0,1] op_sel_hi:[1,0,1]\n\t"
          "v_pk_fma_f32 v[20:21], v[20:21], s[52:53], s[52:53] op_sel:[0,0,1] op_sel_hi:[1,0,1]\n\t"
          "v_pk_fma_f32 v[22:23], v[22:23], s[52:53], s[52:53] op_sel:[0,0,1] op_sel_hi:[1,0,1]\n\t"
          "v_pk_mul_f32 v[16:17], v[16:17], v[68:69]\n\t"
          "v_pk_mul_f32 v[18:19], v[18:19], v[70:71]\n\t"
          "v_pk_mul_f32 v[20:21], v[20:21], v[84:85]\n\t"
          "v_pk_mul_f32 v[22:23], v[22:23], v[86:87]\n\t"
          "v_exp_f32 v16, v16\n\t"
          "v_exp_f32 v17, v17\n\t"
          "v_exp_f32 v18, v18\n\t"
          "v_exp_f32 v19, v19\n\t"
          "v_exp_f32 v20, v20\n\t"
          "v_exp_f32 v21, v21\n\t"
          "v_exp_f32 v22, v22\n\t"
          "v_exp_f32 v23, v23\n\t"
          "v_pk_mul_f32 v[180:181], v[68:69], v[180:181]\n\t"
          "v_pk_mul_f32 v[182:183], v[70:71], v[182:183]\n\t"
          "v_pk_mul_f32 v[196:197], v[84:85], v[196:197]\n\t"
          "v_pk_mul_f32 v[198:199], v[86:87], v[198:199]\n\t"
          "v_pk_add_f32 v[16:17], v[16:17], 1.0 op_sel_hi:[1,0]\n\t"
          "v_pk_add_f32 v[18:19], v[18:19], 1.0 op_sel_hi:[1,0]\n\t"
          "v_pk_add_f32 v[20:21], v[20:21], 1.0 op_sel_hi:[1,0]\n\t"
          "v_pk_add_f32 v[22:23], v[22:23], 1.0 op_sel_hi:[1,0]\n\t"
          "v_rcp_f32 v16, v16\n\t"
          "v_rcp_f32 v17, v17\n\t"
          "v_rcp_f32 v18, v18\n\t"
          "v_rcp_f32 v19, v19\n\t"
          "v_rcp_f32 v20, v20\n\t"
          "v_rcp_f32 v21, v21\n\t"
          "v_rcp_f32 v22, v22\n\t"
          "v_rcp_f32 v23, v23\n\t"
          "v_pk_mul_f32 v[180:181], v[180:181], v[16:17]\n\t"
          "v_pk_mul_f32 v[182:183], v[182:183], v[18:19]\n\t"
          "v_pk_mul_f32 v[196:197], v[196:197], v[20:21]\n\t"
          "v_pk_mul_f32 v[198:199], v[198:199], v[22:23]\n\t"
          "v_cvt_pk_bf16_f32 v28, v180, v181\n\t"
          "v_cvt_pk_bf16_f32 v29, v182, v183\n\t"
          "v_cvt_pk_bf16_f32 v30, v196, v197\n\t"
          "v_cvt_pk_bf16_f32 v31, v198, v199\n\t"
          "buffer_store_dwordx4 v[28:31], %[vx], %[ws], s55 offen offset:1024 sc1\n\t"
          "v_pk_mul_f32 v[16:17], v[72:73], v[72:73]\n\t"
          "v_pk_mul_f32 v[18:19], v[74:75], v[74:75]\n\t"
          "v_pk_mul_f32 v[20:21], v[88:89], v[88:89]\n\t"
          "v_pk_mul_f32 v[22:23], v[90:91], v[90:91]\n\t"
          "v_pk_fma_f32 v[16:17], v[16:17], s[52:53], s[52:53] op_sel:[0,0,1] op_sel_hi:[1,0,1]\n\t"
          "v_pk_fma_f32 v[18:19], v[18:19], s[52:53], s[52:53] op_sel:[0,0,1] op_sel_hi:[1,0,1]\n\t"
          "v_pk_fma_f32 v[20:21], v[20:21], s[52:53], s[52:53] op_sel:[0,0,1] op_sel_hi:[1,0,1]\n\t"
          "v_pk_fma_f32 v[22:23], v[22:23], s[52:53], s[52:53] op_sel:[0,0,1] op_sel_hi:[1,0,1]\n\t"
          "v_pk_mul_f32 v[16:17], v[16:17], v[72:73]\n\t"
          "v_pk_mul_f32 v[18:19], v[18:19], v[74:75]\n\t"
          "v_pk_mul_f32 v[20:21], v[20:21], v[88:89]\n\t"
          "v_pk_mul_f32 v[22:23], v[22:23], v[90:91]\n\t"
          "v_exp_f32 v16, v16\n\t"
          "v_exp_f32 v17, v17\n\t"
          "v_exp_f32 v18, v18\n\t"
          "v_exp_f32 v19, v19\n\t"
          "v_exp_f32 v20, v20\n\t"
          "v_exp_f32 v21, v21\n\t"
          "v_exp_f32 v22, v22\n\t"
          "v_exp_f32 v23, v23\n\t"
          "v_pk_mul_f32 v[184:185], v[72:73], v[184:185]\n\t"
          "v_pk_mul_f32 v[186:187], v[74:75], v[186:187]\n\t"
          "v_pk_mul_f32 v[200:201], v[88:89], v[200:201]\n\t"
          "v_pk_mul_f32 v[202:203], v[90:91], v[202:203]\n\t"
          "v_pk_add_f32 v[16:17], v[16:17], 1.0 op_sel_hi:[1,0]\n\t"
          "v_pk_add_f32 v[18:19], v[18:19], 1.0 op_sel_hi:[1,0]\n\t"
          "v_pk_add_f32 v[20:21], v[20:21], 1.0 op_sel_hi:[1,0]\n\t"
          "v_pk_add_f32 v[22:23], v[22:23], 1.0 op_sel_hi:[1,0]\n\t"
          "v_rcp_f32 v16, v16\n\t"
          "v_rcp_f32 v17, v17\n\t"
          "v_rcp_f32 v18, v18\n\t"
          "v_rcp_f32 v19, v19\n\t"
          "v_rcp_f32 v20, v20\n\t"
          "v_rcp_f32 v21, v21\n\t"
          "v_rcp_f32 v22, v22\n\t"
          "v_rcp_f32 v23, v23\n\t"
          "v_pk_mul_f32 v[184:185], v[184:185], v[16:17]\n\t"
          "v_pk_mul_f32 v[186:187], v[186:187], v[18:19]\n\t"
          "v_pk_mul_f32 v[200:201], v[200:201], v[20:21]\n\t"
          "v_pk_mul_f32 v[202:203], v[202:203], v[22:23]\n\t"
          "v_cvt_pk_bf16_f32 v24, v184, v185\n\t"
          "v_cvt_pk_bf16_f32 v25, v186, v187\n\t"
          "v_cvt_pk_bf16_f32 v26, v200, v201\n\t"
          "v_cvt_pk_bf16_f32 v27, v202, v203\n\t"
          "buffer_store_dwordx4 v[24:27], %[vx], %[ws], s55 offen offset:2048 sc1\n\t"
          "v_pk_mul_f32 v[16:17], v[76:77], v[76:77]\n\t"
          "v_pk_mul_f32 v[18:19], v[78:79], v[78:79]\n\t"
          "v_pk_mul_f32 v[20:21], v[92:93], v[92:93]\n\t"
          "v_pk_mul_f32 v[22:23], v[94:95], v[94:95]\n\t"
          "v_pk_fma_f32 v[16:17], v[16:17], s[52:53], s[52:53] op_sel:[0,0,1] op_sel_hi:[1,0,1]\n\t"
          "v_pk_fma_f32 v[18:19], v[18:19], s[52:53], s[52:53] op_sel:[0,0,1] op_sel_hi:[1,0,1]\n\t"
          "v_pk_fma_f32 v[20:21], v[20:21], s[52:53], s[52:53] op_sel:[0,0,1] op_sel_hi:[1,0,1]\n\t"
          "v_pk_fma_f32 v[22:23], v[22:23], s[52:53], s[52:53] op_sel:[0,0,1] op_sel_hi:[1,0,1]\n\t"
          "v_pk_mul_f32 v[16:17], v[16:17], v[76:77]\n\t"
          "v_pk_mul_f32 v[18:19], v[18:19], v[78:79]\n\t"
          "v_pk_mul_f32 v[20:21], v[20:21], v[92:93]\n\t"
          "v_pk_mul_f32 v[22:23], v[22:23], v[94:95]\n\t"
          "v_exp_f32 v16, v16\n\t"
          "v_exp_f32 v17, v17\n\t"
          "v_exp_f32 v18, v18\n\t"
          "v_exp_f32 v19, v19\n\t"
          "v_exp_f32 v20, v20\n\t"
          "v_exp_f32 v21, v21\n\t"
          "v_exp_f32 v22, v22\n\t"
          "v_exp_f32 v23, v23\n\t"
          "v_pk_mul_f32 v[188:189], v[76:77], v[188:189]\n\t"
          "v_pk_mul_f32 v[190:191], v[78:79], v[190:191]\n\t"
          "v_pk_mul_f32 v[204:205], v[92:93], v[204:205]\n\t"
          "v_pk_mul_f32 v[206:207], v[94:95], v[206:207]\n\t"
          "v_pk_add_f32 v[16:17], v[16:17], 1.0 op_sel_hi:[1,0]\n\t"
          "v_pk_add_f32 v[18:19], v[18:19], 1.0 op_sel_hi:[1,0]\n\t"
          "v_pk_add_f32 v[20:21], v[20:21], 1.0 op_sel_hi:[1,0]\n\t"
          "v_pk_add_f32 v[22:23], v[22:23], 1.0 op_sel_hi:[1,0]\n\t"
          "v_rcp_f32 v16, v16\n\t"
          "v_rcp_f32 v17, v17\n\t"
          "v_rcp_f32 v18, v18\n\t"
          "v_rcp_f32 v19, v19\n\t"
          "v_rcp_f32 v20, v20\n\t"
          "v_rcp_f32 v21, v21\n\t"
          "v_rcp_f32 v22, v22\n\t"
          "v_rcp_f32 v23, v23\n\t"
          "v_pk_mul_f32 v[188:189], v[188:189], v[16:17]\n\t"
          "v_pk_mul_f32 v[190:191], v[190:191], v[18:19]\n\t"
          "v_pk_mul_f32 v[204:205], v[204:205], v[20:21]\n\t"
          "v_pk_mul_f32 v[206:207], v[206:207], v[22:23]\n\t"
          "v_cvt_pk_bf16_f32 v28, v188, v189\n\t"
          "v_cvt_pk_bf16_f32 v29, v190, v191\n\t"
          "v_cvt_pk_bf16_f32 v30, v204, v205\n\t"
          "v_cvt_pk_bf16_f32 v31, v206, v207\n\t"
          "buffer_store_dwordx4 v[28:31], %[vx], %[ws], s55 offen offset:3072 sc1\n\t"
          "v_pk_mul_f32 v[16:17], v[96:97], v[96:97]\n\t"
          "v_pk_mul_f32 v[18:19], v[98:99], v[98:99]\n\t"
          "v_pk_mul_f32 v[20:21], v[112:113], v[112:113]\n\t"
          "v_pk_mul_f32 v[22:23], v[114:115], v[114:115]\n\t"
          "v_pk_fma_f32 v[16:17], v[16:17], s[52:53], s[52:53] op_sel:[0,0,1] op_sel_hi:[1,0,1]\n\t"
          "v_pk_fma_f32 v[18:19], v[18:19], s[52:53], s[52:53] op_sel:[0,0,1] op_sel_hi:[1,0,1]\n\t"
          "v_pk_fma_f32 v[20:21], v[20:21], s[52:53], s[52:53] op_sel:[0,0,1] op_sel_hi:[1,0,1]\n\t"
          "v_pk_fma_f32 v[22:23], v[22:23], s[52:53], s[52:53] op_sel:[0,0,1] op_sel_hi:[1,0,1]\n\t"
          "v_pk_mul_f32 v[16:17], v[16:17], v[96:97]\n\t"
          "v_pk_mul_f32 v[18:19], v[18:19], v[98:99]\n\t"
          "v_pk_mul_f32 v[20:21], v[20:21], v[112:113]\n\t"
          "v_pk_mul_f32 v[22:23], v[22:23], v[114:115]\n\t"
          "v_exp_f32 v16, v16\n\t"
          "v_exp_f32 v17, v17\n\t"
          "v_exp_f32 v18, v18\n\t"
          "v_exp_f32 v19, v19\n\t"
          "v_exp_f32 v20, v20\n\t"
          "v_exp_f32 v21, v21\n\t"
          "v_exp_f32 v22, v22\n\t"
          "v_exp_f32 v23, v23\n\t"
          "v_pk_mul_f32 v[208:209], v[96:97], v[208:209]\n\t"
          "v_pk_mul_f32 v[210:211], v[98:99], v[210:211]\n\t"
          "v_pk_mul_f32 v[224:225], v[112:113], v[224:225]\n\t"
          "v_pk_mul_f32 v[226:227], v[114:115], v[226:227]\n\t"
          "v_pk_add_f32 v[16:17], v[16:17], 1.0 op_sel_hi:[1,0]\n\t"
          "v_pk_add_f32 v[18:19], v[18:19], 1.0 op_sel_hi:[1,0]\n\t"
          "v_pk_add_f32 v[20:21], v[20:21], 1.0 op_sel_hi:[1,0]\n\t"
          "v_pk_add_f32 v[22:23], v[22:23], 1.0 op_sel_hi:[1,0]\n\t"
          "v_rcp_f32 v16, v16\n\t"
          "v_rcp_f32 v17, v17\n\t"
          "v_rcp_f32 v18, v18\n\t"
          "v_rcp_f32 v19, v19\n\t"
          "v_rcp_f32 v20, v20\n\t"
          "v_rcp_f32 v21, v21\n\t"
          "v_rcp_f32 v22, v22\n\t"
          "v_rcp_f32 v23, v23\n\t"
          "v_pk_mul_f32 v[208:209], v[208:209], v[16:17]\n\t"
          "v_pk_mul_f32 v[210:211], v[210:211], v[18:19]\n\t"
          "v_pk_mul_f32 v[224:225], v[224:225], v[20:21]\n\t"
          "v_pk_mul_f32 v[226:227], v[226:227], v[22:23]\n\t"
          "v_cvt_pk_bf16_f32 v24, v208, v209\n\t"
          "v_cvt_pk_bf16_f32 v25, v210, v211\n\t"
          "v_cvt_pk_bf16_f32 v26, v224, v225\n\t"
          "v_cvt_pk_bf16_f32 v27, v226, v227\n\t"
          "buffer_store_dwordx4 v[24:27], %[vx], %[ws], s56 offen offset:0 sc1\n\t"
          "v_pk_mul_f32 v[16:17], v[100:101], v[100:101]\n\t"
          "v_pk_mul_f32 v[18:19], v[102:103], v[102:103]\n\t"
          "v_pk_mul_f32 v[20:21], v[116:117], v[116:117]\n\t"
          "v_pk_mul_f32 v[22:23], v[118:119], v[118:119]\n\t"
          "v_pk_fma_f32 v[16:17], v[16:17], s[52:53], s[52:53] op_sel:[0,0,1] op_sel_hi:[1,0,1]\n\t"
          "v_pk_fma_f32 v[18:19], v[18:19], s[52:53], s[52:53] op_sel:[0,0,1] op_sel_hi:[1,0,1]\n\t"
          "v_pk_fma_f32 v[20:21], v[20:21], s[52:53], s[52:53] op_sel:[0,0,1] op_sel_hi:[1,0,1]\n\t"
          "v_pk_fma_f32 v[22:23], v[22:23], s[52:53], s[52:53] op_sel:[0,0,1] op_sel_hi:[1,0,1]\n\t"
          "v_pk_mul_f32 v[16:17], v[16:17], v[100:101]\n\t"
          "v_pk_mul_f32 v[18:19], v[18:19], v[102:103]\n\t"
          "v_pk_mul_f32 v[20:21], v[20:21], v[116:117]\n\t"
          "v_pk_mul_f32 v[22:23], v[22:23], v[118:119]\n\t"
          "v_exp_f32 v16, v16\n\t"
          "v_exp_f32 v17, v17\n\t"
          "v_exp_f32 v18, v18\n\t"
          "v_exp_f32 v19, v19\n\t"
          "v_exp_f32 v20, v20\n\t"
          "v_exp_f32 v21, v21\n\t"
          "v_exp_f32 v22, v22\n\t"
          "v_exp_f32 v23, v23\n\t"
          "v_pk_mul_f32 v[212:213], v[100:101], v[212:213]\n\t"
          "v_pk_mul_f32 v[214:215], v[102:103], v[214:215]\n\t"
          "v_pk_mul_f32 v[228:229], v[116:117], v[228:229]\n\t"
          "v_pk_mul_f32 v[230:231], v[118:119], v[230:231]\n\t"
          "v_pk_add_f32 v[16:17], v[16:17], 1.0 op_sel_hi:[1,0]\n\t"
          "v_pk_add_f32 v[18:19], v[18:19], 1.0 op_sel_hi:[1,0]\n\t"
          "v_pk_add_f32 v[20:21], v[20:21], 1.0 op_sel_hi:[1,0]\n\t"
          "v_pk_add_f32 v[22:23], v[22:23], 1.0 op_sel_hi:[1,0]\n\t"
          "v_rcp_f32 v16, v16\n\t"
          "v_rcp_f32 v17, v17\n\t"
          "v_rcp_f32 v18, v18\n\t"
          "v_rcp_f32 v19, v19\n\t"
          "v_rcp_f32 v20, v20\n\t"
          "v_rcp_f32 v21, v21\n\t"
          "v_rcp_f32 v22, v22\n\t"
          "v_rcp_f32 v23, v23\n\t"
          "v_pk_mul_f32 v[212:213], v[212:213], v[16:17]\n\t"
          "v_pk_mul_f32 v[214:215], v[214:215], v[18:19]\n\t"
          "v_pk_mul_f32 v[228:229], v[228:229], v[20:21]\n\t"
          "v_pk_mul_f32 v[230:231], v[230:231], v[22:23]\n\t"
          "v_cvt_pk_bf16_f32 v28, v212, v213\n\t"
          "v_cvt_pk_bf16_f32 v29, v214, v215\n\t"
          "v_cvt_pk_bf16_f32 v30, v228, v229\n\t"
          "v_cvt_pk_bf16_f32 v31, v230, v231\n\t"
          "buffer_store_dwordx4 v[28:31], %[vx], %[ws], s56 offen offset:1024 sc1\n\t"
          "v_pk_mul_f32 v[16:17], v[104:105], v[104:105]\n\t"
          "v_pk_mul_f32 v[18:19], v[106:107], v[106:107]\n\t"
          "v_pk_mul_f32 v[20:21], v[120:121], v[120:121]\n\t"
          "v_pk_mul_f32 v[22:23], v[122:123], v[122:123]\n\t"
          "v_pk_fma_f32 v[16:17], v[16:17], s[52:53], s[52:53] op_sel:[0,0,1] op_sel_hi:[1,0,1]\n\t"
          "v_pk_fma_f32 v[18:19], v[18:19], s[52:53], s[52:53] op_sel:[0,0,1] op_sel_hi:[1,0,1]\n\t"
          "v_pk_fma_f32 v[20:21], v[20:21], s[52:53], s[52:53] op_sel:[0,0,1] op_sel_hi:[1,0,1]\n\t"
          "v_pk_fma_f32 v[22:23], v[22:23], s[52:53], s[52:53] op_sel:[0,0,1] op_sel_hi:[1,0,1]\n\t"
          "v_pk_mul_f32 v[16:17], v[16:17], v[104:105]\n\t"
          "v_pk_mul_f32 v[18:19], v[18:19], v[106:107]\n\t"
          "v_pk_mul_f32 v[20:21], v[20:21], v[120:121]\n\t"
          "v_pk_mul_f32 v[22:23], v[22:23], v[122:123]\n\t"
          "v_exp_f32 v16, v16\n\t"
          "v_exp_f32 v17, v17\n\t"
          "v_exp_f32 v18, v18\n\t"
          "v_exp_f32 v19, v19\n\t"
          "v_exp_f32 v20, v20\n\t"
          "v_exp_f32 v21, v21\n\t"
          "v_exp_f32 v22, v22\n\t"
          "v_exp_f32 v23, v23\n\t"
          "v_pk_mul_f32 v[216:217], v[104:105], v[216:217]\n\t"
          "v_pk_mul_f32 v[218:219], v[106:107], v[218:219]\n\t"
          "v_pk_mul_f32 v[232:233], v[120:121], v[232:233]\n\t"
          "v_pk_mul_f32 v[234:235], v[122:123], v[234:235]\n\t"
          "v_pk_add_f32 v[16:17], v[16:17], 1.0 op_sel_hi:[1,0]\n\t"
          "v_pk_add_f32 v[18:19], v[18:19], 1.0 op_sel_hi:[1,0]\n\t"
          "v_pk_add_f32 v[20:21], v[20:21], 1.0 op_sel_hi:[1,0]\n\t"
          "v_pk_add_f32 v[22:23], v[22:23], 1.0 op_sel_hi:[1,0]\n\t"
          "v_rcp_f32 v16, v16\n\t"
          "v_rcp_f32 v17, v17\n\t"
          "v_rcp_f32 v18, v18\n\t"
          "v_rcp_f32 v19, v19\n\t"
          "v_rcp_f32 v20, v20\n\t"
          "v_rcp_f32 v21, v21\n\t"
          "v_rcp_f32 v22, v22\n\t"
          "v_rcp_f32 v23, v23\n\t"
          "v_pk_mul_f32 v[216:217], v[216:217], v[16:17]\n\t"
          "v_pk_mul_f32 v[218:219], v[218:219], v[18:19]\n\t"
          "v_pk_mul_f32 v[232:233], v[232:233], v[20:21]\n\t"
          "v_pk_mul_f32 v[234:235], v[234:235], v[22:23]\n\t"
          "v_cvt_pk_bf16_f32 v24, v216, v217\n\t"
          "v_cvt_pk_bf16_f32 v25, v218, v219\n\t"
          "v_cvt_pk_bf16_f32 v26, v232, v233\n\t"
          "v_cvt_pk_bf16_f32 v27, v234, v235\n\t"
          "buffer_store_dwordx4 v[24:27], %[vx], %[ws], s56 offen offset:2048 sc1\n\t"
          "v_pk_mul_f32 v[16:17], v[108:109], v[108:109]\n\t"
          "v_pk_mul_f32 v[18:19], v[110:111], v[110:111]\n\t"
          "v_pk_mul_f32 v[20:21], v[124:125], v[124:125]\n\t"
          "v_pk_mul_f32 v[22:23], v[126:127], v[126:127]\n\t"
          "v_pk_fma_f32 v[16:17], v[16:17], s[52:53], s[52:53] op_sel:[0,0,1] op_sel_hi:[1,0,1]\n\t"
          "v_pk_fma_f32 v[18:19], v[18:19], s[52:53], s[52:53] op_sel:[0,0,1] op_sel_hi:[1,0,1]\n\t"
          "v_pk_fma_f32 v[20:21], v[20:21], s[52:53], s[52:53] op_sel:[0,0,1] op_sel_hi:[1,0,1]\n\t"
          "v_pk_fma_f32 v[22:23], v[22:23], s[52:53], s[52:53] op_sel:[0,0,1] op_sel_hi:[1,0,1]\n\t"
          "v_pk_mul_f32 v[16:17], v[16:17], v[108:109]\n\t"
          "v_pk_mul_f32 v[18:19], v[18:19], v[110:111]\n\t"
          "v_pk_mul_f32 v[20:21], v[20:21], v[124:125]\n\t"
          "v_pk_mul_f32 v[22:23], v[22:23], v[126:127]\n\t"
          "v_exp_f32 v16, v16\n\t"
          "v_exp_f32 v17, v17\n\t"
          "v_exp_f32 v18, v18\n\t"
          "v_exp_f32 v19, v19\n\t"
          "v_exp_f32 v20, v20\n\t"
          "v_exp_f32 v21, v21\n\t"
          "v_exp_f32 v22, v22\n\t"
          "v_exp_f32 v23, v23\n\t"
          "v_pk_mul_f32 v[220:221], v[108:109], v[220:221]\n\t"
          "v_pk_mul_f32 v[222:223], v[110:111], v[222:223]\n\t"
          "v_pk_mul_f32 v[236:237], v[124:125], v[236:237]\n\t"
          "v_pk_mul_f32 v[238:239], v[126:127], v[238:239]\n\t"
          "v_pk_add_f32 v[16:17], v[16:17], 1.0 op_sel_hi:[1,0]\n\t"
          "v_pk_add_f32 v[18:19], v[18:19], 1.0 op_sel_hi:[1,0]\n\t"
          "v_pk_add_f32 v[20:21], v[20:21], 1.0 op_sel_hi:[1,0]\n\t"
          "v_pk_add_f32 v[22:23], v[22:23], 1.0 op_sel_hi:[1,0]\n\t"
          "v_rcp_f32 v16, v16\n\t"
          "v_rcp_f32 v17, v17\n\t"
          "v_rcp_f32 v18, v18\n\t"
          "v_rcp_f32 v19, v19\n\t"
          "v_rcp_f32 v20, v20\n\t"
          "v_rcp_f32 v21, v21\n\t"
          "v_rcp_f32 v22, v22\n\t"
          "v_rcp_f32 v23, v23\n\t"
          "v_pk_mul_f32 v[220:221], v[220:221], v[16:17]\n\t"
          "v_pk_mul_f32 v[222:223], v[222:223], v[18:19]\n\t"
          "v_pk_mul_f32 v[236:237], v[236:237], v[20:21]\n\t"
          "v_pk_mul_f32 v[238:239], v[238:239], v[22:23]\n\t"
          "v_cvt_pk_bf16_f32 v28, v220, v221\n\t"
          "v_cvt_pk_bf16_f32 v29, v222, v223\n\t"
          "v_cvt_pk_bf16_f32 v30, v236, v237\n\t"
          "v_cvt_pk_bf16_f32 v31, v238, v239\n\t"
          "buffer_store_dwordx4 v[28:31], %[vx], %[ws], s56 offen offset:3072 sc1\n\t"
          "v_pk_mul_f32 v[16:17], v[128:129], v[128:129]\n\t"
          "v_pk_mul_f32 v[18:19], v[130:131], v[130:131]\n\t"
          "v_pk_mul_f32 v[20:21], v[132:133], v[132:133]\n\t"
          "v_pk_mul_f32 v[22:23], v[134:135], v[134:135]\n\t"
          "v_pk_fma_f32 v[16:17], v[16:17], s[52:53], s[52:53] op_sel:[0,0,1] op_sel_hi:[1,0,1]\n\t"
          "v_pk_fma_f32 v[18:19], v[18:19], s[52:53], s[52:53] op_sel:[0,0,1] op_sel_hi:[1,0,1]\n\t"
          "v_pk_fma_f32 v[20:21], v[20:21], s[52:53], s[52:53] op_sel:[0,0,1] op_sel_hi:[1,0,1]\n\t"
          "v_pk_fma_f32 v[22:23], v[22:23], s[52:53], s[52:53] op_sel:[0,0,1] op_sel_hi:[1,0,1]\n\t"
          "v_pk_mul_f32 v[16:17], v[16:17], v[128:129]\n\t"
          "v_pk_mul_f32 v[18:19], v[18:19], v[130:131]\n\t"
          "v_pk_mul_f32 v[20:21], v[20:21], v[132:133]\n\t"
          "v_pk_mul_f32 v[22:23], v[22:23], v[134:135]\n\t"
          "v_exp_f32 v16, v16\n\t"
          "v_exp_f32 v17, v17\n\t"
          "v_exp_f32 v18, v18\n\t"
          "v_exp_f32 v19, v19\n\t"
          "v_exp_f32 v20, v20\n\t"
          "v_exp_f32 v21, v21\n\t"
          "v_exp_f32 v22, v22\n\t"
          "v_exp_f32 v23, v23\n\t"
          "v_pk_mul_f32 v[240:241], v[128:129], v[240:241]\n\t"
          "v_pk_mul_f32 v[242:243], v[130:131], v[242:243]\n\t"
          "v_pk_mul_f32 v[244:245], v[132:133], v[244:245]\n\t"
          "v_pk_mul_f32 v[246:247], v[134:135], v[246:247]\n\t"
          "v_pk_add_f32 v[16:17], v[16:17], 1.0 op_sel_hi:[1,0]\n\t"
          "v_pk_add_f32 v[18:19], v[18:19], 1.0 op_sel_hi:[1,0]\n\t"
          "v_pk_add_f32 v[20:21], v[20:21], 1.0 op_sel_hi:[1,0]\n\t"
          "v_pk_add_f32 v[22:23], v[22:23], 1.0 op_sel_hi:[1,0]\n\t"
          "v_rcp_f32 v16, v16\n\t"
          "v_rcp_f32 v17, v17\n\t"
          "v_rcp_f32 v18, v18\n\t"
          "v_rcp_f32 v19, v19\n\t"
          "v_rcp_f32 v20, v20\n\t"
          "v_rcp_f32 v21, v21\n\t"
          "v_rcp_f32 v22, v22\n\t"
          "v_rcp_f32 v23, v23\n\t"
          "v_pk_mul_f32 v[240:241], v[240:241], v[16:17]\n\t"
          "v_pk_mul_f32 v[242:243], v[242:243], v[18:19]\n\t"
          "v_pk_mul_f32 v[244:245], v[244:245], v[20:21]\n\t"
          "v_pk_mul_f32 v[246:247], v[246:247], v[22:23]\n\t"
          "v_cvt_pk_bf16_f32 v24, v240, v241\n\t"
          "v_cvt_pk_bf16_f32 v25, v242, v243\n\t"
          "v_cvt_pk_bf16_f32 v26, v244, v245\n\t"
          "v_cvt_pk_bf16_f32 v27, v246, v247\n\t"
          "s_nop 1\n\t"
          "v_permlane32_swap_b32 v24, v26\n\t"
          "v_permlane32_swap_b32 v25, v27\n\t"
          "buffer_store_dwordx4 v[24:27], v15, %[ws], s57 offen offset:0 sc1\n\t"
          "v_pk_mul_f32 v[16:17], v[136:137], v[136:137]\n\t"
          "v_pk_mul_f32 v[18:19], v[138:139], v[138:139]\n\t"
          "v_pk_mul_f32 v[20:21], v[140:141], v[140:141]\n\t"
          "v_pk_mul_f32 v[22:23], v[142:143], v[142:143]\n\t"
          "v_pk_fma_f32 v[16:17], v[16:17], s[52:53], s[52:53] op_sel:[0,0,1] op_sel_hi:[1,0,1]\n\t"
          "v_pk_fma_f32 v[18:19], v[18:19], s[52:53], s[52:53] op_sel:[0,0,1] op_sel_hi:[1,0,1]\n\t"
          "v_pk_fma_f32 v[20:21], v[20:21], s[52:53], s[52:53] op_sel:[0,0,1] op_sel_hi:[1,0,1]\n\t"
          "v_pk_fma_f32 v[22:23], v[22:23], s[52:53], s[52:53] op_sel:[0,0,1] op_sel_hi:[1,0,1]\n\t"
          "v_pk_mul_f32 v[16:17], v[16:17], v[136:137]\n\t"
          "v_pk_mul_f32 v[18:19], v[18:19], v[138:139]\n\t"
          "v_pk_mul_f32 v[20:21], v[20:21], v[140:141]\n\t"
          "v_pk_mul_f32 v[22:23], v[22:23], v[142:143]\n\t"
          "v_exp_f32 v16, v16\n\t"
          "v_exp_f32 v17, v17\n\t"
          "v_exp_f32 v18, v18\n\t"
          "v_exp_f32 v19, v19\n\t"
          "v_exp_f32 v20, v20\n\t"
          "v_exp_f32 v21, v21\n\t"
          "v_exp_f32 v22, v22\n\t"
          "v_exp_f32 v23, v23\n\t"
          "v_pk_mul_f32 v[248:249], v[136:137], v[248:249]\n\t"
          "v_pk_mul_f32 v[250:251], v[138:139], v[250:251]\n\t"
          "v_pk_mul_f32 v[252:253], v[140:141], v[252:253]\n\t"
          "v_pk_mul_f32 v[254:255], v[142:143], v[254:255]\n\t"
          "v_pk_add_f32 v[16:17], v[16:17], 1.0 op_sel_hi:[1,0]\n\t"
          "v_pk_add_f32 v[18:19], v[18:19], 1.0 op_sel_hi:[1,0]\n\t"
          "v_pk_add_f32 v[20:21], v[20:21], 1.0 op_sel_hi:[1,0]\n\t"
          "v_pk_add_f32 v[22:23], v[22:23], 1.0 op_sel_hi:[1,0]\n\t"
          "v_rcp_f32 v16, v16\n\t"
          "v_rcp_f32 v17, v17\n\t"
          "v_rcp_f32 v18, v18\n\t"
          "v_rcp_f32 v19, v19\n\t"
          "v_rcp_f32 v20, v20\n\t"
          "v_rcp_f32 v21, v21\n\t"
          "v_rcp_f32 v22, v22\n\t"
          "v_rcp_f32 v23, v23\n\t"
          "v_pk_mul_f32 v[248:249], v[248:249], v[16:17]\n\t"
          "v_pk_mul_f32 v[250:251], v[250:251], v[18:19]\n\t"
          "v_pk_mul_f32 v[252:253], v[252:253], v[20:21]\n\t"
          "v_pk_mul_f32 v[254:255], v[254:255], v[22:23]\n\t"
          "v_cvt_pk_bf16_f32 v28, v248, v249\n\t"
          "v_cvt_pk_bf16_f32 v29, v250, v251\n\t"
          "v_cvt_pk_bf16_f32 v30, v252, v253\n\t"
          "v_cvt_pk_bf16_f32 v31, v254, v255\n\t"
          "s_nop 1\n\t"
          "v_permlane32_swap_b32 v28, v30\n\t"
          "v_permlane32_swap_b32 v29, v31\n\t"
          "buffer_store_dwordx4 v[28:31], v15, %[ws], s57 offen offset:2048 sc1\n\t"
          "s_nop 1\n\t"
          "v_mov_b64 v[24:25], %[tag]\n\t"
          "s_waitcnt vmcnt(0)\n\t"
          "s_mov_b64 exec, 1\n\t"
          "buffer_store_dwordx2 v[24:25], off, %[ws], %[fs1] sc1\n\t"
          "s_mov_b64 exec, -1\n\t"
          "s_mov_b32 s44, %[x2]\n\t"
          "s_add_u32 s48, %[x2], 4096\n\t"
          "v_add_u32 v16, %[r2], %[vx]\n\t"
          "v_xor_b32 v17, 4096, v16\n\t"
          "v_add_u32 v18, 65536, v16\n\t"
          "v_add_u32 v19, 65536, v17\n\t"
          "s_lshr_b32 s50, %[w], 1\n\t"
          "s_lshl_b32 s50, s50, 11\n\t"
          "s_lshl_b32 s51, %[w], 14\n\t"
          "s_add_u32 s51, s51, 65536\n\t"
          "v_add_u32 v14, s50, %[vx]\n\t"
          "v_subrev_u32 v15, s50, %[vx]\n\t"
          "v_add_u32 v15, 2048, v15\n\t"
          "v_add_u32 v6, s51, %[vx]\n\t"
          "v_xor_b32 v7, 32768, v6\n\t"
          "s_mov_b32 s52, %[e0]\n\t"
          "s_add_u32 s53, s52, 4096\n\t"
          "s_xor_b32 s54, s52, 8192\n\t"
          "s_add_u32 s55, s54, 4096\n\t"
          "s_cmp_lg_u32 %[layer], 0\n\t"
          "s_cselect_b32 s56, s52, 0x7ffffff0\n\t"
          "s_cselect_b32 s57, s53, 0x7ffffff0\n\t"
          "s_barrier\n\t"
          "ds_read_b128 a[128:131], v17 offset:0\n\t"
          "ds_read_b128 a[132:135], v17 offset:8192\n\t"
          "ds_read_b128 a[136:139], v17 offset:1024\n\t"
          "ds_read_b128 a[140:143], v17 offset:9216\n\t"
          "s_mov_b32 s58, 262144\n\t"
          "12:\n\t"
          "buffer_load_dwordx2 v[24:25], %[vp2], %[ws], 0 offen sc1\n\t"
          "s_waitcnt vmcnt(0)\n\t"
          "v_cmp_ne_u64 vcc, %[tag], v[24:25]\n\t"
          "s_nop 4\n\t"
          "s_cbranch_vccz 13f\n\t"
          "s_sub_u32 s58, s58, 1\n\t"
          "s_cmp_lg_u32 s58, 0\n\t"
          "s_cbranch_scc1 12b\n\t"
          "13:\n\t"
          "buffer_load_dwordx4 a[0:3], v14, %[ws], s44 offen offset:0 sc1\n\t"
          "buffer_load_dwordx4 a[4:7], v14, %[ws], s44 offen offset:1024 sc1\n\t"
          "buffer_load_dwordx4 a[8:11], v15, %[ws], s44 offen offset:0 sc1\n\t"
          "buffer_load_dwordx4 a[12:15], v15, %[ws], s44 offen offset:1024 sc1\n\t"
          "buffer_load_dwordx4 a[16:19], v14, %[ws], s48 offen offset:0 sc1\n\t"
          "buffer_load_dwordx4 a[20:23], v14, %[ws], s48 offen offset:1024 sc1\n\t"
          "buffer_load_dwordx4 a[24:27], v15, %[ws], s48 offen offset:0 sc1\n\t"
          "buffer_load_dwordx4 a[28:31], v15, %[ws], s48 offen offset:1024 sc1\n\t"
          "s_add_u32 s44, s44, 8192\n\t"
          "s_add_u32 s48, s48, 8192\n\t"
          "s_waitcnt vmcnt(0)\n\t"
          "s_waitcnt lgkmcnt(2)\n\t"
          "v_mfma_f32_16x16x32_bf16 v[192:195], a[128:131], a[0:3], 0\n\t"
          "ds_read_b128 a[144:147], v17 offset:2048\n\t"
          "v_mfma_f32_16x16x32_bf16 v[192:195], a[132:135], a[16:19], v[192:195]\n\t"
          "v_mfma_f32_16x16x32_bf16 v[196:199], a[132:135], a[20:23], 0\n\t"
          "buffer_load_dwordx4 a[32:35], v14, %[ws], s44 offen offset:0 sc1\n\t"
          "v_mfma_f32_16x16x32_bf16 v[196:199], a[128:131], a[4:7], v[196:199]\n\t"
          "v_mfma_f32_16x16x32_bf16 v[200:203], a[128:131], a[8:11], 0\n\t"
          "ds_read_b128 a[148:151], v17 offset:10240\n\t"
          "buffer_load_dwordx4 a[36:39], v14, %[ws], s44 offen offset:1024 sc1\n\t"
          "v_mfma_f32_16x16x32_bf16 v[200:203], a[132:135], a[24:27], v[200:203]\n\t"
          "v_mfma_f32_16x16x32_bf16 v[204:207], a[132:135], a[28:31], 0\n\t"
          "buffer_load_dwordx4 a[40:43], v15, %[ws], s44 offen offset:0 sc1\n\t"
          "v_mfma_f32_16x16x32_bf16 v[204:207], a[128:131], a[12:15], v[204:207]\n\t"
          "s_waitcnt lgkmcnt(2)\n\t"
          "v_mfma_f32_16x16x32_bf16 v[220:223], a[136:139], a[12:15], 0\n\t"
          "ds_read_b128 a[152:155], v17 offset:3072\n\t"
          "v_mfma_f32_16x16x32_bf16 v[220:223], a[140:143], a[28:31], v[220:223]\n\t"
          "buffer_load_dwordx4 a[44:47], v15, %[ws], s44 offen offset:1024 sc1\n\t"
          "v_mfma_f32_16x16x32_bf16 v[216:219], a[140:143], a[24:27], 0\n\t"
          "v_mfma_f32_16x16x32_bf16 v[216:219], a[136:139], a[8:11], v[216:219]\n\t"
          "buffer_load_dwordx4 a[48:51], v14, %[ws], s48 offen offset:0 sc1\n\t"
          "v_mfma_f32_16x16x32_bf16 v[212:215], a[136:139], a[4:7], 0\n\t"
          "ds_read_b128 a[156:159], v17 offset:11264\n\t"
          "v_mfma_f32_16x16x32_bf16 v[212:215], a[140:143], a[20:23], v[212:215]\n\t"
          "v_mfma_f32_16x16x32_bf16 v[208:211], a[140:143], a[16:19], 0\n\t"
          "buffer_load_dwordx4 a[52:55], v14, %[ws], s48 offen offset:1024 sc1\n\t"
          "v_mfma_f32_16x16x32_bf16 v[208:211], a[136:139], a[0:3], v[208:211]\n\t"
          "s_waitcnt lgkmcnt(2)\n\t"
          "v_mfma_f32_16x16x32_bf16 v[224:227], a[144:147], a[0:3], 0\n\t"
          "ds_read_b128 a[128:131], v16 offset:0\n\t"
          "buffer_load_dwordx4 a[56:59], v15, %[ws], s48 offen offset:0 sc1\n\t"
          "v_mfma_f32_16x16x32_bf16 v[224:227], a[148:151], a[16:19], v[224:227]\n\t"
          "v_mfma_f32_16x16x32_bf16 v[228:231], a[148:151], a[20:23], 0\n\t"
          "buffer_load_dwordx4 a[60:63], v15, %[ws], s48 offen offset:1024 sc1\n\t"
          "s_add_u32 s44, s44, 8192\n\t"
          "s_add_u32 s48, s48, 8192\n\t"
          "v_mfma_f32_16x16x32_bf16 v[228:231], a[144:147], a[4:7], v[228:231]\n\t"
          "v_mfma_f32_16x16x32_bf16 v[232:235], a[144:147], a[8:11], 0\n\t"
          "ds_read_b128 a[132:135], v16 offset:8192\n\t"
          "s_add_u32 m0, s59, 65536\n\t"
          "v_mfma_f32_16x16x32_bf16 v[232:235], a[148:151], a[24:27], v[232:235]\n\t"
          "buffer_load_dwordx4 %[vx], %[ws], s60 offen offset:0 sc1 lds\n\t"
          "v_mfma_f32_16x16x32_bf16 v[236:239], a[148:151], a[28:31], 0\n\t"
          "v_mfma_f32_16x16x32_bf16 v[236:239], a[144:147], a[12:15], v[236:239]\n\t"
          "buffer_load_dwordx4 a[64:67], v14, %[ws], s44 offen offset:0 sc1\n\t"
          "s_waitcnt lgkmcnt(2)\n\t"
          "v_mfma_f32_16x16x32_bf16 v[252:255], a[152:155], a[12:15], 0\n\t"
          "ds_read_b128 a[136:139], v16 offset:1024\n\t"
          "v_mfma_f32_16x16x32_bf16 v[252:255], a[156:159], a[28:31], v[252:255]\n\t"
          "v_mfma_f32_16x16x32_bf16 v[248:251], a[156:159], a[24:27], 0\n\t"
          "buffer_load_dwordx4 %[vx], %[ws], s60 offen offset:1024 sc1 lds\n\t"
          "v_mfma_f32_16x16x32_bf16 v[248:251], a[152:155], a[8:11], v[248:251]\n\t"
          "v_mfma_f32_16x16x32_bf16 v[244:247], a[152:155], a[4:7], 0\n\t"
          "ds_read_b128 a[140:143], v16 offset:9216\n\t"
          "buffer_load_dwordx4 a[68:71], v14, %[ws], s44 offen offset:1024 sc1\n\t"
          "v_mfma_f32_16x16x32_bf16 v[244:247], a[156:159], a[20:23], v[244:247]\n\t"
          "v_mfma_f32_16x16x32_bf16 v[240:243], a[156:159], a[16:19], 0\n\t"
          "v_mfma_f32_16x16x32_bf16 v[240:243], a[152:155], a[0:3], v[240:243]\n\t"
          "buffer_load_dwordx4 %[vx], %[ws], s60 offen offset:2048 sc1 lds\n\t"
          "s_waitcnt lgkmcnt(2)\n\t"
          "v_mfma_f32_16x16x32_bf16 v[128:131], a[128:131], a[0:3], 0\n\t"
          "ds_read_b128 a[144:147], v16 offset:2048\n\t"
          "v_mfma_f32_16x16x32_bf16 v[128:131], a[132:135], a[16:19], v[128:131]\n\t"
          "buffer_load_dwordx4 a[72:75], v15, %[ws], s44 offen offset:0 sc1\n\t"
          "v_mfma_f32_16x16x32_bf16 v[132:135], a[132:135], a[20:23], 0\n\t"
          "v_mfma_f32_16x16x32_bf16 v[132:135], a[128:131], a[4:7], v[132:135]\n\t"
          "buffer_load_dwordx4 %[vx], %[ws], s60 offen offset:3072 sc1 lds\n\t"
          "v_mfma_f32_16x16x32_bf16 v[136:139], a[128:131], a[8:11], 0\n\t"
          "ds_read_b128 a[148:151], v16 offset:10240\n\t"
          "v_mfma_f32_16x16x32_bf16 v[136:139], a[132:135], a[24:27], v[136:139]\n\t"
          "v_mfma_f32_16x16x32_bf16 v[140:143], a[132:135], a[28:31], 0\n\t"
          "buffer_load_dwordx4 a[76:79], v15, %[ws], s44 offen offset:1024 sc1\n\t"
          "v_mfma_f32_16x16x32_bf16 v[140:143], a[128:131], a[12:15], v[140:143]\n\t"
          "s_add_u32 m0, s59, 69632\n\t"
          "s_waitcnt lgkmcnt(2)\n\t"
          "v_mfma_f32_16x16x32_bf16 v[156:159], a[136:139], a[12:15], 0\n\t"
          "ds_read_b128 a[152:155], v16 offset:3072\n\t"
          "buffer_load_dwordx4 %[vx], %[ws], s61 offen offset:0 sc1 lds\n\t"
          "v_mfma_f32_16x16x32_bf16 v[156:159], a[140:143], a[28:31], v[156:159]\n\t"
          "v_mfma_f32_16x16x32_bf16 v[152:155], a[140:143], a[24:27], 0\n\t"
          "v_mfma_f32_16x16x32_bf16 v[152:155], a[136:139], a[8:11], v[152:155]\n\t"
          "buffer_load_dwordx4 a[80:83], v14, %[ws], s48 offen offset:0 sc1\n\t"
          "v_mfma_f32_16x16x32_bf16 v[148:151], a[136:139], a[4:7], 0\n\t"
          "ds_read_b128 a[156:159], v16 offset:11264\n\t"
          "v_mfma_f32_16x16x32_bf16 v[148:151], a[140:143], a[20:23], v[148:151]\n\t"
          "buffer_load_dwordx4 %[vx], %[ws], s61 offen offset:1024 sc1 lds\n\t"
          "v_mfma_f32_16x16x32_bf16 v[144:147], a[140:143], a[16:19], 0\n\t"
          "v_mfma_f32_16x16x32_bf16 v[144:147], a[136:139], a[0:3], v[144:147]\n\t"
          "buffer_load_dwordx4 a[84:87], v14, %[ws], s48 offen offset:1024 sc1\n\t"
          "s_barrier\n\t"
          "s_waitcnt lgkmcnt(2)\n\t"
          "v_mfma_f32_16x16x32_bf16 v[160:163], a[144:147], a[0:3], 0\n\t"
          "ds_read_b128 a[128:131], v17 offset:32768\n\t"
          "v_mfma_f32_16x16x32_bf16 v[160:163], a[148:151], a[16:19], v[160:163]\n\t"
          "v_mfma_f32_16x16x32_bf16 v[164:167], a[148:151], a[20:23], 0\n\t"
          "buffer_load_dwordx4 %[vx], %[ws], s61 offen offset:2048 sc1 lds\n\t"
          "v_mfma_f32_16x16x32_bf16 v[164:167], a[144:147], a[4:7], v[164:167]\n\t"
          "v_mfma_f32_16x16x32_bf16 v[168:171], a[144:147], a[8:11], 0\n\t"
          "ds_read_b128 a[132:135], v17 offset:40960\n\t"
          "buffer_load_dwordx4 a[88:91], v15, %[ws], s48 offen offset:0 sc1\n\t"
          "v_mfma_f32_16x16x32_bf16 v[168:171], a[148:151], a[24:27], v[168:171]\n\t"
          "v_mfma_f32_16x16x32_bf16 v[172:175], a[148:151], a[28:31], 0\n\t"
          "v_mfma_f32_16x16x32_bf16 v[172:175], a[144:147], a[12:15], v[172:175]\n\t"
          "buffer_load_dwordx4 %[vx], %[ws], s61 offen offset:3072 sc1 lds\n\t"
          "s_add_u32 s60, s60, 16384\n\t"
          "s_add_u32 s61, s61, 16384\n\t"
          "s_waitcnt lgkmcnt(2)\n\t"
          "v_mfma_f32_16x16x32_bf16 v[188:191], a[152:155], a[12:15], 0\n\t"
          "ds_read_b128 a[136:139], v17 offset:33792\n\t"
          "v_mfma_f32_16x16x32_bf16 v[188:191], a[156:159], a[28:31], v[188:191]\n\t"
          "buffer_load_dwordx4 a[92:95], v15, %[ws], s48 offen offset:1024 sc1\n\t"
          "s_add_u32 s44, s44, 8192\n\t"
          "s_add_u32 s48, s48, 8192\n\t"
          "v_mfma_f32_16x16x32_bf16 v[184:187], a[156:159], a[24:27], 0\n\t"
          "v_mfma_f32_16x16x32_bf16 v[184:187], a[152:155], a[8:11], v[184:187]\n\t"
          "v_mfma_f32_16x16x32_bf16 v[180:183], a[152:155], a[4:7], 0\n\t"
          "ds_read_b128 a[140:143], v17 offset:41984\n\t"
          "v_mfma_f32_16x16x32_bf16 v[180:183], a[156:159], a[20:23], v[180:183]\n\t"
          "v_mfma_f32_16x16x32_bf16 v[176:179], a[156:159], a[16:19], 0\n\t"
          "v_mfma_f32_16x16x32_bf16 v[176:179], a[152:155], a[0:3], v[176:179]\n\t"
          "s_waitcnt vmcnt(16)\n\t"
          "s_waitcnt lgkmcnt(2)\n\t"
          "v_mfma_f32_16x16x32_bf16 v[192:195], a[128:131], a[32:35], v[192:195]\n\t"
          "ds_read_b128 a[144:147], v17 offset:34816\n\t"
          "v_mfma_f32_16x16x32_bf16 v[192:195], a[132:135], a[48:51], v[192:195]\n\t"
          "s_add_u32 m0, s59, 98304\n\t"
          "v_mfma_f32_16x16x32_bf16 v[196:199], a[132:135], a[52:55], v[196:199]\n\t"
          "buffer_load_dwordx4 %[vx], %[ws], s60 offen offset:0 sc1 lds\n\t"
          "v_mfma_f32_16x16x32_bf16 v[196:199], a[128:131], a[36:39], v[196:199]\n\t"
          "v_mfma_f32_16x16x32_bf16 v[200:203], a[128:131], a[40:43], v[200:203]\n\t"
          "ds_read_b128 a[148:151], v17 offset:43008\n\t"
          "v_mfma_f32_16x16x32_bf16 v[200:203], a[132:135], a[56:59], v[200:203]\n\t"
          "buffer_load_dwordx4 a[96:99], v14, %[ws], s44 offen offset:0 sc1\n\t"
          "v_mfma_f32_16x16x32_bf16 v[204:207], a[132:135], a[60:63], v[204:207]\n\t"
          "v_mfma_f32_16x16x32_bf16 v[204:207], a[128:131], a[44:47], v[204:207]\n\t"
          "s_waitcnt lgkmcnt(2)\n\t"
          "v_mfma_f32_16x16x32_bf16 v[220:223], a[136:139], a[44:47], v[220:223]\n\t"
          "ds_read_b128 a[152:155], v17 offset:35840\n\t"
          "v_mfma_f32_16x16x32_bf16 v[220:223], a[140:143], a[60:63], v[220:223]\n\t"
          "buffer_load_dwordx4 %[vx], %[ws], s60 offen offset:1024 sc1 lds\n\t"
          "v_mfma_f32_16x16x32_bf16 v[216:219], a[140:143], a[56:59], v[216:219]\n\t"
          "v_mfma_f32_16x16x32_bf16 v[216:219], a[136:139], a[40:43], v[216:219]\n\t"
          "v_mfma_f32_16x16x32_bf16 v[212:215], a[136:139], a[36:39], v[212:215]\n\t"
          "ds_read_b128 a[156:159], v17 offset:44032\n\t"
          "buffer_load_dwordx4 a[100:103], v14, %[ws], s44 offen offset:1024 sc1\n\t"
          "v_mfma_f32_16x16x32_bf16 v[212:215], a[140:143], a[52:55], v[212:215]\n\t"
          "v_mfma_f32_16x16x32_bf16 v[208:211], a[140:143], a[48:51], v[208:211]\n\t"
          "v_mfma_f32_16x16x32_bf16 v[208:211], a[136:139], a[32:35], v[208:211]\n\t"
          "s_waitcnt lgkmcnt(2)\n\t"
          "v_mfma_f32_16x16x32_bf16 v[224:227], a[144:147], a[32:35], v[224:227]\n\t"
          "ds_read_b128 a[128:131], v16 offset:32768\n\t"
          "buffer_load_dwordx4 %[vx], %[ws], s60 offen offset:2048 sc1 lds\n\t"
          "v_mfma_f32_16x16x32_bf16 v[224:227], a[148:151], a[48:51], v[224:227]\n\t"
          "v_mfma_f32_16x16x32_bf16 v[228:231], a[148:151], a[52:55], v[228:231]\n\t"
          "v_mfma_f32_16x16x32_bf16 v[228:231], a[144:147], a[36:39], v[228:231]\n\t"
          "v_mfma_f32_16x16x32_bf16 v[232:235], a[144:147], a[40:43], v[232:235]\n\t"
          "ds_read_b128 a[132:135], v16 offset:40960\n\t"
          "buffer_load_dwordx4 a[104:107], v15, %[ws], s44 offen offset:0 sc1\n\t"
          "v_mfma_f32_16x16x32_bf16 v[232:235], a[148:151], a[56:59], v[232:235]\n\t"
          "v_mfma_f32_16x16x32_bf16 v[236:239], a[148:151], a[60:63], v[236:239]\n\t"
          "v_mfma_f32_16x16x32_bf16 v[236:239], a[144:147], a[44:47], v[236:239]\n\t"
          "buffer_load_dwordx4 %[vx], %[ws], s60 offen offset:3072 sc1 lds\n\t"
          "s_waitcnt lgkmcnt(2)\n\t"
          "v_mfma_f32_16x16x32_bf16 v[252:255], a[152:155], a[44:47], v[252:255]\n\t"
          "ds_read_b128 a[136:139], v16 offset:33792\n\t"
          "v_mfma_f32_16x16x32_bf16 v[252:255], a[156:159], a[60:63], v[252:255]\n\t"
          "v_mfma_f32_16x16x32_bf16 v[248:251], a[156:159], a[56:59], v[248:251]\n\t"
          "v_mfma_f32_16x16x32_bf16 v[248:251], a[152:155], a[40:43], v[248:251]\n\t"
          "buffer_load_dwordx4 a[108:111], v15, %[ws], s44 offen offset:1024 sc1\n\t"
          "v_mfma_f32_16x16x32_bf16 v[244:247], a[152:155], a[36:39], v[244:247]\n\t"
          "ds_read_b128 a[140:143], v16 offset:41984\n\t"
          "v_mfma_f32_16x16x32_bf16 v[244:247], a[156:159], a[52:55], v[244:247]\n\t"
          "v_mfma_f32_16x16x32_bf16 v[240:243], a[156:159], a[48:51], v[240:243]\n\t"
          "s_add_u32 m0, s59, 102400\n\t"
          "v_mfma_f32_16x16x32_bf16 v[240:243], a[152:155], a[32:35], v[240:243]\n\t"
          "buffer_load_dwordx4 %[vx], %[ws], s61 offen offset:0 sc1 lds\n\t"
          "s_waitcnt lgkmcnt(2)\n\t"
          "v_mfma_f32_16x16x32_bf16 v[128:131], a[128:131], a[32:35], v[128:131]\n\t"
          "ds_read_b128 a[144:147], v16 offset:34816\n\t"
          "v_mfma_f32_16x16x32_bf16 v[128:131], a[132:135], a[48:51], v[128:131]\n\t"
          "v_mfma_f32_16x16x32_bf16 v[132:135], a[132:135], a[52:55], v[132:135]\n\t"
          "buffer_load_dwordx4 a[112:115], v14, %[ws], s48 offen offset:0 sc1\n\t"
          "v_mfma_f32_16x16x32_bf16 v[132:135], a[128:131], a[36:39], v[132:135]\n\t"
          "v_mfma_f32_16x16x32_bf16 v[136:139], a[128:131], a[40:43], v[136:139]\n\t"
          "ds_read_b128 a[148:151], v16 offset:43008\n\t"
          "v_mfma_f32_16x16x32_bf16 v[136:139], a[132:135], a[56:59], v[136:139]\n\t"
          "v_mfma_f32_16x16x32_bf16 v[140:143], a[132:135], a[60:63], v[140:143]\n\t"
          "buffer_load_dwordx4 %[vx], %[ws], s61 offen offset:1024 sc1 lds\n\t"
          "v_mfma_f32_16x16x32_bf16 v[140:143], a[128:131], a[44:47], v[140:143]\n\t"
          "s_waitcnt lgkmcnt(2)\n\t"
          "v_mfma_f32_16x16x32_bf16 v[156:159], a[136:139], a[44:47], v[156:159]\n\t"
          "ds_read_b128 a[152:155], v16 offset:35840\n\t"
          "v_mfma_f32_16x16x32_bf16 v[156:159], a[140:143], a[60:63], v[156:159]\n\t"
          "buffer_load_dwordx4 a[116:119], v14, %[ws], s48 offen offset:1024 sc1\n\t"
          "v_mfma_f32_16x16x32_bf16 v[152:155], a[140:143], a[56:59], v[152:155]\n\t"
          "v_mfma_f32_16x16x32_bf16 v[152:155], a[136:139], a[40:43], v[152:155]\n\t"
          "v_mfma_f32_16x16x32_bf16 v[148:151], a[136:139], a[36:39], v[148:151]\n\t"
          "ds_read_b128 a[156:159], v16 offset:44032\n\t"
          "v_mfma_f32_16x16x32_bf16 v[148:151], a[140:143], a[52:55], v[148:151]\n\t"
          "buffer_load_dwordx4 %[vx], %[ws], s61 offen offset:2048 sc1 lds\n\t"
          "v_mfma_f32_16x16x32_bf16 v[144:147], a[140:143], a[48:51], v[144:147]\n\t"
          "v_mfma_f32_16x16x32_bf16 v[144:147], a[136:139], a[32:35], v[144:147]\n\t"
          "s_waitcnt vmcnt(14)\n\t"
          "s_barrier\n\t"
          "s_waitcnt lgkmcnt(2)\n\t"
          "v_mfma_f32_16x16x32_bf16 v[160:163], a[144:147], a[32:35], v[160:163]\n\t"
          "ds_read_b128 a[128:131], v19 offset:0\n\t"
          "v_mfma_f32_16x16x32_bf16 v[160:163], a[148:151], a[48:51], v[160:163]\n\t"
          "buffer_load_dwordx4 a[120:123], v15, %[ws], s48 offen offset:0 sc1\n\t"
          "v_mfma_f32_16x16x32_bf16 v[164:167], a[148:151], a[52:55], v[164:167]\n\t"
          "v_mfma_f32_16x16x32_bf16 v[164:167], a[144:147], a[36:39], v[164:167]\n\t"
          "v_mfma_f32_16x16x32_bf16 v[168:171], a[144:147], a[40:43], v[168:171]\n\t"
          "ds_read_b128 a[132:135], v19 offset:8192\n\t"
          "buffer_load_dwordx4 %[vx], %[ws], s61 offen offset:3072 sc1 lds\n\t"
          "s_add_u32 s60, s60, 16384\n\t"
          "s_add_u32 s61, s61, 16384\n\t"
          "v_mfma_f32_16x16x32_bf16 v[168:171], a[148:151], a[56:59], v[168:171]\n\t"
          "v_mfma_f32_16x16x32_bf16 v[172:175], a[148:151], a[60:63], v[172:175]\n\t"
          "v_mfma_f32_16x16x32_bf16 v[172:175], a[144:147], a[44:47], v[172:175]\n\t"
          "s_waitcnt lgkmcnt(2)\n\t"
          "v_mfma_f32_16x16x32_bf16 v[188:191], a[152:155], a[44:47], v[188:191]\n\t"
          "ds_read_b128 a[136:139], v19 offset:1024\n\t"
          "buffer_load_dwordx4 a[124:127], v15, %[ws], s48 offen offset:1024 sc1\n\t"
          "s_add_u32 s44, s44, 8192\n\t"
          "s_add_u32 s48, s48, 8192\n\t"
          "v_mfma_f32_16x16x32_bf16 v[188:191], a[156:159], a[60:63], v[188:191]\n\t"
          "v_mfma_f32_16x16x32_bf16 v[184:187], a[156:159], a[56:59], v[184:187]\n\t"
          "v_mfma_f32_16x16x32_bf16 v[184:187], a[152:155], a[40:43], v[184:187]\n\t"
          "v_mfma_f32_16x16x32_bf16 v[180:183], a[152:155], a[36:39], v[180:183]\n\t"
          "ds_read_b128 a[140:143], v19 offset:9216\n\t"
          "v_mfma_f32_16x16x32_bf16 v[180:183], a[156:159], a[52:55], v[180:183]\n\t"
          "v_mfma_f32_16x16x32_bf16 v[176:179], a[156:159], a[48:51], v[176:179]\n\t"
          "v_mfma_f32_16x16x32_bf16 v[176:179], a[152:155], a[32:35], v[176:179]\n\t"
          "s_waitcnt vmcnt(16)\n\t"
          "s_waitcnt lgkmcnt(2)\n\t"
          "v_mfma_f32_16x16x32_bf16 v[192:195], a[128:131], a[64:67], v[192:195]\n\t"
          "ds_read_b128 a[144:147], v19 offset:2048\n\t"
          "v_mfma_f32_16x16x32_bf16 v[192:195], a[132:135], a[80:83], v[192:195]\n\t"
          "s_add_u32 m0, s59, 0\n\t"
          "v_mfma_f32_16x16x32_bf16 v[196:199], a[132:135], a[84:87], v[196:199]\n\t"
          "buffer_load_dwordx4 %[vx], %[ws], s60 offen offset:0 sc1 lds\n\t"
          "v_mfma_f32_16x16x32_bf16 v[196:199], a[128:131], a[68:71], v[196:199]\n\t"
          "v_mfma_f32_16x16x32_bf16 v[200:203], a[128:131], a[72:75], v[200:203]\n\t"
          "ds_read_b128 a[148:151], v19 offset:10240\n\t"
          "v_mfma_f32_16x16x32_bf16 v[200:203], a[132:135], a[88:91], v[200:203]\n\t"
          "buffer_load_dwordx4 a[0:3], v14, %[ws], s44 offen offset:0 sc1\n\t"
          "v_mfma_f32_16x16x32_bf16 v[204:207], a[132:135], a[92:95], v[204:207]\n\t"
          "v_mfma_f32_16x16x32_bf16 v[204:207], a[128:131], a[76:79], v[204:207]\n\t"
          "s_waitcnt lgkmcnt(2)\n\t"
          "v_mfma_f32_16x16x32_bf16 v[220:223], a[136:139], a[76:79], v[220:223]\n\t"
          "ds_read_b128 a[152:155], v19 offset:3072\n\t"
          "v_mfma_f32_16x16x32_bf16 v[220:223], a[140:143], a[92:95], v[220:223]\n\t"
          "buffer_load_dwordx4 %[vx], %[ws], s60 offen offset:1024 sc1 lds\n\t"
          "v_mfma_f32_16x16x32_bf16 v[216:219], a[140:143], a[88:91], v[216:219]\n\t"
          "v_mfma_f32_16x16x32_bf16 v[216:219], a[136:139], a[72:75], v[216:219]\n\t"
          "v_mfma_f32_16x16x32_bf16 v[212:215], a[136:139], a[68:71], v[212:215]\n\t"
          "ds_read_b128 a[156:159], v19 offset:11264\n\t"
          "buffer_load_dwordx4 a[4:7], v14, %[ws], s44 offen offset:1024 sc1\n\t"
          "v_mfma_f32_16x16x32_bf16 v[212:215], a[140:143], a[84:87], v[212:215]\n\t"
          "v_mfma_f32_16x16x32_bf16 v[208:211], a[140:143], a[80:83], v[208:211]\n\t"
          "v_mfma_f32_16x16x32_bf16 v[208:211], a[136:139], a[64:67], v[208:211]\n\t"
          "s_waitcnt lgkmcnt(2)\n\t"
          "v_mfma_f32_16x16x32_bf16 v[224:227], a[144:147], a[64:67], v[224:227]\n\t"
          "ds_read_b128 a[128:131], v18 offset:0\n\t"
          "buffer_load_dwordx4 %[vx], %[ws], s60 offen offset:2048 sc1 lds\n\t"
          "v_mfma_f32_16x16x32_bf16 v[224:227], a[148:151], a[80:83], v[224:227]\n\t"
          "v_mfma_f32_16x16x32_bf16 v[228:231], a[148:151], a[84:87], v[228:231]\n\t"
          "v_mfma_f32_16x16x32_bf16 v[228:231], a[144:147], a[68:71], v[228:231]\n\t"
          "v_mfma_f32_16x16x32_bf16 v[232:235], a[144:147], a[72:75], v[232:235]\n\t"
          "ds_read_b128 a[132:135], v18 offset:8192\n\t"
          "buffer_load_dwordx4 a[8:11], v15, %[ws], s44 offen offset:0 sc1\n\t"
          "v_mfma_f32_16x16x32_bf16 v[232:235], a[148:151], a[88:91], v[232:235]\n\t"
          "v_mfma_f32_16x16x32_bf16 v[236:239], a[148:151], a[92:95], v[236:239]\n\t"
          "v_mfma_f32_16x16x32_bf16 v[236:239], a[144:147], a[76:79], v[236:239]\n\t"
          "buffer_load_dwordx4 %[vx], %[ws], s60 offen offset:3072 sc1 lds\n\t"
          "s_waitcnt lgkmcnt(2)\n\t"
          "v_mfma_f32_16x16x32_bf16 v[252:255], a[152:155], a[76:79], v[252:255]\n\t"
          "ds_read_b128 a[136:139], v18 offset:1024\n\t"
          "v_mfma_f32_16x16x32_bf16 v[252:255], a[156:159], a[92:95], v[252:255]\n\t"
          "v_mfma_f32_16x16x32_bf16 v[248:251], a[156:159], a[88:91], v[248:251]\n\t"
          "v_mfma_f32_16x16x32_bf16 v[248:251], a[152:155], a[72:75], v[248:251]\n\t"
          "buffer_load_dwordx4 a[12:15], v15, %[ws], s44 offen offset:1024 sc1\n\t"
          "v_mfma_f32_16x16x32_bf16 v[244:247], a[152:155], a[68:71], v[244:247]\n\t"
          "ds_read_b128 a[140:143], v18 offset:9216\n\t"
          "v_mfma_f32_16x16x32_bf16 v[244:247], a[156:159], a[84:87], v[244:247]\n\t"
          "v_mfma_f32_16x16x32_bf16 v[240:243], a[156:159], a[80:83], v[240:243]\n\t"
          "s_add_u32 m0, s59, 4096\n\t"
          "v_mfma_f32_16x16x32_bf16 v[240:243], a[152:155], a[64:67], v[240:243]\n\t"
          "buffer_load_dwordx4 %[vx], %[ws], s61 offen offset:0 sc1 lds\n\t"
          "s_waitcnt lgkmcnt(2)\n\t"
          "v_mfma_f32_16x16x32_bf16 v[128:131], a[128:131], a[64:67], v[128:131]\n\t"
          "ds_read_b128 a[144:147], v18 offset:2048\n\t"
          "v_mfma_f32_16x16x32_bf16 v[128:131], a[132:135], a[80:83], v[128:131]\n\t"
          "v_mfma_f32_16x16x32_bf16 v[132:135], a[132:135], a[84:87], v[132:135]\n\t"
          "buffer_load_dwordx4 a[16:19], v14, %[ws], s48 offen offset:0 sc1\n\t"
          "v_mfma_f32_16x16x32_bf16 v[132:135], a[128:131], a[68:71], v[132:135]\n\t"
          "v_mfma_f32_16x16x32_bf16 v[136:139], a[128:131], a[72:75], v[136:139]\n\t"
          "ds_read_b128 a[148:151], v18 offset:10240\n\t"
          "v_mfma_f32_16x16x32_bf16 v[136:139], a[132:135], a[88:91], v[136:139]\n\t"
          "v_mfma_f32_16x16x32_bf16 v[140:143], a[132:135], a[92:95], v[140:143]\n\t"
          "buffer_load_dwordx4 %[vx], %[ws], s61 offen offset:1024 sc1 lds\n\t"
          "v_mfma_f32_16x16x32_bf16 v[140:143], a[128:131], a[76:79], v[140:143]\n\t"
          "s_waitcnt lgkmcnt(2)\n\t"
          "v_mfma_f32_16x16x32_bf16 v[156:159], a[136:139], a[76:79], v[156:159]\n\t"
          "ds_read_b128 a[152:155], v18 offset:3072\n\t"
          "v_mfma_f32_16x16x32_bf16 v[156:159], a[140:143], a[92:95], v[156:159]\n\t"
          "buffer_load_dwordx4 a[20:23], v14, %[ws], s48 offen offset:1024 sc1\n\t"
          "v_mfma_f32_16x16x32_bf16 v[152:155], a[140:143], a[88:91], v[152:155]\n\t"
          "v_mfma_f32_16x16x32_bf16 v[152:155], a[136:139], a[72:75], v[152:155]\n\t"
          "v_mfma_f32_16x16x32_bf16 v[148:151], a[136:139], a[68:71], v[148:151]\n\t"
          "ds_read_b128 a[156:159], v18 offset:11264\n\t"
          "v_mfma_f32_16x16x32_bf16 v[148:151], a[140:143], a[84:87], v[148:151]\n\t"
          "buffer_load_dwordx4 %[vx], %[ws], s61 offen offset:2048 sc1 lds\n\t"
          "v_mfma_f32_16x16x32_bf16 v[144:147], a[140:143], a[80:83], v[144:147]\n\t"
          "v_mfma_f32_16x16x32_bf16 v[144:147], a[136:139], a[64:67], v[144:147]\n\t"
          "s_waitcnt vmcnt(14)\n\t"
          "s_barrier\n\t"
          "s_waitcnt lgkmcnt(2)\n\t"
          "v_mfma_f32_16x16x32_bf16 v[160:163], a[144:147], a[64:67], v[160:163]\n\t"
          "ds_read_b128 a[128:131], v19 offset:32768\n\t"
          "v_mfma_f32_16x16x32_bf16 v[160:163], a[148:151], a[80:83], v[160:163]\n\t"
          "buffer_load_dwordx4 a[24:27], v15, %[ws], s48 offen offset:0 sc1\n\t"
          "v_mfma_f32_16x16x32_bf16 v[164:167], a[148:151], a[84:87], v[164:167]\n\t"
          "v_mfma_f32_16x16x32_bf16 v[164:167], a[144:147], a[68:71], v[164:167]\n\t"
          "v_mfma_f32_16x16x32_bf16 v[168:171], a[144:147], a[72:75], v[168:171]\n\t"
          "ds_read_b128 a[132:135], v19 offset:40960\n\t"
          "buffer_load_dwordx4 %[vx], %[ws], s61 offen offset:3072 sc1 lds\n\t"
          "s_add_u32 s60, s60, 16384\n\t"
          "s_add_u32 s61, s61, 16384\n\t"
          "v_mfma_f32_16x16x32_bf16 v[168:171], a[148:151], a[88:91], v[168:171]\n\t"
          "v_mfma_f32_16x16x32_bf16 v[172:175], a[148:151], a[92:95], v[172:175]\n\t"
          "v_mfma_f32_16x16x32_bf16 v[172:175], a[144:147], a[76:79], v[172:175]\n\t"
          "s_waitcnt lgkmcnt(2)\n\t"
          "v_mfma_f32_16x16x32_bf16 v[188:191], a[152:155], a[76:79], v[188:191]\n\t"
          "ds_read_b128 a[136:139], v19 offset:33792\n\t"
          "buffer_load_dwordx4 a[28:31], v15, %[ws], s48 offen offset:1024 sc1\n\t"
          "s_add_u32 s44, s44, 8192\n\t"
          "s_add_u32 s48, s48, 8192\n\t"
          "v_mfma_f32_16x16x32_bf16 v[188:191], a[156:159], a[92:95], v[188:191]\n\t"
          "v_mfma_f32_16x16x32_bf16 v[184:187], a[156:159], a[88:91], v[184:187]\n\t"
          "v_mfma_f32_16x16x32_bf16 v[184:187], a[152:155], a[72:75], v[184:187]\n\t"
          "v_mfma_f32_16x16x32_bf16 v[180:183], a[152:155], a[68:71], v[180:183]\n\t"
          "ds_read_b128 a[140:143], v19 offset:41984\n\t"
          "v_mfma_f32_16x16x32_bf16 v[180:183], a[156:159], a[84:87], v[180:183]\n\t"
          "v_mfma_f32_16x16x32_bf16 v[176:179], a[156:159], a[80:83], v[176:179]\n\t"
          "v_mfma_f32_16x16x32_bf16 v[176:179], a[152:155], a[64:67], v[176:179]\n\t"
          "s_waitcnt vmcnt(16)\n\t"
          "s_waitcnt lgkmcnt(2)\n\t"
          "v_mfma_f32_16x16x32_bf16 v[192:195], a[128:131], a[96:99], v[192:195]\n\t"
          "ds_read_b128 a[144:147], v19 offset:34816\n\t"
          "v_mfma_f32_16x16x32_bf16 v[192:195], a[132:135], a[112:115], v[192:195]\n\t"
          "s_add_u32 m0, s59, 32768\n\t"
          "v_mfma_f32_16x16x32_bf16 v[196:199], a[132:135], a[116:119], v[196:199]\n\t"
          "buffer_load_dwordx4 %[vx], %[ws], s60 offen offset:0 sc1 lds\n\t"
          "v_mfma_f32_16x16x32_bf16 v[196:199], a[128:131], a[100:103], v[196:199]\n\t"
          "v_mfma_f32_16x16x32_bf16 v[200:203], a[128:131], a[104:107], v[200:203]\n\t"
          "ds_read_b128 a[148:151], v19 offset:43008\n\t"
          "v_mfma_f32_16x16x32_bf16 v[200:203], a[132:135], a[120:123], v[200:203]\n\t"
          "buffer_load_dwordx4 a[32:35], v14, %[ws], s44 offen offset:0 sc1\n\t"
          "v_mfma_f32_16x16x32_bf16 v[204:207], a[132:135], a[124:127], v[204:207]\n\t"
          "v_mfma_f32_16x16x32_bf16 v[204:207], a[128:131], a[108:111], v[204:207]\n\t"
          "s_waitcnt lgkmcnt(2)\n\t"
          "v_mfma_f32_16x16x32_bf16 v[220:223], a[136:139], a[108:111], v[220:223]\n\t"
          "ds_read_b128 a[152:155], v19 offset:35840\n\t"
          "v_mfma_f32_16x16x32_bf16 v[220:223], a[140:143], a[124:127], v[220:223]\n\t"
          "buffer_load_dwordx4 %[vx], %[ws], s60 offen offset:1024 sc1 lds\n\t"
          "v_mfma_f32_16x16x32_bf16 v[216:219], a[140:143], a[120:123], v[216:219]\n\t"
          "v_mfma_f32_16x16x32_bf16 v[216:219], a[136:139], a[104:107], v[216:219]\n\t"
          "v_mfma_f32_16x16x32_bf16 v[212:215], a[136:139], a[100:103], v[212:215]\n\t"
          "ds_read_b128 a[156:159], v19 offset:44032\n\t"
          "buffer_load_dwordx4 a[36:39], v14, %[ws], s44 offen offset:1024 sc1\n\t"
          "v_mfma_f32_16x16x32_bf16 v[212:215], a[140:143], a[116:119], v[212:215]\n\t"
          "v_mfma_f32_16x16x32_bf16 v[208:211], a[140:143], a[112:115], v[208:211]\n\t"
          "v_mfma_f32_16x16x32_bf16 v[208:211], a[136:139], a[96:99], v[208:211]\n\t"
          "s_waitcnt lgkmcnt(2)\n\t"
          "v_mfma_f32_16x16x32_bf16 v[224:227], a[144:147], a[96:99], v[224:227]\n\t"
          "ds_read_b128 a[128:131], v18 offset:32768\n\t"
          "buffer_load_dwordx4 %[vx], %[ws], s60 offen offset:2048 sc1 lds\n\t"
          "v_mfma_f32_16x16x32_bf16 v[224:227], a[148:151], a[112:115], v[224:227]\n\t"
          "v_mfma_f32_16x16x32_bf16 v[228:231], a[148:151], a[116:119], v[228:231]\n\t"
          "v_mfma_f32_16x16x32_bf16 v[228:231], a[144:147], a[100:103], v[228:231]\n\t"
          "v_mfma_f32_16x16x32_bf16 v[232:235], a[144:147], a[104:107], v[232:235]\n\t"
          "ds_read_b128 a[132:135], v18 offset:40960\n\t"
          "buffer_load_dwordx4 a[40:43], v15, %[ws], s44 offen offset:0 sc1\n\t"
          "v_mfma_f32_16x16x32_bf16 v[232:235], a[148:151], a[120:123], v[232:235]\n\t"
          "v_mfma_f32_16x16x32_bf16 v[236:239], a[148:151], a[124:127], v[236:239]\n\t"
          "v_mfma_f32_16x16x32_bf16 v[236:239], a[144:147], a[108:111], v[236:239]\n\t"
          "buffer_load_dwordx4 %[vx], %[ws], s60 offen offset:3072 sc1 lds\n\t"
          "s_waitcnt lgkmcnt(2)\n\t"
          "v_mfma_f32_16x16x32_bf16 v[252:255], a[152:155], a[108:111], v[252:255]\n\t"
          "ds_read_b128 a[136:139], v18 offset:33792\n\t"
          "v_mfma_f32_16x16x32_bf16 v[252:255], a[156:159], a[124:127], v[252:255]\n\t"
          "v_mfma_f32_16x16x32_bf16 v[248:251], a[156:159], a[120:123], v[248:251]\n\t"
          "v_mfma_f32_16x16x32_bf16 v[248:251], a[152:155], a[104:107], v[248:251]\n\t"
          "buffer_load_dwordx4 a[44:47], v15, %[ws], s44 offen offset:1024 sc1\n\t"
          "v_mfma_f32_16x16x32_bf16 v[244:247], a[152:155], a[100:103], v[244:247]\n\t"
          "ds_read_b128 a[140:143], v18 offset:41984\n\t"
          "v_mfma_f32_16x16x32_bf16 v[244:247], a[156:159], a[116:119], v[244:247]\n\t"
          "v_mfma_f32_16x16x32_bf16 v[240:243], a[156:159], a[112:115], v[240:243]\n\t"
          "s_add_u32 m0, s59, 36864\n\t"
          "v_mfma_f32_16x16x32_bf16 v[240:243], a[152:155], a[96:99], v[240:243]\n\t"
          "buffer_load_dwordx4 %[vx], %[ws], s61 offen offset:0 sc1 lds\n\t"
          "s_waitcnt lgkmcnt(2)\n\t"
          "v_mfma_f32_16x16x32_bf16 v[128:131], a[128:131], a[96:99], v[128:131]\n\t"
          "ds_read_b128 a[144:147], v18 offset:34816\n\t"
          "v_mfma_f32_16x16x32_bf16 v[128:131], a[132:135], a[112:115], v[128:131]\n\t"
          "v_mfma_f32_16x16x32_bf16 v[132:135], a[132:135], a[116:119], v[132:135]\n\t"
          "buffer_load_dwordx4 a[48:51], v14, %[ws], s48 offen offset:0 sc1\n\t"
          "v_mfma_f32_16x16x32_bf16 v[132:135], a[128:131], a[100:103], v[132:135]\n\t"
          "v_mfma_f32_16x16x32_bf16 v[136:139], a[128:131], a[104:107], v[136:139]\n\t"
          "ds_read_b128 a[148:151], v18 offset:43008\n\t"
          "v_mfma_f32_16x16x32_bf16 v[136:139], a[132:135], a[120:123], v[136:139]\n\t"
          "v_mfma_f32_16x16x32_bf16 v[140:143], a[132:135], a[124:127], v[140:143]\n\t"
          "buffer_load_dwordx4 %[vx], %[ws], s61 offen offset:1024 sc1 lds\n\t"
          "v_mfma_f32_16x16x32_bf16 v[140:143], a[128:131], a[108:111], v[140:143]\n\t"
          "s_waitcnt lgkmcnt(2)\n\t"
          "v_mfma_f32_16x16x32_bf16 v[156:159], a[136:139], a[108:111], v[156:159]\n\t"
          "ds_read_b128 a[152:155], v18 offset:35840\n\t"
          "v_mfma_f32_16x16x32_bf16 v[156:159], a[140:143], a[124:127], v[156:159]\n\t"
          "buffer_load_dwordx4 a[52:55], v14, %[ws], s48 offen offset:1024 sc1\n\t"
          "v_mfma_f32_16x16x32_bf16 v[152:155], a[140:143], a[120:123], v[152:155]\n\t"
          "v_mfma_f32_16x16x32_bf16 v[152:155], a[136:139], a[104:107], v[152:155]\n\t"
          "v_mfma_f32_16x16x32_bf16 v[148:151], a[136:139], a[100:103], v[148:151]\n\t"
          "ds_read_b128 a[156:159], v18 offset:44032\n\t"
          "v_mfma_f32_16x16x32_bf16 v[148:151], a[140:143], a[116:119], v[148:151]\n\t"
          "buffer_load_dwordx4 %[vx], %[ws], s61 offen offset:2048 sc1 lds\n\t"
          "v_mfma_f32_16x16x32_bf16 v[144:147], a[140:143], a[112:115], v[144:147]\n\t"
          "v_mfma_f32_16x16x32_bf16 v[144:147], a[136:139], a[96:99], v[144:147]\n\t"
          "s_waitcnt vmcnt(14)\n\t"
          "s_barrier\n\t"
          "s_waitcnt lgkmcnt(2)\n\t"
          "v_mfma_f32_16x16x32_bf16 v[160:163], a[144:147], a[96:99], v[160:163]\n\t"
          "ds_read_b128 a[128:131], v17 offset:0\n\t"
          "v_mfma_f32_16x16x32_bf16 v[160:163], a[148:151], a[112:115], v[160:163]\n\t"
          "buffer_load_dwordx4 a[56:59], v15, %[ws], s48 offen offset:0 sc1\n\t"
          "v_mfma_f32_16x16x32_bf16 v[164:167], a[148:151], a[116:119], v[164:167]\n\t"
          "v_mfma_f32_16x16x32_bf16 v[164:167], a[144:147], a[100:103], v[164:167]\n\t"
          "v_mfma_f32_16x16x32_bf16 v[168:171], a[144:147], a[104:107], v[168:171]\n\t"
          "ds_read_b128 a[132:135], v17 offset:8192\n\t"
          "buffer_load_dwordx4 %[vx], %[ws], s61 offen offset:3072 sc1 lds\n\t"
          "s_add_u32 s60, s60, 16384\n\t"
          "s_add_u32 s61, s61, 16384\n\t"
          "v_mfma_f32_16x16x32_bf16 v[168:171], a[148:151], a[120:123], v[168:171]\n\t"
          "v_mfma_f32_16x16x32_bf16 v[172:175], a[148:151], a[124:127], v[172:175]\n\t"
          "v_mfma_f32_16x16x32_bf16 v[172:175], a[144:147], a[108:111], v[172:175]\n\t"
          "s_waitcnt lgkmcnt(2)\n\t"
          "v_mfma_f32_16x16x32_bf16 v[188:191], a[152:155], a[108:111], v[188:191]\n\t"
          "ds_read_b128 a[136:139], v17 offset:1024\n\t"
          "buffer_load_dwordx4 a[60:63], v15, %[ws], s48 offen offset:1024 sc1\n\t"
          "s_add_u32 s44, s44, 8192\n\t"
          "s_add_u32 s48, s48, 8192\n\t"
          "v_mfma_f32_16x16x32_bf16 v[188:191], a[156:159], a[124:127], v[188:191]\n\t"
          "v_mfma_f32_16x16x32_bf16 v[184:187], a[156:159], a[120:123], v[184:187]\n\t"
          "v_mfma_f32_16x16x32_bf16 v[184:187], a[152:155], a[104:107], v[184:187]\n\t"
          "v_mfma_f32_16x16x32_bf16 v[180:183], a[152:155], a[100:103], v[180:183]\n\t"
          "ds_read_b128 a[140:143], v17 offset:9216\n\t"
          "v_mfma_f32_16x16x32_bf16 v[180:183], a[156:159], a[116:119], v[180:183]\n\t"
          "v_mfma_f32_16x16x32_bf16 v[176:179], a[156:159], a[112:115], v[176:179]\n\t"
          "v_mfma_f32_16x16x32_bf16 v[176:179], a[152:155], a[96:99], v[176:179]\n\t"
          "s_waitcnt vmcnt(16)\n\t"
          "s_waitcnt lgkmcnt(2)\n\t"
          "v_mfma_f32_16x16x32_bf16 v[192:195], a[128:131], a[0:3], v[192:195]\n\t"
          "ds_read_b128 a[144:147], v17 offset:2048\n\t"
          "v_mfma_f32_16x16x32_bf16 v[192:195], a[132:135], a[16:19], v[192:195]\n\t"
          "s_add_u32 m0, s59, 65536\n\t"
          "v_mfma_f32_16x16x32_bf16 v[196:199], a[132:135], a[20:23], v[196:199]\n\t"
          "buffer_load_dwordx4 %[vx], %[ws], s60 offen offset:0 sc1 lds\n\t"
          "v_mfma_f32_16x16x32_bf16 v[196:199], a[128:131], a[4:7], v[196:199]\n\t"
          "v_mfma_f32_16x16x32_bf16 v[200:203], a[128:131], a[8:11], v[200:203]\n\t"
          "ds_read_b128 a[148:151], v17 offset:10240\n\t"
          "v_mfma_f32_16x16x32_bf16 v[200:203], a[132:135], a[24:27], v[200:203]\n\t"
          "buffer_load_dwordx4 a[64:67], v14, %[ws], s44 offen offset:0 sc1\n\t"
          "v_mfma_f32_16x16x32_bf16 v[204:207], a[132:135], a[28:31], v[204:207]\n\t"
          "v_mfma_f32_16x16x32_bf16 v[204:207], a[128:131], a[12:15], v[204:207]\n\t"
          "s_waitcnt lgkmcnt(2)\n\t"
          "v_mfma_f32_16x16x32_bf16 v[220:223], a[136:139], a[12:15], v[220:223]\n\t"
          "ds_read_b128 a[152:155], v17 offset:3072\n\t"
          "v_mfma_f32_16x16x32_bf16 v[220:223], a[140:143], a[28:31], v[220:223]\n\t"
          "buffer_load_dwordx4 %[vx], %[ws], s60 offen offset:1024 sc1 lds\n\t"
          "v_mfma_f32_16x16x32_bf16 v[216:219], a[140:143], a[24:27], v[216:219]\n\t"
          "v_mfma_f32_16x16x32_bf16 v[216:219], a[136:139], a[8:11], v[216:219]\n\t"
          "v_mfma_f32_16x16x32_bf16 v[212:215], a[136:139], a[4:7], v[212:215]\n\t"
          "ds_read_b128 a[156:159], v17 offset:11264\n\t"
          "buffer_load_dwordx4 a[68:71], v14, %[ws], s44 offen offset:1024 sc1\n\t"
          "v_mfma_f32_16x16x32_bf16 v[212:215], a[140:143], a[20:23], v[212:215]\n\t"
          "v_mfma_f32_16x16x32_bf16 v[208:211], a[140:143], a[16:19], v[208:211]\n\t"
          "v_mfma_f32_16x16x32_bf16 v[208:211], a[136:139], a[0:3], v[208:211]\n\t"
          "s_waitcnt lgkmcnt(2)\n\t"
          "v_mfma_f32_16x16x32_bf16 v[224:227], a[144:147], a[0:3], v[224:227]\n\t"
          "ds_read_b128 a[128:131], v16 offset:0\n\t"
          "buffer_load_dwordx4 %[vx], %[ws], s60 offen offset:2048 sc1 lds\n\t"
          "v_mfma_f32_16x16x32_bf16 v[224:227], a[148:151], a[16:19], v[224:227]\n\t"
          "v_mfma_f32_16x16x32_bf16 v[228:231], a[148:151], a[20:23], v[228:231]\n\t"
          "v_mfma_f32_16x16x32_bf16 v[228:231], a[144:147], a[4:7], v[228:231]\n\t"
          "v_mfma_f32_16x16x32_bf16 v[232:235], a[144:147], a[8:11], v[232:235]\n\t"
          "ds_read_b128 a[132:135], v16 offset:8192\n\t"
          "buffer_load_dwordx4 a[72:75], v15, %[ws], s44 offen offset:0 sc1\n\t"
          "v_mfma_f32_16x16x32_bf16 v[232:235], a[148:151], a[24:27], v[232:235]\n\t"
          "v_mfma_f32_16x16x32_bf16 v[236:239], a[148:151], a[28:31], v[236:239]\n\t"
          "v_mfma_f32_16x16x32_bf16 v[236:239], a[144:147], a[12:15], v[236:239]\n\t"
          "buffer_load_dwordx4 %[vx], %[ws], s60 offen offset:3072 sc1 lds\n\t"
          "s_waitcnt lgkmcnt(2)\n\t"
          "v_mfma_f32_16x16x32_bf16 v[252:255], a[152:155], a[12:15], v[252:255]\n\t"
          "ds_read_b128 a[136:139], v16 offset:1024\n\t"
          "v_mfma_f32_16x16x32_bf16 v[252:255], a[156:159], a[28:31], v[252:255]\n\t"
          "v_mfma_f32_16x16x32_bf16 v[248:251], a[156:159], a[24:27], v[248:251]\n\t"
          "v_mfma_f32_16x16x32_bf16 v[248:251], a[152:155], a[8:11], v[248:251]\n\t"
          "buffer_load_dwordx4 a[76:79], v15, %[ws], s44 offen offset:1024 sc1\n\t"
          "v_mfma_f32_16x16x32_bf16 v[244:247], a[152:155], a[4:7], v[244:247]\n\t"
          "ds_read_b128 a[140:143], v16 offset:9216\n\t"
          "v_mfma_f32_16x16x32_bf16 v[244:247], a[156:159], a[20:23], v[244:247]\n\t"
          "v_mfma_f32_16x16x32_bf16 v[240:243], a[156:159], a[16:19], v[240:243]\n\t"
          "s_add_u32 m0, s59, 69632\n\t"
          "v_mfma_f32_16x16x32_bf16 v[240:243], a[152:155], a[0:3], v[240:243]\n\t"
          "buffer_load_dwordx4 %[vx], %[ws], s61 offen offset:0 sc1 lds\n\t"
          "s_waitcnt lgkmcnt(2)\n\t"
          "v_mfma_f32_16x16x32_bf16 v[128:131], a[128:131], a[0:3], v[128:131]\n\t"
          "ds_read_b128 a[144:147], v16 offset:2048\n\t"
          "v_mfma_f32_16x16x32_bf16 v[128:131], a[132:135], a[16:19], v[128:131]\n\t"
          "v_mfma_f32_16x16x32_bf16 v[132:135], a[132:135], a[20:23], v[132:135]\n\t"
          "buffer_load_dwordx4 a[80:83], v14, %[ws], s48 offen offset:0 sc1\n\t"
          "v_mfma_f32_16x16x32_bf16 v[132:135], a[128:131], a[4:7], v[132:135]\n\t"
          "v_mfma_f32_16x16x32_bf16 v[136:139], a[128:131], a[8:11], v[136:139]\n\t"
          "ds_read_b128 a[148:151], v16 offset:10240\n\t"
          "v_mfma_f32_16x16x32_bf16 v[136:139], a[132:135], a[24:27], v[136:139]\n\t"
          "v_mfma_f32_16x16x32_bf16 v[140:143], a[132:135], a[28:31], v[140:143]\n\t"
          "buffer_load_dwordx4 %[vx], %[ws], s61 offen offset:1024 sc1 lds\n\t"
          "v_mfma_f32_16x16x32_bf16 v[140:143], a[128:131], a[12:15], v[140:143]\n\t"
          "s_waitcnt lgkmcnt(2)\n\t"
          "v_mfma_f32_16x16x32_bf16 v[156:159], a[136:139], a[12:15], v[156:159]\n\t"
          "ds_read_b128 a[152:155], v16 offset:3072\n\t"
          "v_mfma_f32_16x16x32_bf16 v[156:159], a[140:143], a[28:31], v[156:159]\n\t"
          "buffer_load_dwordx4 a[84:87], v14, %[ws], s48 offen offset:1024 sc1\n\t"
          "v_mfma_f32_16x16x32_bf16 v[152:155], a[140:143], a[24:27], v[152:155]\n\t"
          "v_mfma_f32_16x16x32_bf16 v[152:155], a[136:139], a[8:11], v[152:155]\n\t"
          "v_mfma_f32_16x16x32_bf16 v[148:151], a[136:139], a[4:7], v[148:151]\n\t"
          "ds_read_b128 a[156:159], v16 offset:11264\n\t"
          "v_mfma_f32_16x16x32_bf16 v[148:151], a[140:143], a[20:23], v[148:151]\n\t"
          "buffer_load_dwordx4 %[vx], %[ws], s61 offen offset:2048 sc1 lds\n\t"
          "v_mfma_f32_16x16x32_bf16 v[144:147], a[140:143], a[16:19], v[144:147]\n\t"
          "v_mfma_f32_16x16x32_bf16 v[144:147], a[136:139], a[0:3], v[144:147]\n\t"
          "s_waitcnt vmcnt(14)\n\t"
          "s_barrier\n\t"
          "s_waitcnt lgkmcnt(2)\n\t"
          "v_mfma_f32_16x16x32_bf16 v[160:163], a[144:147], a[0:3], v[160:163]\n\t"
          "ds_read_b128 a[128:131], v17 offset:32768\n\t"
          "v_mfma_f32_16x16x32_bf16 v[160:163], a[148:151], a[16:19], v[160:163]\n\t"
          "buffer_load_dwordx4 a[88:91], v15, %[ws], s48 offen offset:0 sc1\n\t"
          "v_mfma_f32_16x16x32_bf16 v[164:167], a[148:151], a[20:23], v[164:167]\n\t"
          "v_mfma_f32_16x16x32_bf16 v[164:167], a[144:147], a[4:7], v[164:167]\n\t"
          "v_mfma_f32_16x16x32_bf16 v[168:171], a[144:147], a[8:11], v[168:171]\n\t"
          "ds_read_b128 a[132:135], v17 offset:40960\n\t"
          "buffer_load_dwordx4 %[vx], %[ws], s61 offen offset:3072 sc1 lds\n\t"
          "s_add_u32 s60, s60, 16384\n\t"
          "s_add_u32 s61, s61, 16384\n\t"
          "v_mfma_f32_16x16x32_bf16 v[168:171], a[148:151], a[24:27], v[168:171]\n\t"
          "v_mfma_f32_16x16x32_bf16 v[172:175], a[148:151], a[28:31], v[172:175]\n\t"
          "v_mfma_f32_16x16x32_bf16 v[172:175], a[144:147], a[12:15], v[172:175]\n\t"
          "s_waitcnt lgkmcnt(2)\n\t"
          "v_mfma_f32_16x16x32_bf16 v[188:191], a[152:155], a[12:15], v[188:191]\n\t"
          "ds_read_b128 a[136:139], v17 offset:33792\n\t"
          "buffer_load_dwordx4 a[92:95], v15, %[ws], s48 offen offset:1024 sc1\n\t"
          "s_add_u32 s44, s44, 8192\n\t"
          "s_add_u32 s48, s48, 8192\n\t"
          "v_mfma_f32_16x16x32_bf16 v[188:191], a[156:159], a[28:31], v[188:191]\n\t"
          "v_mfma_f32_16x16x32_bf16 v[184:187], a[156:159], a[24:27], v[184:187]\n\t"
          "v_mfma_f32_16x16x32_bf16 v[184:187], a[152:155], a[8:11], v[184:187]\n\t"
          "v_mfma_f32_16x16x32_bf16 v[180:183], a[152:155], a[4:7], v[180:183]\n\t"
          "ds_read_b128 a[140:143], v17 offset:41984\n\t"
          "v_mfma_f32_16x16x32_bf16 v[180:183], a[156:159], a[20:23], v[180:183]\n\t"
          "v_mfma_f32_16x16x32_bf16 v[176:179], a[156:159], a[16:19], v[176:179]\n\t"
          "v_mfma_f32_16x16x32_bf16 v[176:179], a[152:155], a[0:3], v[176:179]\n\t"
          "s_waitcnt vmcnt(16)\n\t"
          "s_waitcnt lgkmcnt(2)\n\t"
          "v_mfma_f32_16x16x32_bf16 v[192:195], a[128:131], a[32:35], v[192:195]\n\t"
          "ds_read_b128 a[144:147], v17 offset:34816\n\t"
          "v_mfma_f32_16x16x32_bf16 v[192:195], a[132:135], a[48:51], v[192:195]\n\t"
          "s_add_u32 m0, s59, 98304\n\t"
          "v_mfma_f32_16x16x32_bf16 v[196:199], a[132:135], a[52:55], v[196:199]\n\t"
          "buffer_load_dwordx4 %[vx], %[ws], s60 offen offset:0 sc1 lds\n\t"
          "v_mfma_f32_16x16x32_bf16 v[196:199], a[128:131], a[36:39], v[196:199]\n\t"
          "v_mfma_f32_16x16x32_bf16 v[200:203], a[128:131], a[40:43], v[200:203]\n\t"
          "ds_read_b128 a[148:151], v17 offset:43008\n\t"
          "v_mfma_f32_16x16x32_bf16 v[200:203], a[132:135], a[56:59], v[200:203]\n\t"
          "buffer_load_dwordx4 a[96:99], v14, %[ws], s44 offen offset:0 sc1\n\t"
          "v_mfma_f32_16x16x32_bf16 v[204:207], a[132:135], a[60:63], v[204:207]\n\t"
          "v_mfma_f32_16x16x32_bf16 v[204:207], a[128:131], a[44:47], v[204:207]\n\t"
          "s_waitcnt lgkmcnt(2)\n\t"
          "v_mfma_f32_16x16x32_bf16 v[220:223], a[136:139], a[44:47], v[220:223]\n\t"
          "ds_read_b128 a[152:155], v17 offset:35840\n\t"
          "v_mfma_f32_16x16x32_bf16 v[220:223], a[140:143], a[60:63], v[220:223]\n\t"
          "buffer_load_dwordx4 %[vx], %[ws], s60 offen offset:1024 sc1 lds\n\t"
          "v_mfma_f32_16x16x32_bf16 v[216:219], a[140:143], a[56:59], v[216:219]\n\t"
          "v_mfma_f32_16x16x32_bf16 v[216:219], a[136:139], a[40:43], v[216:219]\n\t"
          "v_mfma_f32_16x16x32_bf16 v[212:215], a[136:139], a[36:39], v[212:215]\n\t"
          "ds_read_b128 a[156:159], v17 offset:44032\n\t"
          "buffer_load_dwordx4 a[100:103], v14, %[ws], s44 offen offset:1024 sc1\n\t"
          "v_mfma_f32_16x16x32_bf16 v[212:215], a[140:143], a[52:55], v[212:215]\n\t"
          "v_mfma_f32_16x16x32_bf16 v[208:211], a[140:143], a[48:51], v[208:211]\n\t"
          "v_mfma_f32_16x16x32_bf16 v[208:211], a[136:139], a[32:35], v[208:211]\n\t"
          "s_waitcnt lgkmcnt(2)\n\t"
          "v_mfma_f32_16x16x32_bf16 v[224:227], a[144:147], a[32:35], v[224:227]\n\t"
          "ds_read_b128 a[128:131], v16 offset:32768\n\t"
          "buffer_load_dwordx4 %[vx], %[ws], s60 offen offset:2048 sc1 lds\n\t"
          "v_mfma_f32_16x16x32_bf16 v[224:227], a[148:151], a[48:51], v[224:227]\n\t"
          "v_mfma_f32_16x16x32_bf16 v[228:231], a[148:151], a[52:55], v[228:231]\n\t"
          "v_mfma_f32_16x16x32_bf16 v[228:231], a[144:147], a[36:39], v[228:231]\n\t"
          "v_mfma_f32_16x16x32_bf16 v[232:235], a[144:147], a[40:43], v[232:235]\n\t"
          "ds_read_b128 a[132:135], v16 offset:40960\n\t"
          "buffer_load_dwordx4 a[104:107], v15, %[ws], s44 offen offset:0 sc1\n\t"
          "v_mfma_f32_16x16x32_bf16 v[232:235], a[148:151], a[56:59], v[232:235]\n\t"
          "v_mfma_f32_16x16x32_bf16 v[236:239], a[148:151], a[60:63], v[236:239]\n\t"
          "v_mfma_f32_16x16x32_bf16 v[236:239], a[144:147], a[44:47], v[236:239]\n\t"
          "buffer_load_dwordx4 %[vx], %[ws], s60 offen offset:3072 sc1 lds\n\t"
          "s_waitcnt lgkmcnt(2)\n\t"
          "v_mfma_f32_16x16x32_bf16 v[252:255], a[152:155], a[44:47], v[252:255]\n\t"
          "ds_read_b128 a[136:139], v16 offset:33792\n\t"
          "v_mfma_f32_16x16x32_bf16 v[252:255], a[156:159], a[60:63], v[252:255]\n\t"
          "v_mfma_f32_16x16x32_bf16 v[248:251], a[156:159], a[56:59], v[248:251]\n\t"
          "v_mfma_f32_16x16x32_bf16 v[248:251], a[152:155], a[40:43], v[248:251]\n\t"
          "buffer_load_dwordx4 a[108:111], v15, %[ws], s44 offen offset:1024 sc1\n\t"
          "v_mfma_f32_16x16x32_bf16 v[244:247], a[152:155], a[36:39], v[244:247]\n\t"
          "ds_read_b128 a[140:143], v16 offset:41984\n\t"
          "v_mfma_f32_16x16x32_bf16 v[244:247], a[156:159], a[52:55], v[244:247]\n\t"
          "v_mfma_f32_16x16x32_bf16 v[240:243], a[156:159], a[48:51], v[240:243]\n\t"
          "s_add_u32 m0, s59, 102400\n\t"
          "v_mfma_f32_16x16x32_bf16 v[240:243], a[152:155], a[32:35], v[240:243]\n\t"
          "buffer_load_dwordx4 %[vx], %[ws], s61 offen offset:0 sc1 lds\n\t"
          "s_waitcnt lgkmcnt(2)\n\t"
          "v_mfma_f32_16x16x32_bf16 v[128:131], a[128:131], a[32:35], v[128:131]\n\t"
          "ds_read_b128 a[144:147], v16 offset:34816\n\t"
          "v_mfma_f32_16x16x32_bf16 v[128:131], a[132:135], a[48:51], v[128:131]\n\t"
          "v_mfma_f32_16x16x32_bf16 v[132:135], a[132:135], a[52:55], v[132:135]\n\t"
          "buffer_load_dwordx4 a[112:115], v14, %[ws], s48 offen offset:0 sc1\n\t"
          "v_mfma_f32_16x16x32_bf16 v[132:135], a[128:131], a[36:39], v[132:135]\n\t"
          "v_mfma_f32_16x16x32_bf16 v[136:139], a[128:131], a[40:43], v[136:139]\n\t"
          "ds_read_b128 a[148:151], v16 offset:43008\n\t"
          "v_mfma_f32_16x16x32_bf16 v[136:139], a[132:135], a[56:59], v[136:139]\n\t"
          "v_mfma_f32_16x16x32_bf16 v[140:143], a[132:135], a[60:63], v[140:143]\n\t"
          "buffer_load_dwordx4 %[vx], %[ws], s61 offen offset:1024 sc1 lds\n\t"
          "v_mfma_f32_16x16x32_bf16 v[140:143], a[128:131], a[44:47], v[140:143]\n\t"
          "s_waitcnt lgkmcnt(2)\n\t"
          "v_mfma_f32_16x16x32_bf16 v[156:159], a[136:139], a[44:47], v[156:159]\n\t"
          "ds_read_b128 a[152:155], v16 offset:35840\n\t"
          "v_mfma_f32_16x16x32_bf16 v[156:159], a[140:143], a[60:63], v[156:159]\n\t"
          "buffer_load_dwordx4 a[116:119], v14, %[ws], s48 offen offset:1024 sc1\n\t"
          "v_mfma_f32_16x16x32_bf16 v[152:155], a[140:143], a[56:59], v[152:155]\n\t"
          "v_mfma_f32_16x16x32_bf16 v[152:155], a[136:139], a[40:43], v[152:155]\n\t"
          "v_mfma_f32_16x16x32_bf16 v[148:151], a[136:139], a[36:39], v[148:151]\n\t"
          "ds_read_b128 a[156:159], v16 offset:44032\n\t"
          "v_mfma_f32_16x16x32_bf16 v[148:151], a[140:143], a[52:55], v[148:151]\n\t"
          "buffer_load_dwordx4 %[vx], %[ws], s61 offen offset:2048 sc1 lds\n\t"
          "v_mfma_f32_16x16x32_bf16 v[144:147], a[140:143], a[48:51], v[144:147]\n\t"
          "v_mfma_f32_16x16x32_bf16 v[144:147], a[136:139], a[32:35], v[144:147]\n\t"
          "s_waitcnt vmcnt(14)\n\t"
          "s_barrier\n\t"
          "s_waitcnt lgkmcnt(2)\n\t"
          "v_mfma_f32_16x16x32_bf16 v[160:163], a[144:147], a[32:35], v[160:163]\n\t"
          "ds_read_b128 a[128:131], v19 offset:0\n\t"
          "v_mfma_f32_16x16x32_bf16 v[160:163], a[148:151], a[48:51], v[160:163]\n\t"
          "buffer_load_dwordx4 a[120:123], v15, %[ws], s48 offen offset:0 sc1\n\t"
          "v_mfma_f32_16x16x32_bf16 v[164:167], a[148:151], a[52:55], v[164:167]\n\t"
          "v_mfma_f32_16x16x32_bf16 v[164:167], a[144:147], a[36:39], v[164:167]\n\t"
          "v_mfma_f32_16x16x32_bf16 v[168:171], a[144:147], a[40:43], v[168:171]\n\t"
          "ds_read_b128 a[132:135], v19 offset:8192\n\t"
          "buffer_load_dwordx4 %[vx], %[ws], s61 offen offset:3072 sc1 lds\n\t"
          "s_add_u32 s60, s60, 16384\n\t"
          "s_add_u32 s61, s61, 16384\n\t"
          "v_mfma_f32_16x16x32_bf16 v[168:171], a[148:151], a[56:59], v[168:171]\n\t"
          "v_mfma_f32_16x16x32_bf16 v[172:175], a[148:151], a[60:63], v[172:175]\n\t"
          "v_mfma_f32_16x16x32_bf16 v[172:175], a[144:147], a[44:47], v[172:175]\n\t"
          "s_waitcnt lgkmcnt(2)\n\t"
          "v_mfma_f32_16x16x32_bf16 v[188:191], a[152:155], a[44:47], v[188:191]\n\t"
          "ds_read_b128 a[136:139], v19 offset:1024\n\t"
          "buffer_load_dwordx4 a[124:127], v15, %[ws], s48 offen offset:1024 sc1\n\t"
          "s_add_u32 s44, s44, 8192\n\t"
          "s_add_u32 s48, s48, 8192\n\t"
          "v_mfma_f32_16x16x32_bf16 v[188:191], a[156:159], a[60:63], v[188:191]\n\t"
          "v_mfma_f32_16x16x32_bf16 v[184:187], a[156:159], a[56:59], v[184:187]\n\t"
          "v_mfma_f32_16x16x32_bf16 v[184:187], a[152:155], a[40:43], v[184:187]\n\t"
          "v_mfma_f32_16x16x32_bf16 v[180:183], a[152:155], a[36:39], v[180:183]\n\t"
          "ds_read_b128 a[140:143], v19 offset:9216\n\t"
          "v_mfma_f32_16x16x32_bf16 v[180:183], a[156:159], a[52:55], v[180:183]\n\t"
          "v_mfma_f32_16x16x32_bf16 v[176:179], a[156:159], a[48:51], v[176:179]\n\t"
          "v_mfma_f32_16x16x32_bf16 v[176:179], a[152:155], a[32:35], v[176:179]\n\t"
          "s_waitcnt vmcnt(16)\n\t"
          "s_waitcnt lgkmcnt(2)\n\t"
          "v_mfma_f32_16x16x32_bf16 v[192:195], a[128:131], a[64:67], v[192:195]\n\t"
          "ds_read_b128 a[144:147], v19 offset:2048\n\t"
          "v_mfma_f32_16x16x32_bf16 v[192:195], a[132:135], a[80:83], v[192:195]\n\t"
          "s_add_u32 m0, s59, 0\n\t"
          "v_mfma_f32_16x16x32_bf16 v[196:199], a[132:135], a[84:87], v[196:199]\n\t"
          "buffer_load_dwordx4 %[vx], %[ws], s60 offen offset:0 sc1 lds\n\t"
          "v_mfma_f32_16x16x32_bf16 v[196:199], a[128:131], a[68:71], v[196:199]\n\t"
          "v_mfma_f32_16x16x32_bf16 v[200:203], a[128:131], a[72:75], v[200:203]\n\t"
          "ds_read_b128 a[148:151], v19 offset:10240\n\t"
          "v_mfma_f32_16x16x32_bf16 v[200:203], a[132:135], a[88:91], v[200:203]\n\t"
          "buffer_load_dwordx4 a[0:3], v14, %[ws], s44 offen offset:0 sc1\n\t"
          "v_mfma_f32_16x16x32_bf16 v[204:207], a[132:135], a[92:95], v[204:207]\n\t"
          "v_mfma_f32_16x16x32_bf16 v[204:207], a[128:131], a[76:79], v[204:207]\n\t"
          "s_waitcnt lgkmcnt(2)\n\t"
          "v_mfma_f32_16x16x32_bf16 v[220:223], a[136:139], a[76:79], v[220:223]\n\t"
          "ds_read_b128 a[152:155], v19 offset:3072\n\t"
          "v_mfma_f32_16x16x32_bf16 v[220:223], a[140:143], a[92:95], v[220:223]\n\t"
          "buffer_load_dwordx4 %[vx], %[ws], s60 offen offset:1024 sc1 lds\n\t"
          "v_mfma_f32_16x16x32_bf16 v[216:219], a[140:143], a[88:91], v[216:219]\n\t"
          "v_mfma_f32_16x16x32_bf16 v[216:219], a[136:139], a[72:75], v[216:219]\n\t"
          "v_mfma_f32_16x16x32_bf16 v[212:215], a[136:139], a[68:71], v[212:215]\n\t"
          "ds_read_b128 a[156:159], v19 offset:11264\n\t"
          "buffer_load_dwordx4 a[4:7], v14, %[ws], s44 offen offset:1024 sc1\n\t"
          "v_mfma_f32_16x16x32_bf16 v[212:215], a[140:143], a[84:87], v[212:215]\n\t"
          "v_mfma_f32_16x16x32_bf16 v[208:211], a[140:143], a[80:83], v[208:211]\n\t"
          "v_mfma_f32_16x16x32_bf16 v[208:211], a[136:139], a[64:67], v[208:211]\n\t"
          "s_waitcnt lgkmcnt(2)\n\t"
          "v_mfma_f32_16x16x32_bf16 v[224:227], a[144:147], a[64:67], v[224:227]\n\t"
          "ds_read_b128 a[128:131], v18 offset:0\n\t"
          "buffer_load_dwordx4 %[vx], %[ws], s60 offen offset:2048 sc1 lds\n\t"
          "v_mfma_f32_16x16x32_bf16 v[224:227], a[148:151], a[80:83], v[224:227]\n\t"
          "v_mfma_f32_16x16x32_bf16 v[228:231], a[148:151], a[84:87], v[228:231]\n\t"
          "v_mfma_f32_16x16x32_bf16 v[228:231], a[144:147], a[68:71], v[228:231]\n\t"
          "v_mfma_f32_16x16x32_bf16 v[232:235], a[144:147], a[72:75], v[232:235]\n\t"
          "ds_read_b128 a[132:135], v18 offset:8192\n\t"
          "buffer_load_dwordx4 a[8:11], v15, %[ws], s44 offen offset:0 sc1\n\t"
          "v_mfma_f32_16x16x32_bf16 v[232:235], a[148:151], a[88:91], v[232:235]\n\t"
          "v_mfma_f32_16x16x32_bf16 v[236:239], a[148:151], a[92:95], v[236:239]\n\t"
          "v_mfma_f32_16x16x32_bf16 v[236:239], a[144:147], a[76:79], v[236:239]\n\t"
          "buffer_load_dwordx4 %[vx], %[ws], s60 offen offset:3072 sc1 lds\n\t"
          "s_waitcnt lgkmcnt(2)\n\t"
          "v_mfma_f32_16x16x32_bf16 v[252:255], a[152:155], a[76:79], v[252:255]\n\t"
          "ds_read_b128 a[136:139], v18 offset:1024\n\t"
          "v_mfma_f32_16x16x32_bf16 v[252:255], a[156:159], a[92:95], v[252:255]\n\t"
          "v_mfma_f32_16x16x32_bf16 v[248:251], a[156:159], a[88:91], v[248:251]\n\t"
          "v_mfma_f32_16x16x32_bf16 v[248:251], a[152:155], a[72:75], v[248:251]\n\t"
          "buffer_load_dwordx4 a[12:15], v15, %[ws], s44 offen offset:1024 sc1\n\t"
          "v_mfma_f32_16x16x32_bf16 v[244:247], a[152:155], a[68:71], v[244:247]\n\t"
          "ds_read_b128 a[140:143], v18 offset:9216\n\t"
          "v_mfma_f32_16x16x32_bf16 v[244:247], a[156:159], a[84:87], v[244:247]\n\t"
          "v_mfma_f32_16x16x32_bf16 v[240:243], a[156:159], a[80:83], v[240:243]\n\t"
          "s_add_u32 m0, s59, 4096\n\t"
          "v_mfma_f32_16x16x32_bf16 v[240:243], a[152:155], a[64:67], v[240:243]\n\t"
          "buffer_load_dwordx4 %[vx], %[ws], s61 offen offset:0 sc1 lds\n\t"
          "s_waitcnt lgkmcnt(2)\n\t"
          "v_mfma_f32_16x16x32_bf16 v[128:131], a[128:131], a[64:67], v[128:131]\n\t"
          "ds_read_b128 a[144:147], v18 offset:2048\n\t"
          "v_mfma_f32_16x16x32_bf16 v[128:131], a[132:135], a[80:83], v[128:131]\n\t"
          "v_mfma_f32_16x16x32_bf16 v[132:135], a[132:135], a[84:87], v[132:135]\n\t"
          "buffer_load_dwordx4 a[16:19], v14, %[ws], s48 offen offset:0 sc1\n\t"
          "v_mfma_f32_16x16x32_bf16 v[132:135], a[128:131], a[68:71], v[132:135]\n\t"
          "v_mfma_f32_16x16x32_bf16 v[136:139], a[128:131], a[72:75], v[136:139]\n\t"
          "ds_read_b128 a[148:151], v18 offset:10240\n\t"
          "v_mfma_f32_16x16x32_bf16 v[136:139], a[132:135], a[88:91], v[136:139]\n\t"
          "v_mfma_f32_16x16x32_bf16 v[140:143], a[132:135], a[92:95], v[140:143]\n\t"
          "buffer_load_dwordx4 %[vx], %[ws], s61 offen offset:1024 sc1 lds\n\t"
          "v_mfma_f32_16x16x32_bf16 v[140:143], a[128:131], a[76:79], v[140:143]\n\t"
          "s_waitcnt lgkmcnt(2)\n\t"
          "v_mfma_f32_16x16x32_bf16 v[156:159], a[136:139], a[76:79], v[156:159]\n\t"
          "ds_read_b128 a[152:155], v18 offset:3072\n\t"
          "v_mfma_f32_16x16x32_bf16 v[156:159], a[140:143], a[92:95], v[156:159]\n\t"
          "buffer_load_dwordx4 a[20:23], v14, %[ws], s48 offen offset:1024 sc1\n\t"
          "v_mfma_f32_16x16x32_bf16 v[152:155], a[140:143], a[88:91], v[152:155]\n\t"
          "v_mfma_f32_16x16x32_bf16 v[152:155], a[136:139], a[72:75], v[152:155]\n\t"
          "v_mfma_f32_16x16x32_bf16 v[148:151], a[136:139], a[68:71], v[148:151]\n\t"
          "ds_read_b128 a[156:159], v18 offset:11264\n\t"
          "v_mfma_f32_16x16x32_bf16 v[148:151], a[140:143], a[84:87], v[148:151]\n\t"
          "buffer_load_dwordx4 %[vx], %[ws], s61 offen offset:2048 sc1 lds\n\t"
          "v_mfma_f32_16x16x32_bf16 v[144:147], a[140:143], a[80:83], v[144:147]\n\t"
          "v_mfma_f32_16x16x32_bf16 v[144:147], a[136:139], a[64:67], v[144:147]\n\t"
          "s_waitcnt vmcnt(14)\n\t"
          "s_barrier\n\t"
          "s_waitcnt lgkmcnt(2)\n\t"
          "v_mfma_f32_16x16x32_bf16 v[160:163], a[144:147], a[64:67], v[160:163]\n\t"
          "ds_read_b128 a[128:131], v19 offset:32768\n\t"
          "v_mfma_f32_16x16x32_bf16 v[160:163], a[148:151], a[80:83], v[160:163]\n\t"
          "buffer_load_dwordx4 a[24:27], v15, %[ws], s48 offen offset:0 sc1\n\t"
          "v_mfma_f32_16x16x32_bf16 v[164:167], a[148:151], a[84:87], v[164:167]\n\t"
          "v_mfma_f32_16x16x32_bf16 v[164:167], a[144:147], a[68:71], v[164:167]\n\t"
          "v_mfma_f32_16x16x32_bf16 v[168:171], a[144:147], a[72:75], v[168:171]\n\t"
          "ds_read_b128 a[132:135], v19 offset:40960\n\t"
          "buffer_load_dwordx4 %[vx], %[ws], s61 offen offset:3072 sc1 lds\n\t"
          "s_add_u32 s60, s60, 16384\n\t"
          "s_add_u32 s61, s61, 16384\n\t"
          "v_mfma_f32_16x16x32_bf16 v[168:171], a[148:151], a[88:91], v[168:171]\n\t"
          "v_mfma_f32_16x16x32_bf16 v[172:175], a[148:151], a[92:95], v[172:175]\n\t"
          "v_mfma_f32_16x16x32_bf16 v[172:175], a[144:147], a[76:79], v[172:175]\n\t"
          "s_waitcnt lgkmcnt(2)\n\t"
          "v_mfma_f32_16x16x32_bf16 v[188:191], a[152:155], a[76:79], v[188:191]\n\t"
          "ds_read_b128 a[136:139], v19 offset:33792\n\t"
          "buffer_load_dwordx4 a[28:31], v15, %[ws], s48 offen offset:1024 sc1\n\t"
          "s_add_u32 s44, s44, 8192\n\t"
          "s_add_u32 s48, s48, 8192\n\t"
          "v_mfma_f32_16x16x32_bf16 v[188:191], a[156:159], a[92:95], v[188:191]\n\t"
          "v_mfma_f32_16x16x32_bf16 v[184:187], a[156:159], a[88:91], v[184:187]\n\t"
          "v_mfma_f32_16x16x32_bf16 v[184:187], a[152:155], a[72:75], v[184:187]\n\t"
          "v_mfma_f32_16x16x32_bf16 v[180:183], a[152:155], a[68:71], v[180:183]\n\t"
          "ds_read_b128 a[140:143], v19 offset:41984\n\t"
          "v_mfma_f32_16x16x32_bf16 v[180:183], a[156:159], a[84:87], v[180:183]\n\t"
          "v_mfma_f32_16x16x32_bf16 v[176:179], a[156:159], a[80:83], v[176:179]\n\t"
          "v_mfma_f32_16x16x32_bf16 v[176:179], a[152:155], a[64:67], v[176:179]\n\t"
          "s_waitcnt vmcnt(16)\n\t"
          "s_waitcnt lgkmcnt(2)\n\t"
          "v_mfma_f32_16x16x32_bf16 v[192:195], a[128:131], a[96:99], v[192:195]\n\t"
          "ds_read_b128 a[144:147], v19 offset:34816\n\t"
          "v_mfma_f32_16x16x32_bf16 v[192:195], a[132:135], a[112:115], v[192:195]\n\t"
          "s_add_u32 m0, s59, 32768\n\t"
          "v_mfma_f32_16x16x32_bf16 v[196:199], a[132:135], a[116:119], v[196:199]\n\t"
          "buffer_load_dwordx4 %[vx], %[ws], s60 offen offset:0 sc1 lds\n\t"
          "v_mfma_f32_16x16x32_bf16 v[196:199], a[128:131], a[100:103], v[196:199]\n\t"
          "v_mfma_f32_16x16x32_bf16 v[200:203], a[128:131], a[104:107], v[200:203]\n\t"
          "ds_read_b128 a[148:151], v19 offset:43008\n\t"
          "v_mfma_f32_16x16x32_bf16 v[200:203], a[132:135], a[120:123], v[200:203]\n\t"
          "buffer_load_dwordx4 a[32:35], v14, %[ws], s44 offen offset:0 sc1\n\t"
          "v_mfma_f32_16x16x32_bf16 v[204:207], a[132:135], a[124:127], v[204:207]\n\t"
          "v_mfma_f32_16x16x32_bf16 v[204:207], a[128:131], a[108:111], v[204:207]\n\t"
          "s_waitcnt lgkmcnt(2)\n\t"
          "v_mfma_f32_16x16x32_bf16 v[220:223], a[136:139], a[108:111], v[220:223]\n\t"
          "ds_read_b128 a[152:155], v19 offset:35840\n\t"
          "v_mfma_f32_16x16x32_bf16 v[220:223], a[140:143], a[124:127], v[220:223]\n\t"
          "buffer_load_dwordx4 %[vx], %[ws], s60 offen offset:1024 sc1 lds\n\t"
          "v_mfma_f32_16x16x32_bf16 v[216:219], a[140:143], a[120:123], v[216:219]\n\t"
          "v_mfma_f32_16x16x32_bf16 v[216:219], a[136:139], a[104:107], v[216:219]\n\t"
          "v_mfma_f32_16x16x32_bf16 v[212:215], a[136:139], a[100:103], v[212:215]\n\t"
          "ds_read_b128 a[156:159], v19 offset:44032\n\t"
          "buffer_load_dwordx4 a[36:39], v14, %[ws], s44 offen offset:1024 sc1\n\t"
          "v_mfma_f32_16x16x32_bf16 v[212:215], a[140:143], a[116:119], v[212:215]\n\t"
          "v_mfma_f32_16x16x32_bf16 v[208:211], a[140:143], a[112:115], v[208:211]\n\t"
          "v_mfma_f32_16x16x32_bf16 v[208:211], a[136:139], a[96:99], v[208:211]\n\t"
          "s_waitcnt lgkmcnt(2)\n\t"
          "v_mfma_f32_16x16x32_bf16 v[224:227], a[144:147], a[96:99], v[224:227]\n\t"
          "ds_read_b128 a[128:131], v18 offset:32768\n\t"
          "buffer_load_dwordx4 %[vx], %[ws], s60 offen offset:2048 sc1 lds\n\t"
          "v_mfma_f32_16x16x32_bf16 v[224:227], a[148:151], a[112:115], v[224:227]\n\t"
          "v_mfma_f32_16x16x32_bf16 v[228:231], a[148:151], a[116:119], v[228:231]\n\t"
          "v_mfma_f32_16x16x32_bf16 v[228:231], a[144:147], a[100:103], v[228:231]\n\t"
          "v_mfma_f32_16x16x32_bf16 v[232:235], a[144:147], a[104:107], v[232:235]\n\t"
          "ds_read_b128 a[132:135], v18 offset:40960\n\t"
          "buffer_load_dwordx4 a[40:43], v15, %[ws], s44 offen offset:0 sc1\n\t"
          "v_mfma_f32_16x16x32_bf16 v[232:235], a[148:151], a[120:123], v[232:235]\n\t"
          "v_mfma_f32_16x16x32_bf16 v[236:239], a[148:151], a[124:127], v[236:239]\n\t"
          "v_mfma_f32_16x16x32_bf16 v[236:239], a[144:147], a[108:111], v[236:239]\n\t"
          "buffer_load_dwordx4 %[vx], %[ws], s60 offen offset:3072 sc1 lds\n\t"
          "s_waitcnt lgkmcnt(2)\n\t"
          "v_mfma_f32_16x16x32_bf16 v[252:255], a[152:155], a[108:111], v[252:255]\n\t"
          "ds_read_b128 a[136:139], v18 offset:33792\n\t"
          "v_mfma_f32_16x16x32_bf16 v[252:255], a[156:159], a[124:127], v[252:255]\n\t"
          "v_mfma_f32_16x16x32_bf16 v[248:251], a[156:159], a[120:123], v[248:251]\n\t"
          "v_mfma_f32_16x16x32_bf16 v[248:251], a[152:155], a[104:107], v[248:251]\n\t"
          "buffer_load_dwordx4 a[44:47], v15, %[ws], s44 offen offset:1024 sc1\n\t"
          "v_mfma_f32_16x16x32_bf16 v[244:247], a[152:155], a[100:103], v[244:247]\n\t"
          "ds_read_b128 a[140:143], v18 offset:41984\n\t"
          "v_mfma_f32_16x16x32_bf16 v[244:247], a[156:159], a[116:119], v[244:247]\n\t"
          "v_mfma_f32_16x16x32_bf16 v[240:243], a[156:159], a[112:115], v[240:243]\n\t"
          "s_add_u32 m0, s59, 36864\n\t"
          "v_mfma_f32_16x16x32_bf16 v[240:243], a[152:155], a[96:99], v[240:243]\n\t"
          "buffer_load_dwordx4 %[vx], %[ws], s61 offen offset:0 sc1 lds\n\t"
          "s_waitcnt lgkmcnt(2)\n\t"
          "v_mfma_f32_16x16x32_bf16 v[128:131], a[128:131], a[96:99], v[128:131]\n\t"
          "ds_read_b128 a[144:147], v18 offset:34816\n\t"
          "v_mfma_f32_16x16x32_bf16 v[128:131], a[132:135], a[112:115], v[128:131]\n\t"
          "v_mfma_f32_16x16x32_bf16 v[132:135], a[132:135], a[116:119], v[132:135]\n\t"
          "buffer_load_dwordx4 a[48:51], v14, %[ws], s48 offen offset:0 sc1\n\t"
          "v_mfma_f32_16x16x32_bf16 v[132:135], a[128:131], a[100:103], v[132:135]\n\t"
          "v_mfma_f32_16x16x32_bf16 v[136:139], a[128:131], a[104:107], v[136:139]\n\t"
          "ds_read_b128 a[148:151], v18 offset:43008\n\t"
          "v_mfma_f32_16x16x32_bf16 v[136:139], a[132:135], a[120:123], v[136:139]\n\t"
          "v_mfma_f32_16x16x32_bf16 v[140:143], a[132:135], a[124:127], v[140:143]\n\t"
          "buffer_load_dwordx4 %[vx], %[ws], s61 offen offset:1024 sc1 lds\n\t"
          "v_mfma_f32_16x16x32_bf16 v[140:143], a[128:131], a[108:111], v[140:143]\n\t"
          "s_waitcnt lgkmcnt(2)\n\t"
          "v_mfma_f32_16x16x32_bf16 v[156:159], a[136:139], a[108:111], v[156:159]\n\t"
          "ds_read_b128 a[152:155], v18 offset:35840\n\t"
          "v_mfma_f32_16x16x32_bf16 v[156:159], a[140:143], a[124:127], v[156:159]\n\t"
          "buffer_load_dwordx4 a[52:55], v14, %[ws], s48 offen offset:1024 sc1\n\t"
          "v_mfma_f32_16x16x32_bf16 v[152:155], a[140:143], a[120:123], v[152:155]\n\t"
          "v_mfma_f32_16x16x32_bf16 v[152:155], a[136:139], a[104:107], v[152:155]\n\t"
          "v_mfma_f32_16x16x32_bf16 v[148:151], a[136:139], a[100:103], v[148:151]\n\t"
          "ds_read_b128 a[156:159], v18 offset:44032\n\t"
          "v_mfma_f32_16x16x32_bf16 v[148:151], a[140:143], a[116:119], v[148:151]\n\t"
          "buffer_load_dwordx4 %[vx], %[ws], s61 offen offset:2048 sc1 lds\n\t"
          "v_mfma_f32_16x16x32_bf16 v[144:147], a[140:143], a[112:115], v[144:147]\n\t"
          "v_mfma_f32_16x16x32_bf16 v[144:147], a[136:139], a[96:99], v[144:147]\n\t"
          "s_waitcnt vmcnt(14)\n\t"
          "s_barrier\n\t"
          "s_waitcnt lgkmcnt(2)\n\t"
          "v_mfma_f32_16x16x32_bf16 v[160:163], a[144:147], a[96:99], v[160:163]\n\t"
          "ds_read_b128 a[128:131], v17 offset:0\n\t"
          "v_mfma_f32_16x16x32_bf16 v[160:163], a[148:151], a[112:115], v[160:163]\n\t"
          "buffer_load_dwordx4 a[56:59], v15, %[ws], s48 offen offset:0 sc1\n\t"
          "v_mfma_f32_16x16x32_bf16 v[164:167], a[148:151], a[116:119], v[164:167]\n\t"
          "v_mfma_f32_16x16x32_bf16 v[164:167], a[144:147], a[100:103], v[164:167]\n\t"
          "v_mfma_f32_16x16x32_bf16 v[168:171], a[144:147], a[104:107], v[168:171]\n\t"
          "ds_read_b128 a[132:135], v17 offset:8192\n\t"
          "buffer_load_dwordx4 %[vx], %[ws], s61 offen offset:3072 sc1 lds\n\t"
          "s_add_u32 s60, s60, 16384\n\t"
          "s_add_u32 s61, s61, 16384\n\t"
          "v_mfma_f32_16x16x32_bf16 v[168:171], a[148:151], a[120:123], v[168:171]\n\t"
          "v_mfma_f32_16x16x32_bf16 v[172:175], a[148:151], a[124:127], v[172:175]\n\t"
          "v_mfma_f32_16x16x32_bf16 v[172:175], a[144:147], a[108:111], v[172:175]\n\t"
          "s_waitcnt lgkmcnt(2)\n\t"
          "v_mfma_f32_16x16x32_bf16 v[188:191], a[152:155], a[108:111], v[188:191]\n\t"
          "ds_read_b128 a[136:139], v17 offset:1024\n\t"
          "buffer_load_dwordx4 a[60:63], v15, %[ws], s48 offen offset:1024 sc1\n\t"
          "s_add_u32 s44, s44, 8192\n\t"
          "s_add_u32 s48, s48, 8192\n\t"
          "v_mfma_f32_16x16x32_bf16 v[188:191], a[156:159], a[124:127], v[188:191]\n\t"
          "v_mfma_f32_16x16x32_bf16 v[184:187], a[156:159], a[120:123], v[184:187]\n\t"
          "v_mfma_f32_16x16x32_bf16 v[184:187], a[152:155], a[104:107], v[184:187]\n\t"
          "v_mfma_f32_16x16x32_bf16 v[180:183], a[152:155], a[100:103], v[180:183]\n\t"
          "ds_read_b128 a[140:143], v17 offset:9216\n\t"
          "v_mfma_f32_16x16x32_bf16 v[180:183], a[156:159], a[116:119], v[180:183]\n\t"
          "v_mfma_f32_16x16x32_bf16 v[176:179], a[156:159], a[112:115], v[176:179]\n\t"
          "v_mfma_f32_16x16x32_bf16 v[176:179], a[152:155], a[96:99], v[176:179]\n\t"
          "s_waitcnt vmcnt(16)\n\t"
          "s_waitcnt lgkmcnt(2)\n\t"
          "v_mfma_f32_16x16x32_bf16 v[192:195], a[128:131], a[0:3], v[192:195]\n\t"
          "ds_read_b128 a[144:147], v17 offset:2048\n\t"
          "v_mfma_f32_16x16x32_bf16 v[192:195], a[132:135], a[16:19], v[192:195]\n\t"
          "s_add_u32 m0, s59, 65536\n\t"
          "v_mfma_f32_16x16x32_bf16 v[196:199], a[132:135], a[20:23], v[196:199]\n\t"
          "buffer_load_dwordx4 %[vx], %[ws], s60 offen offset:0 sc1 lds\n\t"
          "v_mfma_f32_16x16x32_bf16 v[196:199], a[128:131], a[4:7], v[196:199]\n\t"
          "v_mfma_f32_16x16x32_bf16 v[200:203], a[128:131], a[8:11], v[200:203]\n\t"
          "ds_read_b128 a[148:151], v17 offset:10240\n\t"
          "v_mfma_f32_16x16x32_bf16 v[200:203], a[132:135], a[24:27], v[200:203]\n\t"
          "buffer_load_dwordx4 a[64:67], v14, %[ws], s44 offen offset:0 sc1\n\t"
          "v_mfma_f32_16x16x32_bf16 v[204:207], a[132:135], a[28:31], v[204:207]\n\t"
          "v_mfma_f32_16x16x32_bf16 v[204:207], a[128:131], a[12:15], v[204:207]\n\t"
          "s_waitcnt lgkmcnt(2)\n\t"
          "v_mfma_f32_16x16x32_bf16 v[220:223], a[136:139], a[12:15], v[220:223]\n\t"
          "ds_read_b128 a[152:155], v17 offset:3072\n\t"
          "v_mfma_f32_16x16x32_bf16 v[220:223], a[140:143], a[28:31], v[220:223]\n\t"
          "buffer_load_dwordx4 %[vx], %[ws], s60 offen offset:1024 sc1 lds\n\t"
          "v_mfma_f32_16x16x32_bf16 v[216:219], a[140:143], a[24:27], v[216:219]\n\t"
          "v_mfma_f32_16x16x32_bf16 v[216:219], a[136:139], a[8:11], v[216:219]\n\t"
          "v_mfma_f32_16x16x32_bf16 v[212:215], a[136:139], a[4:7], v[212:215]\n\t"
          "ds_read_b128 a[156:159], v17 offset:11264\n\t"
          "buffer_load_dwordx4 a[68:71], v14, %[ws], s44 offen offset:1024 sc1\n\t"
          "v_mfma_f32_16x16x32_bf16 v[212:215], a[140:143], a[20:23], v[212:215]\n\t"
          "v_mfma_f32_16x16x32_bf16 v[208:211], a[140:143], a[16:19], v[208:211]\n\t"
          "v_mfma_f32_16x16x32_bf16 v[208:211], a[136:139], a[0:3], v[208:211]\n\t"
          "s_waitcnt lgkmcnt(2)\n\t"
          "v_mfma_f32_16x16x32_bf16 v[224:227], a[144:147], a[0:3], v[224:227]\n\t"
          "ds_read_b128 a[128:131], v16 offset:0\n\t"
          "buffer_load_dwordx4 %[vx], %[ws], s60 offen offset:2048 sc1 lds\n\t"
          "v_mfma_f32_16x16x32_bf16 v[224:227], a[148:151], a[16:19], v[224:227]\n\t"
          "v_mfma_f32_16x16x32_bf16 v[228:231], a[148:151], a[20:23], v[228:231]\n\t"
          "v_mfma_f32_16x16x32_bf16 v[228:231], a[144:147], a[4:7], v[228:231]\n\t"
          "v_mfma_f32_16x16x32_bf16 v[232:235], a[144:147], a[8:11], v[232:235]\n\t"
          "ds_read_b128 a[132:135], v16 offset:8192\n\t"
          "buffer_load_dwordx4 a[72:75], v15, %[ws], s44 offen offset:0 sc1\n\t"
          "v_mfma_f32_16x16x32_bf16 v[232:235], a[148:151], a[24:27], v[232:235]\n\t"
          "v_mfma_f32_16x16x32_bf16 v[236:239], a[148:151], a[28:31], v[236:239]\n\t"
          "v_mfma_f32_16x16x32_bf16 v[236:239], a[144:147], a[12:15], v[236:239]\n\t"
          "buffer_load_dwordx4 %[vx], %[ws], s60 offen offset:3072 sc1 lds\n\t"
          "s_waitcnt lgkmcnt(2)\n\t"
          "v_mfma_f32_16x16x32_bf16 v[252:255], a[152:155], a[12:15], v[252:255]\n\t"
          "ds_read_b128 a[136:139], v16 offset:1024\n\t"
          "v_mfma_f32_16x16x32_bf16 v[252:255], a[156:159], a[28:31], v[252:255]\n\t"
          "v_mfma_f32_16x16x32_bf16 v[248:251], a[156:159], a[24:27], v[248:251]\n\t"
          "v_mfma_f32_16x16x32_bf16 v[248:251], a[152:155], a[8:11], v[248:251]\n\t"
          "buffer_load_dwordx4 a[76:79], v15, %[ws], s44 offen offset:1024 sc1\n\t"
          "v_mfma_f32_16x16x32_bf16 v[244:247], a[152:155], a[4:7], v[244:247]\n\t"
          "ds_read_b128 a[140:143], v16 offset:9216\n\t"
          "v_mfma_f32_16x16x32_bf16 v[244:247], a[156:159], a[20:23], v[244:247]\n\t"
          "v_mfma_f32_16x16x32_bf16 v[240:243], a[156:159], a[16:19], v[240:243]\n\t"
          "s_add_u32 m0, s59, 69632\n\t"
          "v_mfma_f32_16x16x32_bf16 v[240:243], a[152:155], a[0:3], v[240:243]\n\t"
          "buffer_load_dwordx4 %[vx], %[ws], s61 offen offset:0 sc1 lds\n\t"
          "s_waitcnt lgkmcnt(2)\n\t"
          "v_mfma_f32_16x16x32_bf16 v[128:131], a[128:131], a[0:3], v[128:131]\n\t"
          "ds_read_b128 a[144:147], v16 offset:2048\n\t"
          "v_mfma_f32_16x16x32_bf16 v[128:131], a[132:135], a[16:19], v[128:131]\n\t"
          "v_mfma_f32_16x16x32_bf16 v[132:135], a[132:135], a[20:23], v[132:135]\n\t"
          "buffer_load_dwordx4 a[80:83], v14, %[ws], s48 offen offset:0 sc1\n\t"
          "v_mfma_f32_16x16x32_bf16 v[132:135], a[128:131], a[4:7], v[132:135]\n\t"
          "v_mfma_f32_16x16x32_bf16 v[136:139], a[128:131], a[8:11], v[136:139]\n\t"
          "ds_read_b128 a[148:151], v16 offset:10240\n\t"
          "v_mfma_f32_16x16x32_bf16 v[136:139], a[132:135], a[24:27], v[136:139]\n\t"
          "v_mfma_f32_16x16x32_bf16 v[140:143], a[132:135], a[28:31], v[140:143]\n\t"
          "buffer_load_dwordx4 %[vx], %[ws], s61 offen offset:1024 sc1 lds\n\t"
          "v_mfma_f32_16x16x32_bf16 v[140:143], a[128:131], a[12:15], v[140:143]\n\t"
          "s_waitcnt lgkmcnt(2)\n\t"
          "v_mfma_f32_16x16x32_bf16 v[156:159], a[136:139], a[12:15], v[156:159]\n\t"
          "ds_read_b128 a[152:155], v16 offset:3072\n\t"
          "v_mfma_f32_16x16x32_bf16 v[156:159], a[140:143], a[28:31], v[156:159]\n\t"
          "buffer_load_dwordx4 a[84:87], v14, %[ws], s48 offen offset:1024 sc1\n\t"
          "v_mfma_f32_16x16x32_bf16 v[152:155], a[140:143], a[24:27], v[152:155]\n\t"
          "v_mfma_f32_16x16x32_bf16 v[152:155], a[136:139], a[8:11], v[152:155]\n\t"
          "v_mfma_f32_16x16x32_bf16 v[148:151], a[136:139], a[4:7], v[148:151]\n\t"
          "ds_read_b128 a[156:159], v16 offset:11264\n\t"
          "v_mfma_f32_16x16x32_bf16 v[148:151], a[140:143], a[20:23], v[148:151]\n\t"
          "buffer_load_dwordx4 %[vx], %[ws], s61 offen offset:2048 sc1 lds\n\t"
          "v_mfma_f32_16x16x32_bf16 v[144:147], a[140:143], a[16:19], v[144:147]\n\t"
          "v_mfma_f32_16x16x32_bf16 v[144:147], a[136:139], a[0:3], v[144:147]\n\t"
          "s_waitcnt vmcnt(14)\n\t"
          "s_barrier\n\t"
          "s_waitcnt lgkmcnt(2)\n\t"
          "v_mfma_f32_16x16x32_bf16 v[160:163], a[144:147], a[0:3], v[160:163]\n\t"
          "ds_read_b128 a[128:131], v17 offset:32768\n\t"
          "v_mfma_f32_16x16x32_bf16 v[160:163], a[148:151], a[16:19], v[160:163]\n\t"
          "buffer_load_dwordx4 a[88:91], v15, %[ws], s48 offen offset:0 sc1\n\t"
          "v_mfma_f32_16x16x32_bf16 v[164:167], a[148:151], a[20:23], v[164:167]\n\t"
          "v_mfma_f32_16x16x32_bf16 v[164:167], a[144:147], a[4:7], v[164:167]\n\t"
          "v_mfma_f32_16x16x32_bf16 v[168:171], a[144:147], a[8:11], v[168:171]\n\t"
          "ds_read_b128 a[132:135], v17 offset:40960\n\t"
          "buffer_load_dwordx4 %[vx], %[ws], s61 offen offset:3072 sc1 lds\n\t"
          "s_add_u32 s60, s60, 16384\n\t"
          "s_add_u32 s61, s61, 16384\n\t"
          "v_mfma_f32_16x16x32_bf16 v[168:171], a[148:151], a[24:27], v[168:171]\n\t"
          "v_mfma_f32_16x16x32_bf16 v[172:175], a[148:151], a[28:31], v[172:175]\n\t"
          "v_mfma_f32_16x16x32_bf16 v[172:175], a[144:147], a[12:15], v[172:175]\n\t"
          "s_waitcnt lgkmcnt(2)\n\t"
          "v_mfma_f32_16x16x32_bf16 v[188:191], a[152:155], a[12:15], v[188:191]\n\t"
          "ds_read_b128 a[136:139], v17 offset:33792\n\t"
          "buffer_load_dwordx4 a[92:95], v15, %[ws], s48 offen offset:1024 sc1\n\t"
          "s_add_u32 s44, s44, 8192\n\t"
          "s_add_u32 s48, s48, 8192\n\t"
          "v_mfma_f32_16x16x32_bf16 v[188:191], a[156:159], a[28:31], v[188:191]\n\t"
          "v_mfma_f32_16x16x32_bf16 v[184:187], a[156:159], a[24:27], v[184:187]\n\t"
          "v_mfma_f32_16x16x32_bf16 v[184:187], a[152:155], a[8:11], v[184:187]\n\t"
          "v_mfma_f32_16x16x32_bf16 v[180:183], a[152:155], a[4:7], v[180:183]\n\t"
          "ds_read_b128 a[140:143], v17 offset:41984\n\t"
          "v_mfma_f32_16x16x32_bf16 v[180:183], a[156:159], a[20:23], v[180:183]\n\t"
          "v_mfma_f32_16x16x32_bf16 v[176:179], a[156:159], a[16:19], v[176:179]\n\t"
          "v_mfma_f32_16x16x32_bf16 v[176:179], a[152:155], a[0:3], v[176:179]\n\t"
          "s_waitcnt vmcnt(16)\n\t"
          "s_waitcnt lgkmcnt(2)\n\t"
          "v_mfma_f32_16x16x32_bf16 v[192:195], a[128:131], a[32:35], v[192:195]\n\t"
          "ds_read_b128 a[144:147], v17 offset:34816\n\t"
          "v_mfma_f32_16x16x32_bf16 v[192:195], a[132:135], a[48:51], v[192:195]\n\t"
          "s_add_u32 m0, s59, 98304\n\t"
          "v_mfma_f32_16x16x32_bf16 v[196:199], a[132:135], a[52:55], v[196:199]\n\t"
          "buffer_load_dwordx4 %[vx], %[ws], s60 offen offset:0 sc1 lds\n\t"
          "v_mfma_f32_16x16x32_bf16 v[196:199], a[128:131], a[36:39], v[196:199]\n\t"
          "v_mfma_f32_16x16x32_bf16 v[200:203], a[128:131], a[40:43], v[200:203]\n\t"
          "ds_read_b128 a[148:151], v17 offset:43008\n\t"
          "v_mfma_f32_16x16x32_bf16 v[200:203], a[132:135], a[56:59], v[200:203]\n\t"
          "buffer_load_dwordx4 a[96:99], v14, %[ws], s44 offen offset:0 sc1\n\t"
          "v_mfma_f32_16x16x32_bf16 v[204:207], a[132:135], a[60:63], v[204:207]\n\t"
          "v_mfma_f32_16x16x32_bf16 v[204:207], a[128:131], a[44:47], v[204:207]\n\t"
          "s_waitcnt lgkmcnt(2)\n\t"
          "v_mfma_f32_16x16x32_bf16 v[220:223], a[136:139], a[44:47], v[220:223]\n\t"
          "ds_read_b128 a[152:155], v17 offset:35840\n\t"
          "v_mfma_f32_16x16x32_bf16 v[220:223], a[140:143], a[60:63], v[220:223]\n\t"
          "buffer_load_dwordx4 %[vx], %[ws], s60 offen offset:1024 sc1 lds\n\t"
          "v_mfma_f32_16x16x32_bf16 v[216:219], a[140:143], a[56:59], v[216:219]\n\t"
          "v_mfma_f32_16x16x32_bf16 v[216:219], a[136:139], a[40:43], v[216:219]\n\t"
          "v_mfma_f32_16x16x32_bf16 v[212:215], a[136:139], a[36:39], v[212:215]\n\t"
          "ds_read_b128 a[156:159], v17 offset:44032\n\t"
          "buffer_load_dwordx4 a[100:103], v14, %[ws], s44 offen offset:1024 sc1\n\t"
          "v_mfma_f32_16x16x32_bf16 v[212:215], a[140:143], a[52:55], v[212:215]\n\t"
          "v_mfma_f32_16x16x32_bf16 v[208:211], a[140:143], a[48:51], v[208:211]\n\t"
          "v_mfma_f32_16x16x32_bf16 v[208:211], a[136:139], a[32:35], v[208:211]\n\t"
          "s_waitcnt lgkmcnt(2)\n\t"
          "v_mfma_f32_16x16x32_bf16 v[224:227], a[144:147], a[32:35], v[224:227]\n\t"
          "ds_read_b128 a[128:131], v16 offset:32768\n\t"
          "buffer_load_dwordx4 %[vx], %[ws], s60 offen offset:2048 sc1 lds\n\t"
          "v_mfma_f32_16x16x32_bf16 v[224:227], a[148:151], a[48:51], v[224:227]\n\t"
          "v_mfma_f32_16x16x32_bf16 v[228:231], a[148:151], a[52:55], v[228:231]\n\t"
          "v_mfma_f32_16x16x32_bf16 v[228:231], a[144:147], a[36:39], v[228:231]\n\t"
          "v_mfma_f32_16x16x32_bf16 v[232:235], a[144:147], a[40:43], v[232:235]\n\t"
          "ds_read_b128 a[132:135], v16 offset:40960\n\t"
          "buffer_load_dwordx4 a[104:107], v15, %[ws], s44 offen offset:0 sc1\n\t"
          "v_mfma_f32_16x16x32_bf16 v[232:235], a[148:151], a[56:59], v[232:235]\n\t"
          "v_mfma_f32_16x16x32_bf16 v[236:239], a[148:151], a[60:63], v[236:239]\n\t"
          "v_mfma_f32_16x16x32_bf16 v[236:239], a[144:147], a[44:47], v[236:239]\n\t"
          "buffer_load_dwordx4 %[vx], %[ws], s60 offen offset:3072 sc1 lds\n\t"
          "s_waitcnt lgkmcnt(2)\n\t"
          "v_mfma_f32_16x16x32_bf16 v[252:255], a[152:155], a[44:47], v[252:255]\n\t"
          "ds_read_b128 a[136:139], v16 offset:33792\n\t"
          "v_mfma_f32_16x16x32_bf16 v[252:255], a[156:159], a[60:63], v[252:255]\n\t"
          "v_mfma_f32_16x16x32_bf16 v[248:251], a[156:159], a[56:59], v[248:251]\n\t"
          "v_mfma_f32_16x16x32_bf16 v[248:251], a[152:155], a[40:43], v[248:251]\n\t"
          "buffer_load_dwordx4 a[108:111], v15, %[ws], s44 offen offset:1024 sc1\n\t"
          "v_mfma_f32_16x16x32_bf16 v[244:247], a[152:155], a[36:39], v[244:247]\n\t"
          "ds_read_b128 a[140:143], v16 offset:41984\n\t"
          "v_mfma_f32_16x16x32_bf16 v[244:247], a[156:159], a[52:55], v[244:247]\n\t"
          "v_mfma_f32_16x16x32_bf16 v[240:243], a[156:159], a[48:51], v[240:243]\n\t"
          "s_add_u32 m0, s59, 102400\n\t"
          "v_mfma_f32_16x16x32_bf16 v[240:243], a[152:155], a[32:35], v[240:243]\n\t"
          "buffer_load_dwordx4 %[vx], %[ws], s61 offen offset:0 sc1 lds\n\t"
          "s_waitcnt lgkmcnt(2)\n\t"
          "v_mfma_f32_16x16x32_bf16 v[128:131], a[128:131], a[32:35], v[128:131]\n\t"
          "ds_read_b128 a[144:147], v16 offset:34816\n\t"
          "v_mfma_f32_16x16x32_bf16 v[128:131], a[132:135], a[48:51], v[128:131]\n\t"
          "v_mfma_f32_16x16x32_bf16 v[132:135], a[132:135], a[52:55], v[132:135]\n\t"
          "buffer_load_dwordx4 a[112:115], v14, %[ws], s48 offen offset:0 sc1\n\t"
          "v_mfma_f32_16x16x32_bf16 v[132:135], a[128:131], a[36:39], v[132:135]\n\t"
          "v_mfma_f32_16x16x32_bf16 v[136:139], a[128:131], a[40:43], v[136:139]\n\t"
          "ds_read_b128 a[148:151], v16 offset:43008\n\t"
          "v_mfma_f32_16x16x32_bf16 v[136:139], a[132:135], a[56:59], v[136:139]\n\t"
          "v_mfma_f32_16x16x32_bf16 v[140:143], a[132:135], a[60:63], v[140:143]\n\t"
          "buffer_load_dwordx4 %[vx], %[ws], s61 offen offset:1024 sc1 lds\n\t"
          "v_mfma_f32_16x16x32_bf16 v[140:143], a[128:131], a[44:47], v[140:143]\n\t"
          "s_waitcnt lgkmcnt(2)\n\t"
          "v_mfma_f32_16x16x32_bf16 v[156:159], a[136:139], a[44:47], v[156:159]\n\t"
          "ds_read_b128 a[152:155], v16 offset:35840\n\t"
          "v_mfma_f32_16x16x32_bf16 v[156:159], a[140:143], a[60:63], v[156:159]\n\t"
          "buffer_load_dwordx4 a[116:119], v14, %[ws], s48 offen offset:1024 sc1\n\t"
          "v_mfma_f32_16x16x32_bf16 v[152:155], a[140:143], a[56:59], v[152:155]\n\t"
          "v_mfma_f32_16x16x32_bf16 v[152:155], a[136:139], a[40:43], v[152:155]\n\t"
          "v_mfma_f32_16x16x32_bf16 v[148:151], a[136:139], a[36:39], v[148:151]\n\t"
          "ds_read_b128 a[156:159], v16 offset:44032\n\t"
          "v_mfma_f32_16x16x32_bf16 v[148:151], a[140:143], a[52:55], v[148:151]\n\t"
          "buffer_load_dwordx4 %[vx], %[ws], s61 offen offset:2048 sc1 lds\n\t"
          "v_mfma_f32_16x16x32_bf16 v[144:147], a[140:143], a[48:51], v[144:147]\n\t"
          "v_mfma_f32_16x16x32_bf16 v[144:147], a[136:139], a[32:35], v[144:147]\n\t"
          "s_waitcnt vmcnt(14)\n\t"
          "s_barrier\n\t"
          "s_waitcnt lgkmcnt(2)\n\t"
          "v_mfma_f32_16x16x32_bf16 v[160:163], a[144:147], a[32:35], v[160:163]\n\t"
          "ds_read_b128 a[128:131], v19 offset:0\n\t"
          "v_mfma_f32_16x16x32_bf16 v[160:163], a[148:151], a[48:51], v[160:163]\n\t"
          "buffer_load_dwordx4 a[120:123], v15, %[ws], s48 offen offset:0 sc1\n\t"
          "v_mfma_f32_16x16x32_bf16 v[164:167], a[148:151], a[52:55], v[164:167]\n\t"
          "v_mfma_f32_16x16x32_bf16 v[164:167], a[144:147], a[36:39], v[164:167]\n\t"
          "v_mfma_f32_16x16x32_bf16 v[168:171], a[144:147], a[40:43], v[168:171]\n\t"
          "ds_read_b128 a[132:135], v19 offset:8192\n\t"
          "buffer_load_dwordx4 %[vx], %[ws], s61 offen offset:3072 sc1 lds\n\t"
          "s_add_u32 s60, s60, 16384\n\t"
          "s_add_u32 s61, s61, 16384\n\t"
          "v_mfma_f32_16x16x32_bf16 v[168:171], a[148:151], a[56:59], v[168:171]\n\t"
          "v_mfma_f32_16x16x32_bf16 v[172:175], a[148:151], a[60:63], v[172:175]\n\t"
          "v_mfma_f32_16x16x32_bf16 v[172:175], a[144:147], a[44:47], v[172:175]\n\t"
          "s_waitcnt lgkmcnt(2)\n\t"
          "v_mfma_f32_16x16x32_bf16 v[188:191], a[152:155], a[44:47], v[188:191]\n\t"
          "ds_read_b128 a[136:139], v19 offset:1024\n\t"
          "buffer_load_dwordx4 a[124:127], v15, %[ws], s48 offen offset:1024 sc1\n\t"
          "s_add_u32 s44, s44, 8192\n\t"
          "s_add_u32 s48, s48, 8192\n\t"
          "v_mfma_f32_16x16x32_bf16 v[188:191], a[156:159], a[60:63], v[188:191]\n\t"
          "v_mfma_f32_16x16x32_bf16 v[184:187], a[156:159], a[56:59], v[184:187]\n\t"
          "v_mfma_f32_16x16x32_bf16 v[184:187], a[152:155], a[40:43], v[184:187]\n\t"
          "v_mfma_f32_16x16x32_bf16 v[180:183], a[152:155], a[36:39], v[180:183]\n\t"
          "ds_read_b128 a[140:143], v19 offset:9216\n\t"
          "v_mfma_f32_16x16x32_bf16 v[180:183], a[156:159], a[52:55], v[180:183]\n\t"
          "v_mfma_f32_16x16x32_bf16 v[176:179], a[156:159], a[48:51], v[176:179]\n\t"
          "v_mfma_f32_16x16x32_bf16 v[176:179], a[152:155], a[32:35], v[176:179]\n\t"
          "s_waitcnt vmcnt(16)\n\t"
          "s_waitcnt lgkmcnt(2)\n\t"
          "v_mfma_f32_16x16x32_bf16 v[192:195], a[128:131], a[64:67], v[192:195]\n\t"
          "ds_read_b128 a[144:147], v19 offset:2048\n\t"
          "v_mfma_f32_16x16x32_bf16 v[192:195], a[132:135], a[80:83], v[192:195]\n\t"
          "s_add_u32 m0, s59, 0\n\t"
          "v_mfma_f32_16x16x32_bf16 v[196:199], a[132:135], a[84:87], v[196:199]\n\t"
          "buffer_load_dwordx4 %[vx], %[ws], s60 offen offset:0 sc1 lds\n\t"
          "v_mfma_f32_16x16x32_bf16 v[196:199], a[128:131], a[68:71], v[196:199]\n\t"
          "v_mfma_f32_16x16x32_bf16 v[200:203], a[128:131], a[72:75], v[200:203]\n\t"
          "ds_read_b128 a[148:151], v19 offset:10240\n\t"
          "v_mfma_f32_16x16x32_bf16 v[200:203], a[132:135], a[88:91], v[200:203]\n\t"
          "buffer_load_dwordx4 a[0:3], v14, %[ws], s44 offen offset:0 sc1\n\t"
          "v_mfma_f32_16x16x32_bf16 v[204:207], a[132:135], a[92:95], v[204:207]\n\t"
          "v_mfma_f32_16x16x32_bf16 v[204:207], a[128:131], a[76:79], v[204:207]\n\t"
          "s_waitcnt lgkmcnt(2)\n\t"
          "v_mfma_f32_16x16x32_bf16 v[220:223], a[136:139], a[76:79], v[220:223]\n\t"
          "ds_read_b128 a[152:155], v19 offset:3072\n\t"
          "v_mfma_f32_16x16x32_bf16 v[220:223], a[140:143], a[92:95], v[220:223]\n\t"
          "buffer_load_dwordx4 %[vx], %[ws], s60 offen offset:1024 sc1 lds\n\t"
          "v_mfma_f32_16x16x32_bf16 v[216:219], a[140:143], a[88:91], v[216:219]\n\t"
          "v_mfma_f32_16x16x32_bf16 v[216:219], a[136:139], a[72:75], v[216:219]\n\t"
          "v_mfma_f32_16x16x32_bf16 v[212:215], a[136:139], a[68:71], v[212:215]\n\t"
          "ds_read_b128 a[156:159], v19 offset:11264\n\t"
          "buffer_load_dwordx4 a[4:7], v14, %[ws], s44 offen offset:1024 sc1\n\t"
          "v_mfma_f32_16x16x32_bf16 v[212:215], a[140:143], a[84:87], v[212:215]\n\t"
          "v_mfma_f32_16x16x32_bf16 v[208:211], a[140:143], a[80:83], v[208:211]\n\t"
          "v_mfma_f32_16x16x32_bf16 v[208:211], a[136:139], a[64:67], v[208:211]\n\t"
          "s_waitcnt lgkmcnt(2)\n\t"
          "v_mfma_f32_16x16x32_bf16 v[224:227], a[144:147], a[64:67], v[224:227]\n\t"
          "ds_read_b128 a[128:131], v18 offset:0\n\t"
          "buffer_load_dwordx4 %[vx], %[ws], s60 offen offset:2048 sc1 lds\n\t"
          "v_mfma_f32_16x16x32_bf16 v[224:227], a[148:151], a[80:83], v[224:227]\n\t"
          "v_mfma_f32_16x16x32_bf16 v[228:231], a[148:151], a[84:87], v[228:231]\n\t"
          "v_mfma_f32_16x16x32_bf16 v[228:231], a[144:147], a[68:71], v[228:231]\n\t"
          "v_mfma_f32_16x16x32_bf16 v[232:235], a[144:147], a[72:75], v[232:235]\n\t"
          "ds_read_b128 a[132:135], v18 offset:8192\n\t"
          "buffer_load_dwordx4 a[8:11], v15, %[ws], s44 offen offset:0 sc1\n\t"
          "v_mfma_f32_16x16x32_bf16 v[232:235], a[148:151], a[88:91], v[232:235]\n\t"
          "v_mfma_f32_16x16x32_bf16 v[236:239], a[148:151], a[92:95], v[236:239]\n\t"
          "v_mfma_f32_16x16x32_bf16 v[236:239], a[144:147], a[76:79], v[236:239]\n\t"
          "buffer_load_dwordx4 %[vx], %[ws], s60 offen offset:3072 sc1 lds\n\t"
          "s_waitcnt lgkmcnt(2)\n\t"
          "v_mfma_f32_16x16x32_bf16 v[252:255], a[152:155], a[76:79], v[252:255]\n\t"
          "ds_read_b128 a[136:139], v18 offset:1024\n\t"
          "v_mfma_f32_16x16x32_bf16 v[252:255], a[156:159], a[92:95], v[252:255]\n\t"
          "v_mfma_f32_16x16x32_bf16 v[248:251], a[156:159], a[88:91], v[248:251]\n\t"
          "v_mfma_f32_16x16x32_bf16 v[248:251], a[152:155], a[72:75], v[248:251]\n\t"
          "buffer_load_dwordx4 a[12:15], v15, %[ws], s44 offen offset:1024 sc1\n\t"
          "v_mfma_f32_16x16x32_bf16 v[244:247], a[152:155], a[68:71], v[244:247]\n\t"
          "ds_read_b128 a[140:143], v18 offset:9216\n\t"
          "v_mfma_f32_16x16x32_bf16 v[244:247], a[156:159], a[84:87], v[244:247]\n\t"
          "v_mfma_f32_16x16x32_bf16 v[240:243], a[156:159], a[80:83], v[240:243]\n\t"
          "s_add_u32 m0, s59, 4096\n\t"
          "v_mfma_f32_16x16x32_bf16 v[240:243], a[152:155], a[64:67], v[240:243]\n\t"
          "buffer_load_dwordx4 %[vx], %[ws], s61 offen offset:0 sc1 lds\n\t"
          "s_waitcnt lgkmcnt(2)\n\t"
          "v_mfma_f32_16x16x32_bf16 v[128:131], a[128:131], a[64:67], v[128:131]\n\t"
          "ds_read_b128 a[144:147], v18 offset:2048\n\t"
          "v_mfma_f32_16x16x32_bf16 v[128:131], a[132:135], a[80:83], v[128:131]\n\t"
          "v_mfma_f32_16x16x32_bf16 v[132:135], a[132:135], a[84:87], v[132:135]\n\t"
          "buffer_load_dwordx4 a[16:19], v14, %[ws], s48 offen offset:0 sc1\n\t"
          "v_mfma_f32_16x16x32_bf16 v[132:135], a[128:131], a[68:71], v[132:135]\n\t"
          "v_mfma_f32_16x16x32_bf16 v[136:139], a[128:131], a[72:75], v[136:139]\n\t"
          "ds_read_b128 a[148:151], v18 offset:10240\n\t"
          "v_mfma_f32_16x16x32_bf16 v[136:139], a[132:135], a[88:91], v[136:139]\n\t"
          "v_mfma_f32_16x16x32_bf16 v[140:143], a[132:135], a[92:95], v[140:143]\n\t"
          "buffer_load_dwordx4 %[vx], %[ws], s61 offen offset:1024 sc1 lds\n\t"
          "v_mfma_f32_16x16x32_bf16 v[140:143], a[128:131], a[76:79], v[140:143]\n\t"
          "s_waitcnt lgkmcnt(2)\n\t"
          "v_mfma_f32_16x16x32_bf16 v[156:159], a[136:139], a[76:79], v[156:159]\n\t"
          "ds_read_b128 a[152:155], v18 offset:3072\n\t"
          "v_mfma_f32_16x16x32_bf16 v[156:159], a[140:143], a[92:95], v[156:159]\n\t"
          "buffer_load_dwordx4 a[20:23], v14, %[ws], s48 offen offset:1024 sc1\n\t"
          "v_mfma_f32_16x16x32_bf16 v[152:155], a[140:143], a[88:91], v[152:155]\n\t"
          "v_mfma_f32_16x16x32_bf16 v[152:155], a[136:139], a[72:75], v[152:155]\n\t"
          "v_mfma_f32_16x16x32_bf16 v[148:151], a[136:139], a[68:71], v[148:151]\n\t"
          "ds_read_b128 a[156:159], v18 offset:11264\n\t"
          "v_mfma_f32_16x16x32_bf16 v[148:151], a[140:143], a[84:87], v[148:151]\n\t"
          "buffer_load_dwordx4 %[vx], %[ws], s61 offen offset:2048 sc1 lds\n\t"
          "v_mfma_f32_16x16x32_bf16 v[144:147], a[140:143], a[80:83], v[144:147]\n\t"
          "v_mfma_f32_16x16x32_bf16 v[144:147], a[136:139], a[64:67], v[144:147]\n\t"
          "s_waitcnt vmcnt(14)\n\t"
          "s_barrier\n\t"
          "s_waitcnt lgkmcnt(2)\n\t"
          "v_mfma_f32_16x16x32_bf16 v[160:163], a[144:147], a[64:67], v[160:163]\n\t"
          "ds_read_b128 a[128:131], v19 offset:32768\n\t"
          "v_mfma_f32_16x16x32_bf16 v[160:163], a[148:151], a[80:83], v[160:163]\n\t"
          "buffer_load_dwordx4 a[24:27], v15, %[ws], s48 offen offset:0 sc1\n\t"
          "v_mfma_f32_16x16x32_bf16 v[164:167], a[148:151], a[84:87], v[164:167]\n\t"
          "v_mfma_f32_16x16x32_bf16 v[164:167], a[144:147], a[68:71], v[164:167]\n\t"
          "v_mfma_f32_16x16x32_bf16 v[168:171], a[144:147], a[72:75], v[168:171]\n\t"
          "ds_read_b128 a[132:135], v19 offset:40960\n\t"
          "buffer_load_dwordx4 %[vx], %[ws], s61 offen offset:3072 sc1 lds\n\t"
          "s_add_u32 s60, s60, 16384\n\t"
          "s_add_u32 s61, s61, 16384\n\t"
          "v_mfma_f32_16x16x32_bf16 v[168:171], a[148:151], a[88:91], v[168:171]\n\t"
          "v_mfma_f32_16x16x32_bf16 v[172:175], a[148:151], a[92:95], v[172:175]\n\t"
          "v_mfma_f32_16x16x32_bf16 v[172:175], a[144:147], a[76:79], v[172:175]\n\t"
          "s_waitcnt lgkmcnt(2)\n\t"
          "v_mfma_f32_16x16x32_bf16 v[188:191], a[152:155], a[76:79], v[188:191]\n\t"
          "ds_read_b128 a[136:139], v19 offset:33792\n\t"
          "buffer_load_dwordx4 a[28:31], v15, %[ws], s48 offen offset:1024 sc1\n\t"
          "s_add_u32 s44, s44, 8192\n\t"
          "s_add_u32 s48, s48, 8192\n\t"
          "v_mfma_f32_16x16x32_bf16 v[188:191], a[156:159], a[92:95], v[188:191]\n\t"
          "v_mfma_f32_16x16x32_bf16 v[184:187], a[156:159], a[88:91], v[184:187]\n\t"
          "v_mfma_f32_16x16x32_bf16 v[184:187], a[152:155], a[72:75], v[184:187]\n\t"
          "v_mfma_f32_16x16x32_bf16 v[180:183], a[152:155], a[68:71], v[180:183]\n\t"
          "ds_read_b128 a[140:143], v19 offset:41984\n\t"
          "v_mfma_f32_16x16x32_bf16 v[180:183], a[156:159], a[84:87], v[180:183]\n\t"
          "v_mfma_f32_16x16x32_bf16 v[176:179], a[156:159], a[80:83], v[176:179]\n\t"
          "v_mfma_f32_16x16x32_bf16 v[176:179], a[152:155], a[64:67], v[176:179]\n\t"
          "s_waitcnt vmcnt(16)\n\t"
          "s_waitcnt lgkmcnt(2)\n\t"
          "v_mfma_f32_16x16x32_bf16 v[192:195], a[128:131], a[96:99], v[192:195]\n\t"
          "ds_read_b128 a[144:147], v19 offset:34816\n\t"
          "v_mfma_f32_16x16x32_bf16 v[192:195], a[132:135], a[112:115], v[192:195]\n\t"
          "s_add_u32 m0, s59, 32768\n\t"
          "v_mfma_f32_16x16x32_bf16 v[196:199], a[132:135], a[116:119], v[196:199]\n\t"
          "buffer_load_dwordx4 %[vx], %[ws], s60 offen offset:0 sc1 lds\n\t"
          "v_mfma_f32_16x16x32_bf16 v[196:199], a[128:131], a[100:103], v[196:199]\n\t"
          "v_mfma_f32_16x16x32_bf16 v[200:203], a[128:131], a[104:107], v[200:203]\n\t"
          "ds_read_b128 a[148:151], v19 offset:43008\n\t"
          "v_mfma_f32_16x16x32_bf16 v[200:203], a[132:135], a[120:123], v[200:203]\n\t"
          "buffer_load_dwordx4 a[32:35], v14, %[ws], s44 offen offset:0 sc1\n\t"
          "v_mfma_f32_16x16x32_bf16 v[204:207], a[132:135], a[124:127], v[204:207]\n\t"
          "v_mfma_f32_16x16x32_bf16 v[204:207], a[128:131], a[108:111], v[204:207]\n\t"
          "s_waitcnt lgkmcnt(2)\n\t"
          "v_mfma_f32_16x16x32_bf16 v[220:223], a[136:139], a[108:111], v[220:223]\n\t"
          "ds_read_b128 a[152:155], v19 offset:35840\n\t"
          "v_mfma_f32_16x16x32_bf16 v[220:223], a[140:143], a[124:127], v[220:223]\n\t"
          "buffer_load_dwordx4 %[vx], %[ws], s60 offen offset:1024 sc1 lds\n\t"
          "v_mfma_f32_16x16x32_bf16 v[216:219], a[140:143], a[120:123], v[216:219]\n\t"
          "v_mfma_f32_16x16x32_bf16 v[216:219], a[136:139], a[104:107], v[216:219]\n\t"
          "v_mfma_f32_16x16x32_bf16 v[212:215], a[136:139], a[100:103], v[212:215]\n\t"
          "ds_read_b128 a[156:159], v19 offset:44032\n\t"
          "buffer_load_dwordx4 a[36:39], v14, %[ws], s44 offen offset:1024 sc1\n\t"
          "v_mfma_f32_16x16x32_bf16 v[212:215], a[140:143], a[116:119], v[212:215]\n\t"
          "v_mfma_f32_16x16x32_bf16 v[208:211], a[140:143], a[112:115], v[208:211]\n\t"
          "v_mfma_f32_16x16x32_bf16 v[208:211], a[136:139], a[96:99], v[208:211]\n\t"
          "s_waitcnt lgkmcnt(2)\n\t"
          "v_mfma_f32_16x16x32_bf16 v[224:227], a[144:147], a[96:99], v[224:227]\n\t"
          "ds_read_b128 a[128:131], v18 offset:32768\n\t"
          "buffer_load_dwordx4 %[vx], %[ws], s60 offen offset:2048 sc1 lds\n\t"
          "v_mfma_f32_16x16x32_bf16 v[224:227], a[148:151], a[112:115], v[224:227]\n\t"
          "v_mfma_f32_16x16x32_bf16 v[228:231], a[148:151], a[116:119], v[228:231]\n\t"
          "v_mfma_f32_16x16x32_bf16 v[228:231], a[144:147], a[100:103], v[228:231]\n\t"
          "v_mfma_f32_16x16x32_bf16 v[232:235], a[144:147], a[104:107], v[232:235]\n\t"
          "ds_read_b128 a[132:135], v18 offset:40960\n\t"
          "buffer_load_dwordx4 a[40:43], v15, %[ws], s44 offen offset:0 sc1\n\t"
          "v_mfma_f32_16x16x32_bf16 v[232:235], a[148:151], a[120:123], v[232:235]\n\t"
          "v_mfma_f32_16x16x32_bf16 v[236:239], a[148:151], a[124:127], v[236:239]\n\t"
          "v_mfma_f32_16x16x32_bf16 v[236:239], a[144:147], a[108:111], v[236:239]\n\t"
          "buffer_load_dwordx4 %[vx], %[ws], s60 offen offset:3072 sc1 lds\n\t"
          "s_waitcnt lgkmcnt(2)\n\t"
          "v_mfma_f32_16x16x32_bf16 v[252:255], a[152:155], a[108:111], v[252:255]\n\t"
          "ds_read_b128 a[136:139], v18 offset:33792\n\t"
          "v_mfma_f32_16x16x32_bf16 v[252:255], a[156:159], a[124:127], v[252:255]\n\t"
          "v_mfma_f32_16x16x32_bf16 v[248:251], a[156:159], a[120:123], v[248:251]\n\t"
          "v_mfma_f32_16x16x32_bf16 v[248:251], a[152:155], a[104:107], v[248:251]\n\t"
          "buffer_load_dwordx4 a[44:47], v15, %[ws], s44 offen offset:1024 sc1\n\t"
          "v_mfma_f32_16x16x32_bf16 v[244:247], a[152:155], a[100:103], v[244:247]\n\t"
          "ds_read_b128 a[140:143], v18 offset:41984\n\t"
          "v_mfma_f32_16x16x32_bf16 v[244:247], a[156:159], a[116:119], v[244:247]\n\t"
          "v_mfma_f32_16x16x32_bf16 v[240:243], a[156:159], a[112:115], v[240:243]\n\t"
          "s_add_u32 m0, s59, 36864\n\t"
          "v_mfma_f32_16x16x32_bf16 v[240:243], a[152:155], a[96:99], v[240:243]\n\t"
          "buffer_load_dwordx4 %[vx], %[ws], s61 offen offset:0 sc1 lds\n\t"
          "s_waitcnt lgkmcnt(2)\n\t"
          "v_mfma_f32_16x16x32_bf16 v[128:131], a[128:131], a[96:99], v[128:131]\n\t"
          "ds_read_b128 a[144:147], v18 offset:34816\n\t"
          "v_mfma_f32_16x16x32_bf16 v[128:131], a[132:135], a[112:115], v[128:131]\n\t"
          "v_mfma_f32_16x16x32_bf16 v[132:135], a[132:135], a[116:119], v[132:135]\n\t"
          "buffer_load_dwordx4 a[48:51], v14, %[ws], s48 offen offset:0 sc1\n\t"
          "v_mfma_f32_16x16x32_bf16 v[132:135], a[128:131], a[100:103], v[132:135]\n\t"
          "v_mfma_f32_16x16x32_bf16 v[136:139], a[128:131], a[104:107], v[136:139]\n\t"
          "ds_read_b128 a[148:151], v18 offset:43008\n\t"
          "v_mfma_f32_16x16x32_bf16 v[136:139], a[132:135], a[120:123], v[136:139]\n\t"
          "v_mfma_f32_16x16x32_bf16 v[140:143], a[132:135], a[124:127], v[140:143]\n\t"
          "buffer_load_dwordx4 %[vx], %[ws], s61 offen offset:1024 sc1 lds\n\t"
          "v_mfma_f32_16x16x32_bf16 v[140:143], a[128:131], a[108:111], v[140:143]\n\t"
          "s_waitcnt lgkmcnt(2)\n\t"
          "v_mfma_f32_16x16x32_bf16 v[156:159], a[136:139], a[108:111], v[156:159]\n\t"
          "ds_read_b128 a[152:155], v18 offset:35840\n\t"
          "v_mfma_f32_16x16x32_bf16 v[156:159], a[140:143], a[124:127], v[156:159]\n\t"
          "buffer_load_dwordx4 a[52:55], v14, %[ws], s48 offen offset:1024 sc1\n\t"
          "v_mfma_f32_16x16x32_bf16 v[152:155], a[140:143], a[120:123], v[152:155]\n\t"
          "v_mfma_f32_16x16x32_bf16 v[152:155], a[136:139], a[104:107], v[152:155]\n\t"
          "v_mfma_f32_16x16x32_bf16 v[148:151], a[136:139], a[100:103], v[148:151]\n\t"
          "ds_read_b128 a[156:159], v18 offset:44032\n\t"
          "v_mfma_f32_16x16x32_bf16 v[148:151], a[140:143], a[116:119], v[148:151]\n\t"
          "buffer_load_dwordx4 %[vx], %[ws], s61 offen offset:2048 sc1 lds\n\t"
          "v_mfma_f32_16x16x32_bf16 v[144:147], a[140:143], a[112:115], v[144:147]\n\t"
          "v_mfma_f32_16x16x32_bf16 v[144:147], a[136:139], a[96:99], v[144:147]\n\t"
          "s_waitcnt vmcnt(14)\n\t"
          "s_barrier\n\t"
          "s_waitcnt lgkmcnt(2)\n\t"
          "v_mfma_f32_16x16x32_bf16 v[160:163], a[144:147], a[96:99], v[160:163]\n\t"
          "ds_read_b128 a[128:131], v17 offset:0\n\t"
          "v_mfma_f32_16x16x32_bf16 v[160:163], a[148:151], a[112:115], v[160:163]\n\t"
          "buffer_load_dwordx4 a[56:59], v15, %[ws], s48 offen offset:0 sc1\n\t"
          "v_mfma_f32_16x16x32_bf16 v[164:167], a[148:151], a[116:119], v[164:167]\n\t"
          "v_mfma_f32_16x16x32_bf16 v[164:167], a[144:147], a[100:103], v[164:167]\n\t"
          "v_mfma_f32_16x16x32_bf16 v[168:171], a[144:147], a[104:107], v[168:171]\n\t"
          "ds_read_b128 a[132:135], v17 offset:8192\n\t"
          "buffer_load_dwordx4 %[vx], %[ws], s61 offen offset:3072 sc1 lds\n\t"
          "s_add_u32 s60, s60, 16384\n\t"
          "s_add_u32 s61, s61, 16384\n\t"
          "v_mfma_f32_16x16x32_bf16 v[168:171], a[148:151], a[120:123], v[168:171]\n\t"
          "v_mfma_f32_16x16x32_bf16 v[172:175], a[148:151], a[124:127], v[172:175]\n\t"
          "v_mfma_f32_16x16x32_bf16 v[172:175], a[144:147], a[108:111], v[172:175]\n\t"
          "s_waitcnt lgkmcnt(2)\n\t"
          "v_mfma_f32_16x16x32_bf16 v[188:191], a[152:155], a[108:111], v[188:191]\n\t"
          "ds_read_b128 a[136:139], v17 offset:1024\n\t"
          "buffer_load_dwordx4 a[60:63], v15, %[ws], s48 offen offset:1024 sc1\n\t"
          "s_add_u32 s44, s44, 8192\n\t"
          "s_add_u32 s48, s48, 8192\n\t"
          "v_mfma_f32_16x16x32_bf16 v[188:191], a[156:159], a[124:127], v[188:191]\n\t"
          "v_mfma_f32_16x16x32_bf16 v[184:187], a[156:159], a[120:123], v[184:187]\n\t"
          "v_mfma_f32_16x16x32_bf16 v[184:187], a[152:155], a[104:107], v[184:187]\n\t"
          "v_mfma_f32_16x16x32_bf16 v[180:183], a[152:155], a[100:103], v[180:183]\n\t"
          "ds_read_b128 a[140:143], v17 offset:9216\n\t"
          "v_mfma_f32_16x16x32_bf16 v[180:183], a[156:159], a[116:119], v[180:183]\n\t"
          "v_mfma_f32_16x16x32_bf16 v[176:179], a[156:159], a[112:115], v[176:179]\n\t"
          "v_mfma_f32_16x16x32_bf16 v[176:179], a[152:155], a[96:99], v[176:179]\n\t"
          "s_waitcnt vmcnt(16)\n\t"
          "s_waitcnt lgkmcnt(2)\n\t"
          "v_mfma_f32_16x16x32_bf16 v[192:195], a[128:131], a[0:3], v[192:195]\n\t"
          "ds_read_b128 a[144:147], v17 offset:2048\n\t"
          "v_mfma_f32_16x16x32_bf16 v[192:195], a[132:135], a[16:19], v[192:195]\n\t"
          "v_mfma_f32_16x16x32_bf16 v[196:199], a[132:135], a[20:23], v[196:199]\n\t"
          "buffer_load_dwordx4 v[96:99], %[vx], %[ws], s56 offen offset:0 sc1\n\t"
          "v_mfma_f32_16x16x32_bf16 v[196:199], a[128:131], a[4:7], v[196:199]\n\t"
          "v_mfma_f32_16x16x32_bf16 v[200:203], a[128:131], a[8:11], v[200:203]\n\t"
          "ds_read_b128 a[148:151], v17 offset:10240\n\t"
          "v_mfma_f32_16x16x32_bf16 v[200:203], a[132:135], a[24:27], v[200:203]\n\t"
          "v_mfma_f32_16x16x32_bf16 v[204:207], a[132:135], a[28:31], v[204:207]\n\t"
          "v_mfma_f32_16x16x32_bf16 v[204:207], a[128:131], a[12:15], v[204:207]\n\t"
          "s_waitcnt lgkmcnt(2)\n\t"
          "v_mfma_f32_16x16x32_bf16 v[220:223], a[136:139], a[12:15], v[220:223]\n\t"
          "ds_read_b128 a[152:155], v17 offset:3072\n\t"
          "v_mfma_f32_16x16x32_bf16 v[220:223], a[140:143], a[28:31], v[220:223]\n\t"
          "v_mfma_f32_16x16x32_bf16 v[216:219], a[140:143], a[24:27], v[216:219]\n\t"
          "v_mfma_f32_16x16x32_bf16 v[216:219], a[136:139], a[8:11], v[216:219]\n\t"
          "v_mfma_f32_16x16x32_bf16 v[212:215], a[136:139], a[4:7], v[212:215]\n\t"
          "ds_read_b128 a[156:159], v17 offset:11264\n\t"
          "v_mfma_f32_16x16x32_bf16 v[212:215], a[140:143], a[20:23], v[212:215]\n\t"
          "v_mfma_f32_16x16x32_bf16 v[208:211], a[140:143], a[16:19], v[208:211]\n\t"
          "v_mfma_f32_16x16x32_bf16 v[208:211], a[136:139], a[0:3], v[208:211]\n\t"
          "s_waitcnt lgkmcnt(2)\n\t"
          "v_mfma_f32_16x16x32_bf16 v[224:227], a[144:147], a[0:3], v[224:227]\n\t"
          "ds_read_b128 a[128:131], v16 offset:0\n\t"
          "buffer_load_dwordx4 v[100:103], %[vx], %[ws], s56 offen offset:1024 sc1\n\t"
          "v_mfma_f32_16x16x32_bf16 v[224:227], a[148:151], a[16:19], v[224:227]\n\t"
          "v_mfma_f32_16x16x32_bf16 v[228:231], a[148:151], a[20:23], v[228:231]\n\t"
          "v_mfma_f32_16x16x32_bf16 v[228:231], a[144:147], a[4:7], v[228:231]\n\t"
          "v_mfma_f32_16x16x32_bf16 v[232:235], a[144:147], a[8:11], v[232:235]\n\t"
          "ds_read_b128 a[132:135], v16 offset:8192\n\t"
          "v_mfma_f32_16x16x32_bf16 v[232:235], a[148:151], a[24:27], v[232:235]\n\t"
          "v_mfma_f32_16x16x32_bf16 v[236:239], a[148:151], a[28:31], v[236:239]\n\t"
          "v_mfma_f32_16x16x32_bf16 v[236:239], a[144:147], a[12:15], v[236:239]\n\t"
          "s_waitcnt lgkmcnt(2)\n\t"
          "v_mfma_f32_16x16x32_bf16 v[252:255], a[152:155], a[12:15], v[252:255]\n\t"
          "ds_read_b128 a[136:139], v16 offset:1024\n\t"
          "v_mfma_f32_16x16x32_bf16 v[252:255], a[156:159], a[28:31], v[252:255]\n\t"
          "v_mfma_f32_16x16x32_bf16 v[248:251], a[156:159], a[24:27], v[248:251]\n\t"
          "v_mfma_f32_16x16x32_bf16 v[248:251], a[152:155], a[8:11], v[248:251]\n\t"
          "v_mfma_f32_16x16x32_bf16 v[244:247], a[152:155], a[4:7], v[244:247]\n\t"
          "ds_read_b128 a[140:143], v16 offset:9216\n\t"
          "v_mfma_f32_16x16x32_bf16 v[244:247], a[156:159], a[20:23], v[244:247]\n\t"
          "v_mfma_f32_16x16x32_bf16 v[240:243], a[156:159], a[16:19], v[240:243]\n\t"
          "v_mfma_f32_16x16x32_bf16 v[240:243], a[152:155], a[0:3], v[240:243]\n\t"
          "buffer_load_dwordx4 v[104:107], %[vx], %[ws], s57 offen offset:0 sc1\n\t"
          "s_waitcnt lgkmcnt(2)\n\t"
          "v_mfma_f32_16x16x32_bf16 v[128:131], a[128:131], a[0:3], v[128:131]\n\t"
          "ds_read_b128 a[144:147], v16 offset:2048\n\t"
          "v_mfma_f32_16x16x32_bf16 v[128:131], a[132:135], a[16:19], v[128:131]\n\t"
          "v_mfma_f32_16x16x32_bf16 v[132:135], a[132:135], a[20:23], v[132:135]\n\t"
          "v_mfma_f32_16x16x32_bf16 v[132:135], a[128:131], a[4:7], v[132:135]\n\t"
          "v_mfma_f32_16x16x32_bf16 v[136:139], a[128:131], a[8:11], v[136:139]\n\t"
          "ds_read_b128 a[148:151], v16 offset:10240\n\t"
          "v_mfma_f32_16x16x32_bf16 v[136:139], a[132:135], a[24:27], v[136:139]\n\t"
          "v_mfma_f32_16x16x32_bf16 v[140:143], a[132:135], a[28:31], v[140:143]\n\t"
          "v_mfma_f32_16x16x32_bf16 v[140:143], a[128:131], a[12:15], v[140:143]\n\t"
          "s_waitcnt lgkmcnt(2)\n\t"
          "v_mfma_f32_16x16x32_bf16 v[156:159], a[136:139], a[12:15], v[156:159]\n\t"
          "ds_read_b128 a[152:155], v16 offset:3072\n\t"
          "v_mfma_f32_16x16x32_bf16 v[156:159], a[140:143], a[28:31], v[156:159]\n\t"
          "v_mfma_f32_16x16x32_bf16 v[152:155], a[140:143], a[24:27], v[152:155]\n\t"
          "v_mfma_f32_16x16x32_bf16 v[152:155], a[136:139], a[8:11], v[152:155]\n\t"
          "v_mfma_f32_16x16x32_bf16 v[148:151], a[136:139], a[4:7], v[148:151]\n\t"
          "ds_read_b128 a[156:159], v16 offset:11264\n\t"
          "v_mfma_f32_16x16x32_bf16 v[148:151], a[140:143], a[20:23], v[148:151]\n\t"
          "buffer_load_dwordx4 v[108:111], %[vx], %[ws], s57 offen offset:1024 sc1\n\t"
          "v_mfma_f32_16x16x32_bf16 v[144:147], a[140:143], a[16:19], v[144:147]\n\t"
          "v_mfma_f32_16x16x32_bf16 v[144:147], a[136:139], a[0:3], v[144:147]\n\t"
          "s_waitcnt vmcnt(5)\n\t"
          "s_barrier\n\t"
          "s_waitcnt lgkmcnt(2)\n\t"
          "v_mfma_f32_16x16x32_bf16 v[160:163], a[144:147], a[0:3], v[160:163]\n\t"
          "ds_read_b128 a[128:131], v17 offset:32768\n\t"
          "v_mfma_f32_16x16x32_bf16 v[160:163], a[148:151], a[16:19], v[160:163]\n\t"
          "v_mfma_f32_16x16x32_bf16 v[164:167], a[148:151], a[20:23], v[164:167]\n\t"
          "v_mfma_f32_16x16x32_bf16 v[164:167], a[144:147], a[4:7], v[164:167]\n\t"
          "v_mfma_f32_16x16x32_bf16 v[168:171], a[144:147], a[8:11], v[168:171]\n\t"
          "ds_read_b128 a[132:135], v17 offset:40960\n\t"
          "v_mfma_f32_16x16x32_bf16 v[168:171], a[148:151], a[24:27], v[168:171]\n\t"
          "v_mfma_f32_16x16x32_bf16 v[172:175], a[148:151], a[28:31], v[172:175]\n\t"
          "v_mfma_f32_16x16x32_bf16 v[172:175], a[144:147], a[12:15], v[172:175]\n\t"
          "s_waitcnt lgkmcnt(2)\n\t"
          "v_mfma_f32_16x16x32_bf16 v[188:191], a[152:155], a[12:15], v[188:191]\n\t"
          "ds_read_b128 a[136:139], v17 offset:33792\n\t"
          "v_mfma_f32_16x16x32_bf16 v[188:191], a[156:159], a[28:31], v[188:191]\n\t"
          "v_mfma_f32_16x16x32_bf16 v[184:187], a[156:159], a[24:27], v[184:187]\n\t"
          "v_mfma_f32_16x16x32_bf16 v[184:187], a[152:155], a[8:11], v[184:187]\n\t"
          "v_mfma_f32_16x16x32_bf16 v[180:183], a[152:155], a[4:7], v[180:183]\n\t"
          "ds_read_b128 a[140:143], v17 offset:41984\n\t"
          "v_mfma_f32_16x16x32_bf16 v[180:183], a[156:159], a[20:23], v[180:183]\n\t"
          "v_mfma_f32_16x16x32_bf16 v[176:179], a[156:159], a[16:19], v[176:179]\n\t"
          "v_mfma_f32_16x16x32_bf16 v[176:179], a[152:155], a[0:3], v[176:179]\n\t"
          "s_waitcnt vmcnt(4)\n\t"
          "s_waitcnt lgkmcnt(2)\n\t"
          "v_mfma_f32_16x16x32_bf16 v[192:195], a[128:131], a[32:35], v[192:195]\n\t"
          "ds_read_b128 a[144:147], v17 offset:34816\n\t"
          "v_mfma_f32_16x16x32_bf16 v[192:195], a[132:135], a[48:51], v[192:195]\n\t"
          "v_mfma_f32_16x16x32_bf16 v[196:199], a[132:135], a[52:55], v[196:199]\n\t"
          "v_mfma_f32_16x16x32_bf16 v[196:199], a[128:131], a[36:39], v[196:199]\n\t"
          "v_mfma_f32_16x16x32_bf16 v[200:203], a[128:131], a[40:43], v[200:203]\n\t"
          "ds_read_b128 a[148:151], v17 offset:43008\n\t"
          "v_mfma_f32_16x16x32_bf16 v[200:203], a[132:135], a[56:59], v[200:203]\n\t"
          "v_mfma_f32_16x16x32_bf16 v[204:207], a[132:135], a[60:63], v[204:207]\n\t"
          "v_mfma_f32_16x16x32_bf16 v[204:207], a[128:131], a[44:47], v[204:207]\n\t"
          "s_waitcnt lgkmcnt(2)\n\t"
          "v_mfma_f32_16x16x32_bf16 v[220:223], a[136:139], a[44:47], v[220:223]\n\t"
          "ds_read_b128 a[152:155], v17 offset:35840\n\t"
          "v_mfma_f32_16x16x32_bf16 v[220:223], a[140:143], a[60:63], v[220:223]\n\t"
          "v_mfma_f32_16x16x32_bf16 v[216:219], a[140:143], a[56:59], v[216:219]\n\t"
          "v_mfma_f32_16x16x32_bf16 v[216:219], a[136:139], a[40:43], v[216:219]\n\t"
          "v_mfma_f32_16x16x32_bf16 v[212:215], a[136:139], a[36:39], v[212:215]\n\t"
          "ds_read_b128 a[156:159], v17 offset:44032\n\t"
          "v_mfma_f32_16x16x32_bf16 v[212:215], a[140:143], a[52:55], v[212:215]\n\t"
          "ds_write_b128 v6, v[200:203] offset:8192\n\t"
          "v_mfma_f32_16x16x32_bf16 v[208:211], a[140:143], a[48:51], v[208:211]\n\t"
          "v_mfma_f32_16x16x32_bf16 v[208:211], a[136:139], a[32:35], v[208:211]\n\t"
          "ds_write_b128 v6, v[204:207] offset:9216\n\t"
          "s_waitcnt lgkmcnt(4)\n\t"
          "v_mfma_f32_16x16x32_bf16 v[224:227], a[144:147], a[32:35], v[224:227]\n\t"
          "ds_read_b128 a[128:131], v16 offset:32768\n\t"
          "v_mfma_f32_16x16x32_bf16 v[224:227], a[148:151], a[48:51], v[224:227]\n\t"
          "v_mfma_f32_16x16x32_bf16 v[228:231], a[148:151], a[52:55], v[228:231]\n\t"
          "v_mfma_f32_16x16x32_bf16 v[228:231], a[144:147], a[36:39], v[228:231]\n\t"
          "v_mfma_f32_16x16x32_bf16 v[232:235], a[144:147], a[40:43], v[232:235]\n\t"
          "ds_read_b128 a[132:135], v16 offset:40960\n\t"
          "v_mfma_f32_16x16x32_bf16 v[232:235], a[148:151], a[56:59], v[232:235]\n\t"
          "ds_write_b128 v6, v[216:219] offset:10240\n\t"
          "v_mfma_f32_16x16x32_bf16 v[236:239], a[148:151], a[60:63], v[236:239]\n\t"
          "v_mfma_f32_16x16x32_bf16 v[236:239], a[144:147], a[44:47], v[236:239]\n\t"
          "ds_write_b128 v6, v[220:223] offset:11264\n\t"
          "s_waitcnt lgkmcnt(6)\n\t"
          "v_mfma_f32_16x16x32_bf16 v[252:255], a[152:155], a[44:47], v[252:255]\n\t"
          "ds_read_b128 a[136:139], v16 offset:33792\n\t"
          "v_mfma_f32_16x16x32_bf16 v[252:255], a[156:159], a[60:63], v[252:255]\n\t"
          "v_mfma_f32_16x16x32_bf16 v[248:251], a[156:159], a[56:59], v[248:251]\n\t"
          "v_mfma_f32_16x16x32_bf16 v[248:251], a[152:155], a[40:43], v[248:251]\n\t"
          "v_mfma_f32_16x16x32_bf16 v[244:247], a[152:155], a[36:39], v[244:247]\n\t"
          "ds_read_b128 a[140:143], v16 offset:41984\n\t"
          "v_mfma_f32_16x16x32_bf16 v[244:247], a[156:159], a[52:55], v[244:247]\n\t"
          "ds_write_b128 v6, v[232:235] offset:12288\n\t"
          "v_mfma_f32_16x16x32_bf16 v[240:243], a[156:159], a[48:51], v[240:243]\n\t"
          "v_mfma_f32_16x16x32_bf16 v[240:243], a[152:155], a[32:35], v[240:243]\n\t"
          "ds_write_b128 v6, v[236:239] offset:13312\n\t"
          "s_waitcnt lgkmcnt(6)\n\t"
          "v_mfma_f32_16x16x32_bf16 v[128:131], a[128:131], a[32:35], v[128:131]\n\t"
          "ds_read_b128 a[144:147], v16 offset:34816\n\t"
          "v_mfma_f32_16x16x32_bf16 v[128:131], a[132:135], a[48:51], v[128:131]\n\t"
          "v_mfma_f32_16x16x32_bf16 v[132:135], a[132:135], a[52:55], v[132:135]\n\t"
          "v_mfma_f32_16x16x32_bf16 v[132:135], a[128:131], a[36:39], v[132:135]\n\t"
          "v_mfma_f32_16x16x32_bf16 v[136:139], a[128:131], a[40:43], v[136:139]\n\t"
          "ds_read_b128 a[148:151], v16 offset:43008\n\t"
          "v_mfma_f32_16x16x32_bf16 v[136:139], a[132:135], a[56:59], v[136:139]\n\t"
          "ds_write_b128 v6, v[248:251] offset:14336\n\t"
          "v_mfma_f32_16x16x32_bf16 v[140:143], a[132:135], a[60:63], v[140:143]\n\t"
          "v_mfma_f32_16x16x32_bf16 v[140:143], a[128:131], a[44:47], v[140:143]\n\t"
          "ds_write_b128 v6, v[252:255] offset:15360\n\t"
          "s_waitcnt lgkmcnt(6)\n\t"
          "v_mfma_f32_16x16x32_bf16 v[156:159], a[136:139], a[44:47], v[156:159]\n\t"
          "ds_read_b128 a[152:155], v16 offset:35840\n\t"
          "v_mfma_f32_16x16x32_bf16 v[156:159], a[140:143], a[60:63], v[156:159]\n\t"
          "v_mfma_f32_16x16x32_bf16 v[152:155], a[140:143], a[56:59], v[152:155]\n\t"
          "v_mfma_f32_16x16x32_bf16 v[152:155], a[136:139], a[40:43], v[152:155]\n\t"
          "v_mfma_f32_16x16x32_bf16 v[148:151], a[136:139], a[36:39], v[148:151]\n\t"
          "ds_read_b128 a[156:159], v16 offset:44032\n\t"
          "v_mfma_f32_16x16x32_bf16 v[148:151], a[140:143], a[52:55], v[148:151]\n\t"
          "ds_write_b128 v6, v[136:139] offset:0\n\t"
          "v_mfma_f32_16x16x32_bf16 v[144:147], a[140:143], a[48:51], v[144:147]\n\t"
          "v_mfma_f32_16x16x32_bf16 v[144:147], a[136:139], a[32:35], v[144:147]\n\t"
          "ds_write_b128 v6, v[140:143] offset:1024\n\t"
          "s_waitcnt lgkmcnt(6)\n\t"
          "v_mfma_f32_16x16x32_bf16 v[160:163], a[144:147], a[32:35], v[160:163]\n\t"
          "v_mfma_f32_16x16x32_bf16 v[160:163], a[148:151], a[48:51], v[160:163]\n\t"
          "v_mfma_f32_16x16x32_bf16 v[164:167], a[148:151], a[52:55], v[164:167]\n\t"
          "v_mfma_f32_16x16x32_bf16 v[164:167], a[144:147], a[36:39], v[164:167]\n\t"
          "v_mfma_f32_16x16x32_bf16 v[168:171], a[144:147], a[40:43], v[168:171]\n\t"
          "v_mfma_f32_16x16x32_bf16 v[168:171], a[148:151], a[56:59], v[168:171]\n\t"
          "ds_write_b128 v6, v[152:155] offset:2048\n\t"
          "v_mfma_f32_16x16x32_bf16 v[172:175], a[148:151], a[60:63], v[172:175]\n\t"
          "v_mfma_f32_16x16x32_bf16 v[172:175], a[144:147], a[44:47], v[172:175]\n\t"
          "ds_write_b128 v6, v[156:159] offset:3072\n\t"
          "s_waitcnt lgkmcnt(4)\n\t"
          "v_mfma_f32_16x16x32_bf16 v[188:191], a[152:155], a[44:47], v[188:191]\n\t"
          "v_mfma_f32_16x16x32_bf16 v[188:191], a[156:159], a[60:63], v[188:191]\n\t"
          "v_mfma_f32_16x16x32_bf16 v[184:187], a[156:159], a[56:59], v[184:187]\n\t"
          "v_mfma_f32_16x16x32_bf16 v[184:187], a[152:155], a[40:43], v[184:187]\n\t"
          "v_mfma_f32_16x16x32_bf16 v[180:183], a[152:155], a[36:39], v[180:183]\n\t"
          "v_mfma_f32_16x16x32_bf16 v[180:183], a[156:159], a[52:55], v[180:183]\n\t"
          "ds_write_b128 v6, v[168:171] offset:4096\n\t"
          "v_mfma_f32_16x16x32_bf16 v[176:179], a[156:159], a[48:51], v[176:179]\n\t"
          "v_mfma_f32_16x16x32_bf16 v[176:179], a[152:155], a[32:35], v[176:179]\n\t"
          "ds_write_b128 v6, v[172:175] offset:5120\n\t"
          "s_nop 7\n\t"
          "ds_write_b128 v6, v[184:187] offset:6144\n\t"
          "ds_write_b128 v6, v[188:191] offset:7168\n\t"
          "s_waitcnt vmcnt(0) lgkmcnt(0)\n\t"
          "s_barrier\n\t"
          "ds_read_b128 v[32:35], v7 offset:8192\n\t"
          "ds_read_b128 v[36:39], v7 offset:9216\n\t"
          "ds_read_b128 v[40:43], v7 offset:10240\n\t"
          "ds_read_b128 v[44:47], v7 offset:11264\n\t"
          "ds_read_b128 v[48:51], v7 offset:12288\n\t"
          "ds_read_b128 v[52:55], v7 offset:13312\n\t"
          "ds_read_b128 v[56:59], v7 offset:14336\n\t"
          "ds_read_b128 v[60:63], v7 offset:15360\n\t"
          "ds_read_b128 v[64:67], v7 offset:0\n\t"
          "ds_read_b128 v[68:71], v7 offset:1024\n\t"
          "ds_read_b128 v[72:75], v7 offset:2048\n\t"
          "ds_read_b128 v[76:79], v7 offset:3072\n\t"
          "ds_read_b128 v[80:83], v7 offset:4096\n\t"
          "ds_read_b128 v[84:87], v7 offset:5120\n\t"
          "ds_read_b128 v[88:91], v7 offset:6144\n\t"
          "s_waitcnt lgkmcnt(14)\n\t"
          "ds_read_b128 v[92:95], v7 offset:7168\n\t"
          "s_cmp_lg_u32 %[layer], 0\n\t"
          "s_cbranch_scc1 20f\n\t"
          "s_waitcnt lgkmcnt(14)\n\t"
          "v_pk_add_f32 v[192:193], v[192:193], v[32:33]\n\t"
          "v_pk_add_f32 v[194:195], v[194:195], v[34:35]\n\t"
          "v_pk_add_f32 v[196:197], v[196:197], v[36:37]\n\t"
          "v_pk_add_f32 v[198:199], v[198:199], v[38:39]\n\t"
          "s_waitcnt lgkmcnt(12)\n\t"
          "v_pk_add_f32 v[208:209], v[208:209], v[40:41]\n\t"
          "v_pk_add_f32 v[210:211], v[210:211], v[42:43]\n\t"
          "v_pk_add_f32 v[212:213], v[212:213], v[44:45]\n\t"
          "v_pk_add_f32 v[214:215], v[214:215], v[46:47]\n\t"
          "v_cvt_pk_bf16_f32 v96, v192, v193\n\t"
          "v_cvt_pk_bf16_f32 v97, v194, v195\n\t"
          "v_cvt_pk_bf16_f32 v98, v208, v209\n\t"
          "v_cvt_pk_bf16_f32 v99, v210, v211\n\t"
          "buffer_store_dwordx4 v[96:99], %[vx], %[ws], s54 offen offset:0 sc1\n\t"
          "v_cvt_pk_bf16_f32 v100, v196, v197\n\t"
          "v_cvt_pk_bf16_f32 v101, v198, v199\n\t"
          "v_cvt_pk_bf16_f32 v102, v212, v213\n\t"
          "v_cvt_pk_bf16_f32 v103, v214, v215\n\t"
          "buffer_store_dwordx4 v[100:103], %[vx], %[ws], s54 offen offset:1024 sc1\n\t"
          "s_waitcnt lgkmcnt(10)\n\t"
          "v_pk_add_f32 v[224:225], v[224:225], v[48:49]\n\t"
          "v_pk_add_f32 v[226:227], v[226:227], v[50:51]\n\t"
          "v_pk_add_f32 v[228:229], v[228:229], v[52:53]\n\t"
          "v_pk_add_f32 v[230:231], v[230:231], v[54:55]\n\t"
          "s_waitcnt lgkmcnt(8)\n\t"
          "v_pk_add_f32 v[240:241], v[240:241], v[56:57]\n\t"
          "v_pk_add_f32 v[242:243], v[242:243], v[58:59]\n\t"
          "v_pk_add_f32 v[244:245], v[244:245], v[60:61]\n\t"
          "v_pk_add_f32 v[246:247], v[246:247], v[62:63]\n\t"
          "v_cvt_pk_bf16_f32 v104, v224, v225\n\t"
          "v_cvt_pk_bf16_f32 v105, v226, v227\n\t"
          "v_cvt_pk_bf16_f32 v106, v240, v241\n\t"
          "v_cvt_pk_bf16_f32 v107, v242, v243\n\t"
          "buffer_store_dwordx4 v[104:107], %[vx], %[ws], s55 offen offset:0 sc1\n\t"
          "v_cvt_pk_bf16_f32 v108, v228, v229\n\t"
          "v_cvt_pk_bf16_f32 v109, v230, v231\n\t"
          "v_cvt_pk_bf16_f32 v110, v244, v245\n\t"
          "v_cvt_pk_bf16_f32 v111, v246, v247\n\t"
          "buffer_store_dwordx4 v[108:111], %[vx], %[ws], s55 offen offset:1024 sc1\n\t"
          "s_waitcnt lgkmcnt(6)\n\t"
          "v_pk_add_f32 v[128:129], v[128:129], v[64:65]\n\t"
          "v_pk_add_f32 v[130:131], v[130:131], v[66:67]\n\t"
          "v_pk_add_f32 v[132:133], v[132:133], v[68:69]\n\t"
          "v_pk_add_f32 v[134:135], v[134:135], v[70:71]\n\t"
          "s_waitcnt lgkmcnt(4)\n\t"
          "v_pk_add_f32 v[144:145], v[144:145], v[72:73]\n\t"
          "v_pk_add_f32 v[146:147], v[146:147], v[74:75]\n\t"
          "v_pk_add_f32 v[148:149], v[148:149], v[76:77]\n\t"
          "v_pk_add_f32 v[150:151], v[150:151], v[78:79]\n\t"
          "v_cvt_pk_bf16_f32 v112, v128, v129\n\t"
          "v_cvt_pk_bf16_f32 v113, v130, v131\n\t"
          "v_cvt_pk_bf16_f32 v114, v144, v145\n\t"
          "v_cvt_pk_bf16_f32 v115, v146, v147\n\t"
          "buffer_store_dwordx4 v[112:115], %[vx], %[ws], s52 offen offset:0 sc1\n\t"
          "v_cvt_pk_bf16_f32 v116, v132, v133\n\t"
          "v_cvt_pk_bf16_f32 v117, v134, v135\n\t"
          "v_cvt_pk_bf16_f32 v118, v148, v149\n\t"
          "v_cvt_pk_bf16_f32 v119, v150, v151\n\t"
          "buffer_store_dwordx4 v[116:119], %[vx], %[ws], s52 offen offset:1024 sc1\n\t"
          "s_waitcnt lgkmcnt(2)\n\t"
          "v_pk_add_f32 v[160:161], v[160:161], v[80:81]\n\t"
          "v_pk_add_f32 v[162:163], v[162:163], v[82:83]\n\t"
          "v_pk_add_f32 v[164:165], v[164:165], v[84:85]\n\t"
          "v_pk_add_f32 v[166:167], v[166:167], v[86:87]\n\t"
          "s_waitcnt lgkmcnt(0)\n\t"
          "v_pk_add_f32 v[176:177], v[176:177], v[88:89]\n\t"
          "v_pk_add_f32 v[178:179], v[178:179], v[90:91]\n\t"
          "v_pk_add_f32 v[180:181], v[180:181], v[92:93]\n\t"
          "v_pk_add_f32 v[182:183], v[182:183], v[94:95]\n\t"
          "v_cvt_pk_bf16_f32 v120, v160, v161\n\t"
          "v_cvt_pk_bf16_f32 v121, v162, v163\n\t"
          "v_cvt_pk_bf16_f32 v122, v176, v177\n\t"
          "v_cvt_pk_bf16_f32 v123, v178, v179\n\t"
          "buffer_store_dwordx4 v[120:123], %[vx], %[ws], s53 offen offset:0 sc1\n\t"
          "v_cvt_pk_bf16_f32 v124, v164, v165\n\t"
          "v_cvt_pk_bf16_f32 v125, v166, v167\n\t"
          "v_cvt_pk_bf16_f32 v126, v180, v181\n\t"
          "v_cvt_pk_bf16_f32 v127, v182, v183\n\t"
          "buffer_store_dwordx4 v[124:127], %[vx], %[ws], s53 offen offset:1024 sc1\n\t"
          "v_mov_b64 v[24:25], %[tag]\n\t"
          "s_waitcnt vmcnt(0)\n\t"
          "s_mov_b64 exec, 1\n\t"
          "buffer_store_dwordx2 v[24:25], off, %[ws], %[fs2] sc1\n\t"
          "s_mov_b64 exec, -1\n\t"
          "s_branch 29f\n\t"
          "20:\n\t"
          "s_add_u32 s56, %[xs], 4096\n\t"
          "s_xor_b32 s50, %[xs], 32768\n\t"
          "s_add_u32 s51, s50, 4096\n\t"
          "s_xor_b32 s57, %[fs2], 512\n\t"
          "s_waitcnt lgkmcnt(14)\n\t"
          "v_pk_add_f32 v[192:193], v[192:193], v[32:33]\n\t"
          "v_pk_add_f32 v[194:195], v[194:195], v[34:35]\n\t"
          "v_pk_add_f32 v[196:197], v[196:197], v[36:37]\n\t"
          "v_pk_add_f32 v[198:199], v[198:199], v[38:39]\n\t"
          "buffer_store_dwordx4 v[192:195], %[vx], %[ws], %[xs] offen offset:0 sc1\n\t"
          "buffer_store_dwordx4 v[196:199], %[vx], %[ws], %[xs] offen offset:1024 sc1\n\t"
          "s_waitcnt lgkmcnt(12)\n\t"
          "v_pk_add_f32 v[208:209], v[208:209], v[40:41]\n\t"
          "v_pk_add_f32 v[210:211], v[210:211], v[42:43]\n\t"
          "v_pk_add_f32 v[212:213], v[212:213], v[44:45]\n\t"
          "v_pk_add_f32 v[214:215], v[214:215], v[46:47]\n\t"
          "buffer_store_dwordx4 v[208:211], %[vx], %[ws], %[xs] offen offset:2048 sc1\n\t"
          "buffer_store_dwordx4 v[212:215], %[vx], %[ws], %[xs] offen offset:3072 sc1\n\t"
          "s_waitcnt lgkmcnt(10)\n\t"
          "v_pk_add_f32 v[224:225], v[224:225], v[48:49]\n\t"
          "v_pk_add_f32 v[226:227], v[226:227], v[50:51]\n\t"
          "v_pk_add_f32 v[228:229], v[228:229], v[52:53]\n\t"
          "v_pk_add_f32 v[230:231], v[230:231], v[54:55]\n\t"
          "buffer_store_dwordx4 v[224:227], %[vx], %[ws], s56 offen offset:0 sc1\n\t"
          "buffer_store_dwordx4 v[228:231], %[vx], %[ws], s56 offen offset:1024 sc1\n\t"
          "s_waitcnt lgkmcnt(8)\n\t"
          "v_pk_add_f32 v[240:241], v[240:241], v[56:57]\n\t"
          "v_pk_add_f32 v[242:243], v[242:243], v[58:59]\n\t"
          "v_pk_add_f32 v[244:245], v[244:245], v[60:61]\n\t"
          "v_pk_add_f32 v[246:247], v[246:247], v[62:63]\n\t"
          "buffer_store_dwordx4 v[240:243], %[vx], %[ws], s56 offen offset:2048 sc1\n\t"
          "buffer_store_dwordx4 v[244:247], %[vx], %[ws], s56 offen offset:3072 sc1\n\t"
          "s_waitcnt lgkmcnt(6)\n\t"
          "v_pk_add_f32 v[128:129], v[128:129], v[64:65]\n\t"
          "v_pk_add_f32 v[130:131], v[130:131], v[66:67]\n\t"
          "v_pk_add_f32 v[132:133], v[132:133], v[68:69]\n\t"
          "v_pk_add_f32 v[134:135], v[134:135], v[70:71]\n\t"
          "s_waitcnt lgkmcnt(4)\n\t"
          "v_pk_add_f32 v[144:145], v[144:145], v[72:73]\n\t"
          "v_pk_add_f32 v[146:147], v[146:147], v[74:75]\n\t"
          "v_pk_add_f32 v[148:149], v[148:149], v[76:77]\n\t"
          "v_pk_add_f32 v[150:151], v[150:151], v[78:79]\n\t"
          "s_waitcnt lgkmcnt(2)\n\t"
          "v_pk_add_f32 v[160:161], v[160:161], v[80:81]\n\t"
          "v_pk_add_f32 v[162:163], v[162:163], v[82:83]\n\t"
          "v_pk_add_f32 v[164:165], v[164:165], v[84:85]\n\t"
          "v_pk_add_f32 v[166:167], v[166:167], v[86:87]\n\t"
          "s_waitcnt lgkmcnt(0)\n\t"
          "v_pk_add_f32 v[176:177], v[176:177], v[88:89]\n\t"
          "v_pk_add_f32 v[178:179], v[178:179], v[90:91]\n\t"
          "v_pk_add_f32 v[180:181], v[180:181], v[92:93]\n\t"
          "v_pk_add_f32 v[182:183], v[182:183], v[94:95]\n\t"
          "s_mov_b32 s47, 0xffff0000\n\t"
          "v_lshlrev_b32 v26, 16, v96\n\t"
          "v_and_b32 v27, s47, v96\n\t"
          "v_pk_add_f32 v[128:129], v[128:129], v[26:27]\n\t"
          "v_lshlrev_b32 v26, 16, v97\n\t"
          "v_and_b32 v27, s47, v97\n\t"
          "v_pk_add_f32 v[130:131], v[130:131], v[26:27]\n\t"
          "v_lshlrev_b32 v26, 16, v98\n\t"
          "v_and_b32 v27, s47, v98\n\t"
          "v_pk_add_f32 v[144:145], v[144:145], v[26:27]\n\t"
          "v_lshlrev_b32 v26, 16, v99\n\t"
          "v_and_b32 v27, s47, v99\n\t"
          "v_pk_add_f32 v[146:147], v[146:147], v[26:27]\n\t"
          "v_lshlrev_b32 v26, 16, v100\n\t"
          "v_and_b32 v27, s47, v100\n\t"
          "v_pk_add_f32 v[132:133], v[132:133], v[26:27]\n\t"
          "v_lshlrev_b32 v26, 16, v101\n\t"
          "v_and_b32 v27, s47, v101\n\t"
          "v_pk_add_f32 v[134:135], v[134:135], v[26:27]\n\t"
          "v_lshlrev_b32 v26, 16, v102\n\t"
          "v_and_b32 v27, s47, v102\n\t"
          "v_pk_add_f32 v[148:149], v[148:149], v[26:27]\n\t"
          "v_lshlrev_b32 v26, 16, v103\n\t"
          "v_and_b32 v27, s47, v103\n\t"
          "v_pk_add_f32 v[150:151], v[150:151], v[26:27]\n\t"
          "v_lshlrev_b32 v26, 16, v104\n\t"
          "v_and_b32 v27, s47, v104\n\t"
          "v_pk_add_f32 v[160:161], v[160:161], v[26:27]\n\t"
          "v_lshlrev_b32 v26, 16, v105\n\t"
          "v_and_b32 v27, s47, v105\n\t"
          "v_pk_add_f32 v[162:163], v[162:163], v[26:27]\n\t"
          "v_lshlrev_b32 v26, 16, v106\n\t"
          "v_and_b32 v27, s47, v106\n\t"
          "v_pk_add_f32 v[176:177], v[176:177], v[26:27]\n\t"
          "v_lshlrev_b32 v26, 16, v107\n\t"
          "v_and_b32 v27, s47, v107\n\t"
          "v_pk_add_f32 v[178:179], v[178:179], v[26:27]\n\t"
          "v_lshlrev_b32 v26, 16, v108\n\t"
          "v_and_b32 v27, s47, v108\n\t"
          "v_pk_add_f32 v[164:165], v[164:165], v[26:27]\n\t"
          "v_lshlrev_b32 v26, 16, v109\n\t"
          "v_and_b32 v27, s47, v109\n\t"
          "v_pk_add_f32 v[166:167], v[166:167], v[26:27]\n\t"
          "v_lshlrev_b32 v26, 16, v110\n\t"
          "v_and_b32 v27, s47, v110\n\t"
          "v_pk_add_f32 v[180:181], v[180:181], v[26:27]\n\t"
          "v_lshlrev_b32 v26, 16, v111\n\t"
          "v_and_b32 v27, s47, v111\n\t"
          "v_pk_add_f32 v[182:183], v[182:183], v[26:27]\n\t"
          "v_mov_b64 v[24:25], %[tag]\n\t"
          "s_waitcnt vmcnt(0)\n\t"
          "s_mov_b64 exec, 1\n\t"
          "buffer_store_dwordx2 v[24:25], off, %[ws], %[fs2] sc1\n\t"
          "s_mov_b64 exec, -1\n\t"
          "s_mov_b32 s58, 262144\n\t"
          "22:\n\t"
          "buffer_load_dwordx2 v[24:25], off, %[ws], s57 sc1\n\t"
          "s_waitcnt vmcnt(0)\n\t"
          "v_cmp_ne_u64 vcc, %[tag], v[24:25]\n\t"
          "s_nop 4\n\t"
          "s_cbranch_vccz 23f\n\t"
          "s_sub_u32 s58, s58, 1\n\t"
          "s_cmp_lg_u32 s58, 0\n\t"
          "s_cbranch_scc1 22b\n\t"
          "23:\n\t"
          "buffer_load_dwordx4 v[32:35], %[vx], %[ws], s50 offen offset:0 sc1\n\t"
          "buffer_load_dwordx4 v[36:39], %[vx], %[ws], s50 offen offset:1024 sc1\n\t"
          "buffer_load_dwordx4 v[40:43], %[vx], %[ws], s50 offen offset:2048 sc1\n\t"
          "buffer_load_dwordx4 v[44:47], %[vx], %[ws], s50 offen offset:3072 sc1\n\t"
          "buffer_load_dwordx4 v[48:51], %[vx], %[ws], s51 offen offset:0 sc1\n\t"
          "buffer_load_dwordx4 v[52:55], %[vx], %[ws], s51 offen offset:1024 sc1\n\t"
          "buffer_load_dwordx4 v[56:59], %[vx], %[ws], s51 offen offset:2048 sc1\n\t"
          "buffer_load_dwordx4 v[60:63], %[vx], %[ws], s51 offen offset:3072 sc1\n\t"
          "v_add_u32 v26, 32768, %[vo]\n\t"
          "s_waitcnt vmcnt(4)\n\t"
          "v_pk_add_f32 v[128:129], v[128:129], v[32:33]\n\t"
          "v_pk_add_f32 v[130:131], v[130:131], v[34:35]\n\t"
          "v_pk_add_f32 v[132:133], v[132:133], v[36:37]\n\t"
          "v_pk_add_f32 v[134:135], v[134:135], v[38:39]\n\t"
          "v_pk_add_f32 v[144:145], v[144:145], v[40:41]\n\t"
          "v_pk_add_f32 v[146:147], v[146:147], v[42:43]\n\t"
          "v_pk_add_f32 v[148:149], v[148:149], v[44:45]\n\t"
          "v_pk_add_f32 v[150:151], v[150:151], v[46:47]\n\t"
          "v_cvt_pk_bf16_f32 v96, v128, v129\n\t"
          "v_cvt_pk_bf16_f32 v97, v130, v131\n\t"
          "v_cvt_pk_bf16_f32 v98, v144, v145\n\t"
          "v_cvt_pk_bf16_f32 v99, v146, v147\n\t"
          "v_cvt_pk_bf16_f32 v100, v132, v133\n\t"
          "v_cvt_pk_bf16_f32 v101, v134, v135\n\t"
          "v_cvt_pk_bf16_f32 v102, v148, v149\n\t"
          "v_cvt_pk_bf16_f32 v103, v150, v151\n\t"
          "s_nop 1\n\t"
          "v_permlane32_swap_b32 v96, v98\n\t"
          "v_permlane32_swap_b32 v97, v99\n\t"
          "v_permlane32_swap_b32 v100, v102\n\t"
          "v_permlane32_swap_b32 v101, v103\n\t"
          "s_nop 1\n\t"
          "v_permlane16_swap_b32 v96, v98\n\t"
          "v_permlane16_swap_b32 v97, v99\n\t"
          "v_permlane16_swap_b32 v100, v102\n\t"
          "v_permlane16_swap_b32 v101, v103\n\t"
          "buffer_store_dwordx4 v[96:99], %[vo], %[srdO], 0 offen offset:0 sc1\n\t"
          "buffer_store_dwordx4 v[100:103], v26, %[srdO], 0 offen offset:0 sc1\n\t"
          "s_waitcnt vmcnt(2)\n\t"
          "v_pk_add_f32 v[160:161], v[160:161], v[48:49]\n\t"
          "v_pk_add_f32 v[162:163], v[162:163], v[50:51]\n\t"
          "v_pk_add_f32 v[164:165], v[164:165], v[52:53]\n\t"
          "v_pk_add_f32 v[166:167], v[166:167], v[54:55]\n\t"
          "v_pk_add_f32 v[176:177], v[176:177], v[56:57]\n\t"
          "v_pk_add_f32 v[178:179], v[178:179], v[58:59]\n\t"
          "v_pk_add_f32 v[180:181], v[180:181], v[60:61]\n\t"
          "v_pk_add_f32 v[182:183], v[182:183], v[62:63]\n\t"
          "v_cvt_pk_bf16_f32 v104, v160, v161\n\t"
          "v_cvt_pk_bf16_f32 v105, v162, v163\n\t"
          "v_cvt_pk_bf16_f32 v106, v176, v177\n\t"
          "v_cvt_pk_bf16_f32 v107, v178, v179\n\t"
          "v_cvt_pk_bf16_f32 v108, v164, v165\n\t"
          "v_cvt_pk_bf16_f32 v109, v166, v167\n\t"
          "v_cvt_pk_bf16_f32 v110, v180, v181\n\t"
          "v_cvt_pk_bf16_f32 v111, v182, v183\n\t"
          "s_nop 1\n\t"
          "v_permlane32_swap_b32 v104, v106\n\t"
          "v_permlane32_swap_b32 v105, v107\n\t"
          "v_permlane32_swap_b32 v108, v110\n\t"
          "v_permlane32_swap_b32 v109, v111\n\t"
          "s_nop 1\n\t"
          "v_permlane16_swap_b32 v104, v106\n\t"
          "v_permlane16_swap_b32 v105, v107\n\t"
          "v_permlane16_swap_b32 v108, v110\n\t"
          "v_permlane16_swap_b32 v109, v111\n\t"
          "buffer_store_dwordx4 v[104:107], %[vo], %[srdO], 0 offen offset:64 sc1\n\t"
          "buffer_store_dwordx4 v[108:111], v26, %[srdO], 0 offen offset:64 sc1\n\t"
          "29:\n\t"
          "s_nop 1\n\t"
          :
          : [vx] "v"(vx), [vp1] "v"(vp1), [vp2] "v"(vp2), [vo] "v"(vo), [ws] "s"(ws), [srdW1] "s"(srdW1), [r2] "s"(r2), [srdO] "s"(srdO), [tag] "s"(tag), [w] "s"(w), [w0] "s"(w0), [rot] "s"(rot), [xo] "s"(xo), [hb] "s"(hb),
            [fs1] "s"(fs1), [w2] "s"(w2), [x2] "s"(x2), [e0] "s"(e0), [fs2] "s"(fs2), [xs] "s"(xs), [layer] "s"(layer)          : "memory", "scc", "vcc", "s44","s45","s46","s47","s48","s49","s50","s51","s52","s53","s54","s55","s56","s57","s58","s59","s60","s61", "v6","v7","v8","v9","v10","v11","v12","v13","v14","v15","v16","v17","v18","v19","v20","v21","v22","v23","v24","v25","v26","v27","v28","v29","v30","v31","v32","v33","v34","v35","v36","v37","v38","v39","v40","v41","v42","v43","v44","v45","v46","v47","v48","v49","v50","v51","v52","v53","v54","v55","v56","v57","v58","v59","v60","v61","v62","v63","v64","v65","v66","v67","v68","v69","v70","v71","v72","v73","v74","v75","v76","v77","v78","v79","v80","v81","v82","v83","v84","v85","v86","v87","v88","v89","v90","v91","v92","v93","v94","v95","v96","v97","v98","v99","v100","v101","v102","v103","v104","v105","v106","v107","v108","v109","v110","v111","v112","v113","v114","v115","v116","v117","v118","v119","v120","v121","v122","v123","v124","v125","v126","v127","v128","v129","v130","v131","v132","v133","v134","v135","v136","v137","v138","v139","v140","v141","v142","v143","v144","v145","v146","v147","v148","v149","v150","v151","v152","v153","v154","v155","v156","v157","v158","v159","v160","v161","v162","v163","v164","v165","v166","v167","v168","v169","v170","v171","v172","v173","v174","v175","v176","v177","v178","v179","v180","v181","v182","v183","v184","v185","v186","v187","v188","v189","v190","v191","v192","v193","v194","v195","v196","v197","v198","v199","v200","v201","v202","v203","v204","v205","v206","v207","v208","v209","v210","v211","v212","v213","v214","v215","v216","v217","v218","v219","v220","v221","v222","v223","v224","v225","v226","v227","v228","v229","v230","v231","v232","v233","v234","v235","v236","v237","v238","v239","v240","v241","v242","v243","v244","v245","v246","v247","v248","v249","v250","v251","v252","v253","v254","v255", "a0","a1","a2","a3","a4","a5","a6","a7","a8","a9","a10","a11","a12","a13","a14","a15","a16","a17","a18","a19","a20","a21","a22","a23","a24","a25","a26","a27","a28","a29","a30","a31","a32","a33","a34","a35","a36","a37","a38","a39","a40","a41","a42","a43","a44","a45","a46","a47","a48","a49","a50","a51","a52","a53","a54","a55","a56","a57","a58","a59","a60","a61","a62","a63","a64","a65","a66","a67","a68","a69","a70","a71","a72","a73","a74","a75","a76","a77","a78","a79","a80","a81","a82","a83","a84","a85","a86","a87","a88","a89","a90","a91","a92","a93","a94","a95","a96","a97","a98","a99","a100","a101","a102","a103","a104","a105","a106","a107","a108","a109","a110","a111","a112","a113","a114","a115","a116","a117","a118","a119","a120","a121","a122","a123","a124","a125","a126","a127","a128","a129","a130","a131","a132","a133","a134","a135","a136","a137","a138","a139","a140","a141","a142","a143","a144","a145","a146","a147","a148","a149","a150","a151","a152","a153","a154","a155","a156","a157","a158","a159");
    }
    if (layer == 0) {
      const int lane = lane_id();
      const int r8 = lane & 7, lq = (lane >> 3) & 3, kk = lane >> 5, lr = (blk & 1) * 8 + r8;
      const unsigned fb = ((m16 >> 2) * 128 + (m16 & 3)) * 1024 + (8 * w + kk) * 4096, fo = fb + (lq * 16 + lr) * 16;
      u32x2 ya[4], yb[4]; u32x4 x1[4], p0[4], p1[4];
#pragma unroll
      for (int it = 0; it < 4; it++) {
        const unsigned yo = fb + it * 8192 + lq * 256 + (blk & 1) * 64 + (r8 & 3) * 16 + (r8 >> 2) * 8;
        ya[it] = ld8(ws, yo, WS_Y); yb[it] = ld8(ws, yo + 128, WS_Y);
        x1[it] = *(LAS const u32x4*)(lds + LDS_X1 + (it * 256 + w * 64 + lane) * 16);
      }
      unsigned vw[7];
      w1_offsets(vw, lane, rot);
      w1_prefetch<0>(srdW1, vw, w, w0);
      const float sh = *(LAS const float*)(lds + LDS_RED + 512 + r8 * 4);
      f32x2 xs[16], y[16];
#pragma unroll
      for (int e = 0; e < 16; e++) { xs[e] = up2(x1[e >> 2][e & 3]) - sh; y[e] = up2((e & 2) ? yb[e >> 2][e & 1] : ya[e >> 2][e & 1]); }
      PIN16(xs); PIN16(y);
      wait_flags(ws, F_DN + ((((m16 >> 3) * 8 + 2 * w + (lane & 1)) * 2 + ((lane >> 1) & 1)) * 4 + ((m16 >> 2) & 1) + (m16 & 2)) * 128, tag);
#pragma unroll
      for (int it = 0; it < 4; it++) { p0[it] = ld16(ws, fo + it * 8192, WS_P); p1[it] = ld16(ws, fo + it * 8192, WS_P + 4 * MB); }
#pragma unroll
      for (int e = 0; e < 16; e++) xs[e] += up2(p0[e >> 2][e & 3]) + up2(p1[e >> 2][e & 3]);
      row_partials(xs, (LAS f32x2*)(lds + LDS_RED) + w * 8, lane);
      __builtin_amdgcn_sched_barrier(0);
      w1_prefetch<1>(srdW1, vw, w, w0);
      f32x2 xy[16];
#pragma unroll
      for (int e = 0; e < 16; e++) xy[e] = xs[e] * y[e];
      PIN16(xy);
      LDSBAR();
      float dm, rstd;
      row_stats((LAS f32x2*)(lds + LDS_RED), r8, dm, rstd);
      const float nb = -dm * rstd;
#pragma unroll
      for (int e = 0; e < 16; e++) xs[e] = y[e] * nb + (xy[e] * rstd + xs[e]);
      ln_tail<false>(lds, ws, xs, sh, fo, w, lane);
      WAITVM(0);
      put_flag(ws, F_LN + 131072 + (blk * 4 + w) * 128, tag, lane);
    }
  }
  asm volatile(".Lh3_code_end:");
}

extern "C" void kernel_launch(void* const* d_in, const int* in_sizes, int n_in, void* d_out, int out_size, void* d_ws, size_t ws_size, hipStream_t st) {
  (void)hipFuncSetAttribute((const void*)k_h3_geglu, hipFuncAttributeMaxDynamicSharedMemorySize, MAIN_LDS);
  k_h3_geglu<<<256, 256, MAIN_LDS, st>>>((const u16*)d_in[0], (const u16*)d_in[1], (const u16*)d_in[2], (const u16*)d_in[3], (const u16*)d_in[4],
                                         (const u16*)d_in[5], (const u16*)d_in[7], (const u16*)d_in[8], (const u16*)d_in[9], (char*)d_ws, (u16*)d_out);
}
```
